```python
import torch

def _softmax(x, axis=-1):
    return torch.softmax(x, dim=axis)
def _gelu(x):
    return torch.nn.functional.gelu(x, approximate="tanh")
def _silu(x):
    return torch.nn.functional.silu(x)
def _sigmoid(x):
    return torch.sigmoid(x)
def _layernorm(x, axis=-1, eps=1e-5):
    m = x.mean(dim=axis, keepdim=True)
    v = x.var(dim=axis, keepdim=True, unbiased=False)
    return (x - m) / torch.sqrt(v + eps)
def _scatter_add(tgt, idx, src, axis=0):
    return tgt.clone().index_add_(axis, idx, src)
def _segment_sum(src, seg, n):
    out = torch.zeros((n,) + src.shape[1:], dtype=src.dtype)
    return out.index_add_(0, seg, src)
def _pad(x, pw):
    flat = tuple(v for pair in reversed(pw) for v in pair)
    return torch.nn.functional.pad(x, flat)

kernel_name = 'sequential_preLN__linear_kernel_hedgehog__mixer_s_mlp__prompt'

def setup_inputs():

    _w = (lambda a: (lambda t: t.to(torch.bfloat16) if t.is_floating_point() else t)(torch.as_tensor(a)))
    def _A_setup():
        B, S, D, H = 1, 2048, 448, 16
        Dh = D // H
        return {
            "x": _w((lambda *s: torch.randn(*s, dtype=torch.bfloat16) * 0.05)(B, S, D)),
            "wqkv": _w((lambda *s: torch.randn(*s, dtype=torch.bfloat16) * 0.2)(D, 3 * D)),
            "w_phi": _w((lambda *s: torch.randn(*s, dtype=torch.bfloat16) * 0.2)(H, Dh, Dh) * (Dh ** -0.5)),
            "wo": _w((lambda *s: torch.randn(*s, dtype=torch.bfloat16) * 0.05)(D, D)),
        }
    _A_in = _A_setup()

    def _B_setup():
        B, S, D, F = 1, 2048, 448, 1792
        return {
            "x":  _w((lambda *s: torch.randn(*s, dtype=torch.bfloat16) * 0.05)(B, S, D)),
            "w1": _w((lambda *s: torch.randn(*s, dtype=torch.bfloat16) * 3.2)(D, D) * (D ** -0.5)),
            "w2": _w((lambda *s: torch.randn(*s, dtype=torch.bfloat16) * 3.2)(D, D) * (D ** -0.5)),
            "w3": _w((lambda *s: torch.randn(*s, dtype=torch.bfloat16) * 3.2)(D, F) * (D ** -0.5)),
            "w4": _w((lambda *s: torch.randn(*s, dtype=torch.bfloat16) * 3.2)(F, D) * (F ** -0.5)),
        }
    _B_in = _B_setup()

    return {
        "A_x": _A_in["x"],
        "A_wqkv": _A_in["wqkv"],
        "A_w_phi": _A_in["w_phi"],
        "A_wo": _A_in["wo"],
        "B_x": _B_in["x"],
        "B_w1": _B_in["w1"],
        "B_w2": _B_in["w2"],
        "B_w3": _B_in["w3"],
        "B_w4": _B_in["w4"],
    }

def reference(A_x, A_wqkv, A_w_phi, A_wo, B_x, B_w1, B_w2, B_w3, B_w4):
    def _A_ref(x, wqkv, w_phi, wo):
        B, S, D = x.shape
        H = 16; Dh = D // H
        qkv = (lambda x, *d: x.permute(*d))((x @ wqkv).reshape(B, S, 3, H, Dh), 2, 0, 3, 1, 4)
        q, k, v = qkv[0], qkv[1], qkv[2]
        pq = torch.einsum("bhsd,hdf->bhsf", q, w_phi)
        pk = torch.einsum("bhsd,hdf->bhsf", k, w_phi)
        phi_q = torch.concatenate([_softmax(pq, axis=-1), _softmax(-pq, axis=-1)], axis=-1)
        phi_k = torch.concatenate([_softmax(pk, axis=-1), _softmax(-pk, axis=-1)], axis=-1)
        kv = torch.einsum("bhsf,bhsd->bhfd", phi_k, v)
        z  = torch.einsum("bhsf->bhf", phi_k)
        num = torch.einsum("bhqf,bhfd->bhqd", phi_q, kv)
        den = torch.einsum("bhqf,bhf->bhq", phi_q, z)[..., None] + 1e-6
        o = num / den
        return (lambda x, *d: x.permute(*d))(o, 0, 2, 1, 3).reshape(B, S, D) @ wo

    def _B_ref(x, w1, w2, w3, w4):
        B, S, D = x.shape
        h = _gelu(_layernorm(x, axis=-1) @ w1)
        c = D // 4
        g0 = _pad(h[:, :, 0*c:1*c], ((0, 0), (1, 0), (0, 0)))[:, :S, :]
        g1 = _pad(h[:, :, 1*c:2*c], ((0, 0), (0, 1), (0, 0)))[:, 1:, :]
        g2 = _pad(h[:, :, 2*c:3*c], ((0, 0), (2, 0), (0, 0)))[:, :S, :]
        g3 = _pad(h[:, :, 3*c:   ], ((0, 0), (0, 2), (0, 0)))[:, 2:, :]
        hs = torch.concatenate([g0, g1, g2, g3], axis=-1) @ w2
        x1 = x + hs
        h2 = _gelu(_layernorm(x1, axis=-1) @ w3) @ w4
        return x1 + h2

    x = A_x
    h = x + _A_ref(_layernorm(x, axis=-1), A_wqkv, A_w_phi, A_wo)
    h2 = h + _B_ref(_layernorm(h, axis=-1), B_w1, B_w2, B_w3, B_w4)
    return h2
```

```cpp
#include <hip/hip_runtime.h>
#include <hip/hip_bf16.h>
typedef __hip_bfloat16 bf16;
constexpr int S=2048, D=448, H=16, DH=28, F=1792;
__device__ inline float b2f(bf16 x){return __bfloat162float(x);}
__device__ inline float rb(float x){return __bfloat162float(__float2bfloat16(x));}
__device__ inline float gelu(float x){ float u=0.7978845608028654f*(x+0.044715f*x*x*x); return 0.5f*x*(1.f+tanhf(u)); }
__global__ void k_tofloat(const bf16* x, float* y, int n){int i=blockIdx.x*256+threadIdx.x; if(i<n) y[i]=b2f(x[i]);}
__global__ void k_ln(const float* x, float* y){
  int r=blockIdx.x; int l=threadIdx.x; float v[7]; float s=0; for(int i=0;i<7;i++){v[i]=x[r*D+l+64*i]; s+=v[i];}
  for(int m=32;m;m>>=1) s+=__shfl_xor(s,m); float mu=s/D; float q=0; for(int i=0;i<7;i++){float d=v[i]-mu; q+=d*d;}
  for(int m=32;m;m>>=1) q+=__shfl_xor(q,m); float rs=rsqrtf(q/D+1e-5f); for(int i=0;i<7;i++) y[r*D+l+64*i]=rb((v[i]-mu)*rs);
}
__global__ void k_gemm(const float* A, const bf16* W, float* C, int K, int N, int mode, const float* add1, int shift){
  int n=blockIdx.x*64+threadIdx.x; int m=blockIdx.y; if(n>=N) return; float acc=0;
  for(int k=0;k<K;k++){ int mm=m; if(shift){int g=k/112; mm = g==0? m-1 : g==1? m+1 : g==2? m-2 : m+2;} float a = (mm>=0&&mm<S)? A[mm*K+k]:0.f; acc+=a*b2f(W[k*N+n]); }
  acc=rb(acc); if(mode==1) acc=rb(gelu(acc)); if(add1) acc=rb(acc+add1[m*N+n]); C[m*N+n]=acc;
}
__global__ void k_phi(const float* qkv, const bf16* wphi, float* phi){
  int s=blockIdx.x,h=blockIdx.y,t=blockIdx.z,f=threadIdx.x; float p=0; if(f<DH){ for(int d=0;d<DH;d++) p+=qkv[s*3*D+t*D+h*DH+d]*b2f(wphi[h*DH*DH+d*DH+f]); p=rb(p);}
  float mx=f<DH?p:-1e30f, mn=f<DH?-p:-1e30f; for(int m=32;m;m>>=1){mx=fmaxf(mx,__shfl_xor(mx,m)); mn=fmaxf(mn,__shfl_xor(mn,m));}
  float e1=f<DH?expf(p-mx):0.f, e2=f<DH?expf(-p-mn):0.f; float s1=e1,s2=e2; for(int m=32;m;m>>=1){s1+=__shfl_xor(s1,m); s2+=__shfl_xor(s2,m);}
  if(f<DH){ float* o=phi+((size_t)(t*H+h)*S+s)*56; o[f]=rb(e1/s1); o[DH+f]=rb(e2/s2);} }
__global__ void k_kv(const float* phi, const float* qkv, float* kv){
  int h=blockIdx.x,f=blockIdx.y,d=threadIdx.x; if(d>DH) return; float a=0; const float* pk=phi+((size_t)(H+h)*S)*56;
  for(int s=0;s<S;s++){ float v=d<DH? qkv[s*3*D+2*D+h*DH+d]:1.f; a+=pk[s*56+f]*v;} kv[(h*56+f)*32+d]=rb(a);}
__global__ void k_o(const float* phi, const float* kv, float* o){ int s=blockIdx.x,h=blockIdx.y,d=threadIdx.x; if(d>=DH) return; const float* pq=phi+((size_t)h*S+s)*56; float n=0,de=0;
  for(int f=0;f<56;f++){n+=pq[f]*kv[(h*56+f)*32+d]; de+=pq[f]*kv[(h*56+f)*32+28];} de=rb(rb(de)+1e-6f); o[s*D+h*DH+d]=rb(rb(n)/de);}
__global__ void k_fin(const float* h, const float* y, bf16* out, int n){int i=blockIdx.x*256+threadIdx.x; if(i<n) out[i]=__float2bfloat16(h[i]+y[i]);}
extern "C" void kernel_launch(void* const* d_in, const int* in_sizes, int n_in, void* d_out, int out_size, void* d_ws, size_t ws_size, hipStream_t st){
  const bf16* x=(const bf16*)d_in[0]; const bf16* wqkv=(const bf16*)d_in[1]; const bf16* wphi=(const bf16*)d_in[2]; const bf16* wo=(const bf16*)d_in[3];
  const bf16* w1=(const bf16*)d_in[5]; const bf16* w2=(const bf16*)d_in[6]; const bf16* w3=(const bf16*)d_in[7]; const bf16* w4=(const bf16*)d_in[8];
  float* ws=(float*)d_ws; size_t SD=(size_t)S*D;
  float* xf=ws; float* ln=xf+SD; float* qkv=ln+SD; float* phi=qkv+3*SD; float* kv=phi+2*2*SD; float* o=kv+SD; float* h=o+SD; float* xb=h+SD; float* hh=xb+SD; float* x1=hh+SD; float* hid=x1+SD; float* y=hid+4*SD;
  k_tofloat<<<SD/256,256,0,st>>>(x,xf,SD);
  k_ln<<<S,64,0,st>>>(xf,ln);
  k_gemm<<<dim3(21,S),64,0,st>>>(ln,wqkv,qkv,D,3*D,0,nullptr,0);
  k_phi<<<dim3(S,H,2),64,0,st>>>(qkv,wphi,phi);
  k_kv<<<dim3(H,56),64,0,st>>>(phi,qkv,kv);
  k_o<<<dim3(S,H),64,0,st>>>(phi,kv,o);
  k_gemm<<<dim3(7,S),64,0,st>>>(o,wo,h,D,D,0,xf,0);
  k_ln<<<S,64,0,st>>>(h,xb);
  k_ln<<<S,64,0,st>>>(xb,ln);
  k_gemm<<<dim3(7,S),64,0,st>>>(ln,w1,hh,D,D,1,nullptr,0);
  k_gemm<<<dim3(7,S),64,0,st>>>(hh,w2,x1,D,D,0,xb,1);
  k_ln<<<S,64,0,st>>>(x1,ln);
  k_gemm<<<dim3(28,S),64,0,st>>>(ln,w3,hid,D,F,1,nullptr,0);
  k_gemm<<<dim3(7,S),64,0,st>>>(hid,w4,y,F,D,0,x1,0);
  k_fin<<<SD/256,256,0,st>>>(h,y,(bf16*)d_out,SD);
}
```

```cpp
#include <hip/hip_runtime.h>
#include <hip/hip_bf16.h>
typedef __hip_bfloat16 bf16;
constexpr int S=2048, D=448, H=16, DH=28, F=1792;
__device__ inline float b2f(bf16 x){return __bfloat162float(x);}
__device__ inline float rb(float x){return __bfloat162float(__float2bfloat16(x));}
__device__ inline float gelu(float x){ float u=0.7978845608028654f*(x+0.044715f*x*x*x); return 0.5f*x*(1.f+tanhf(u)); }
__global__ void k_tofloat(const bf16* x, float* y, int n){int i=blockIdx.x*256+threadIdx.x; if(i<n) y[i]=b2f(x[i]);}
__global__ void k_ln(const float* x, float* y){
  int r=blockIdx.x; int l=threadIdx.x; float v[7]; float s=0; for(int i=0;i<7;i++){v[i]=x[r*D+l+64*i]; s+=v[i];}
  for(int m=32;m;m>>=1) s+=__shfl_xor(s,m); float mu=s/D; float q=0; for(int i=0;i<7;i++){float d=v[i]-mu; q+=d*d;}
  for(int m=32;m;m>>=1) q+=__shfl_xor(q,m); float rs=rsqrtf(q/D+1e-5f); for(int i=0;i<7;i++) y[r*D+l+64*i]=rb((v[i]-mu)*rs);
}
__global__ void k_gemm(const float* A, const bf16* W, float* C, int K, int N, int mode, const float* add1, int shift){
  int n=blockIdx.x*64+threadIdx.x; int m=blockIdx.y; if(n>=N) return; float acc=0;
  for(int k=0;k<K;k++){ int mm=m; if(shift){int g=k/112; mm = g==0? m-1 : g==1? m+1 : g==2? m-2 : m+2;} float a = (mm>=0&&mm<S)? A[mm*K+k]:0.f; acc+=a*b2f(W[k*N+n]); }
  acc=rb(acc); if(mode==1) acc=rb(gelu(acc)); if(add1) acc=rb(acc+add1[m*N+n]); C[m*N+n]=acc;
}
__global__ void k_phi(const float* qkv, const bf16* wphi, float* phi){
  int s=blockIdx.x,h=blockIdx.y,t=blockIdx.z,f=threadIdx.x; float p=0; if(f<DH){ for(int d=0;d<DH;d++) p+=qkv[s*3*D+t*D+h*DH+d]*b2f(wphi[h*DH*DH+d*DH+f]); p=rb(p);}
  float mx=f<DH?p:-1e30f, mn=f<DH?-p:-1e30f; for(int m=32;m;m>>=1){mx=fmaxf(mx,__shfl_xor(mx,m)); mn=fmaxf(mn,__shfl_xor(mn,m));}
  float e1=f<DH?expf(p-mx):0.f, e2=f<DH?expf(-p-mn):0.f; float s1=e1,s2=e2; for(int m=32;m;m>>=1){s1+=__shfl_xor(s1,m); s2+=__shfl_xor(s2,m);}
  if(f<DH){ float* o=phi+((size_t)(t*H+h)*S+s)*56; o[f]=rb(e1/s1); o[DH+f]=rb(e2/s2);} }
__global__ void k_kv(const float* phi, const float* qkv, float* kv){
  int h=blockIdx.x,f=blockIdx.y,d=threadIdx.x; if(d>DH) return; float a=0; const float* pk=phi+((size_t)(H+h)*S)*56;
  for(int s=0;s<S;s++){ float v=d<DH? qkv[s*3*D+2*D+h*DH+d]:1.f; a+=pk[s*56+f]*v;} kv[(h*56+f)*32+d]=rb(a);}
__global__ void k_o(const float* phi, const float* kv, float* o){ int s=blockIdx.x,h=blockIdx.y,d=threadIdx.x; if(d>=DH) return; const float* pq=phi+((size_t)h*S+s)*56; float n=0,de=0;
  for(int f=0;f<56;f++){n+=pq[f]*kv[(h*56+f)*32+d]; de+=pq[f]*kv[(h*56+f)*32+28];} de=rb(rb(de)+1e-6f); o[s*D+h*DH+d]=rb(rb(n)/de);}
__global__ void k_fin(const float* h, const float* y, bf16* out, int n){int i=blockIdx.x*256+threadIdx.x; if(i<n) out[i]=__float2bfloat16(h[i]+y[i]);}

typedef unsigned v4u __attribute__((ext_vector_type(4)));
__device__ __forceinline__ unsigned long long clk(){ unsigned long long t; asm volatile("s_memtime %0\n\ts_waitcnt lgkmcnt(0)" : "=s"(t) :: "memory"); return t; }
__global__ void k_probe(const unsigned char* in, unsigned* sink){
  int t = threadIdx.x;
  v4u d = *(const v4u*)(in + t*16);
  asm volatile("s_waitcnt vmcnt(0)" : "+v"(d) :: "memory");
  unsigned long long tot = 0; unsigned acc = d.x;
  for (int i = 1; i <= 8; ++i) {
    unsigned long long c0 = clk();
    v4u e = *(const v4u*)(in + i*40960 + t*16);
    asm volatile("s_waitcnt vmcnt(0)" : "+v"(e) :: "memory");
    unsigned long long c1 = clk();
    tot += c1 - c0; acc += e.x;
  }
  unsigned long long s0 = clk();
  while (clk() - s0 < tot * 300) { }
  if (acc == 0x1234567) sink[0] = acc;
}
extern "C" void kernel_launch(void* const* d_in, const int* in_sizes, int n_in, void* d_out, int out_size, void* d_ws, size_t ws_size, hipStream_t st){
  const bf16* x=(const bf16*)d_in[0]; const bf16* wqkv=(const bf16*)d_in[1]; const bf16* wphi=(const bf16*)d_in[2]; const bf16* wo=(const bf16*)d_in[3];
  const bf16* w1=(const bf16*)d_in[5]; const bf16* w2=(const bf16*)d_in[6]; const bf16* w3=(const bf16*)d_in[7]; const bf16* w4=(const bf16*)d_in[8];
  float* ws=(float*)d_ws; size_t SD=(size_t)S*D;
  float* xf=ws; float* ln=xf+SD; float* qkv=ln+SD; float* phi=qkv+3*SD; float* kv=phi+2*2*SD; float* o=kv+SD; float* h=o+SD; float* xb=h+SD; float* hh=xb+SD; float* x1=hh+SD; float* hid=x1+SD; float* y=hid+4*SD;
  k_probe<<<1,64,0,st>>>((const unsigned char*)d_in[8], (unsigned*)d_ws + (60<<20));
  k_tofloat<<<SD/256,256,0,st>>>(x,xf,SD);
  k_ln<<<S,64,0,st>>>(xf,ln);
  k_gemm<<<dim3(21,S),64,0,st>>>(ln,wqkv,qkv,D,3*D,0,nullptr,0);
  k_phi<<<dim3(S,H,2),64,0,st>>>(qkv,wphi,phi);
  k_kv<<<dim3(H,56),64,0,st>>>(phi,qkv,kv);
  k_o<<<dim3(S,H),64,0,st>>>(phi,kv,o);
  k_gemm<<<dim3(7,S),64,0,st>>>(o,wo,h,D,D,0,xf,0);
  k_ln<<<S,64,0,st>>>(h,xb);
  k_ln<<<S,64,0,st>>>(xb,ln);
  k_gemm<<<dim3(7,S),64,0,st>>>(ln,w1,hh,D,D,1,nullptr,0);
  k_gemm<<<dim3(7,S),64,0,st>>>(hh,w2,x1,D,D,0,xb,1);
  k_ln<<<S,64,0,st>>>(x1,ln);
  k_gemm<<<dim3(28,S),64,0,st>>>(ln,w3,hid,D,F,1,nullptr,0);
  k_gemm<<<dim3(7,S),64,0,st>>>(hid,w4,y,F,D,0,x1,0);
  k_fin<<<SD/256,256,0,st>>>(h,y,(bf16*)d_out,SD);
}
```

```cpp
#include <hip/hip_runtime.h>
#include <stdint.h>

typedef unsigned int u32;
typedef unsigned long long u64;
typedef unsigned short u16;
typedef u32 v4u __attribute__((ext_vector_type(4)));
typedef u32 v2u __attribute__((ext_vector_type(2)));
typedef float f4 __attribute__((ext_vector_type(4)));
typedef float f2 __attribute__((ext_vector_type(2)));
typedef __bf16 bf8 __attribute__((ext_vector_type(8)));
typedef __bf16 bf2 __attribute__((ext_vector_type(2)));
typedef short s4v __attribute__((ext_vector_type(4)));
typedef __amdgpu_buffer_rsrc_t rsrc_t;
#define GAS __attribute__((address_space(1)))
#define LAS __attribute__((address_space(3)))
#define DI __device__ __forceinline__
#define RLX_AGENT __ATOMIC_RELAXED, __HIP_MEMORY_SCOPE_AGENT

constexpr int S_ = 2048, D_ = 448, F_ = 1792;
constexpr u32 KB = 1024;
constexpr u32 OFF_E = 0, OFF_TMO = 64;
constexpr u32 OFF_FLAG1 = 4096;
constexpr u32 OFF_CNT = OFF_FLAG1 + 512 * 64;
constexpr u32 OFF_HINT = OFF_CNT + 4096;
constexpr u32 OFF_WQKV = 256 * KB;
constexpr u32 OFF_WO = OFF_WQKV + 1344 * KB;
constexpr u32 OFF_W1 = OFF_WO + 448 * KB;
constexpr u32 OFF_W2 = OFF_W1 + 392 * KB;
constexpr u32 OFF_W3 = OFF_W2 + 392 * KB;
constexpr u32 OFF_W4 = OFF_W3 + 1568 * KB;
constexpr u32 OFF_KVP = OFF_W4 + 1568 * KB;
constexpr u32 OFF_O = OFF_KVP + 1024 * KB;
constexpr u32 OFF_H = OFF_O + 2048 * KB;
constexpr u32 OFF_HS = OFF_H + 1792 * KB;
constexpr u32 OFF_X1 = OFF_HS + 1792 * KB;
constexpr u32 OFF_RS = OFF_X1 + 1792 * KB;
constexpr u32 WS_BYTES = OFF_RS + 256 * 56 * KB;
constexpr u32 OFF_DBG = WS_BYTES;
constexpr u32 L_W = 0;
constexpr u32 L_PK = 86016, PK_RS = 136;
constexpr u32 L_V = L_PK + 128 * PK_RS, V_RS = 72;
constexpr u32 L_KVS = L_V + 128 * V_RS;
constexpr u32 L_ST = L_KVS + 8192;
constexpr u32 L_BYTES = L_ST + 1024;
constexpr u32 L_ACT = 0;
constexpr u32 L_HID = 57344;
constexpr u32 SPIN_CAP = 1u << 15;
constexpr u32 OOB = 0x80000000u;
constexpr int SC1 = 16;
constexpr u32 CODE_BYTES = 32768;
#define NSTAMP 18

#define DIAG 0
#define STAMP(k) do { } while (0)

DI f4 mfma(v4u a, v4u b, f4 c) {
  return __builtin_amdgcn_mfma_f32_16x16x32_bf16(__builtin_bit_cast(bf8, a), __builtin_bit_cast(bf8, b), c, 0, 0, 0);
}
DI u32 pk2(float a, float b) { f2 v = {a, b}; return __builtin_bit_cast(u32, __builtin_convertvector(v, bf2)); }
DI float blo(u32 u) { return __builtin_bit_cast(float, u << 16); }
DI float bhi(u32 u) { return __builtin_bit_cast(float, u & 0xffff0000u); }
DI u32 f2u(float x) { return __builtin_bit_cast(u32, x); }
DI float u2f(u32 x) { return __builtin_bit_cast(float, x); }
DI u32 nosent(u32 x) { return x < 0xfffffffeu ? x : 0xfffffffeu; }
DI v4u nosent4(v4u v) { return v4u{nosent(v.x), nosent(v.y), nosent(v.z), nosent(v.w)}; }
DI u32 umax(u32 a, u32 b) { return a > b ? a : b; }
DI u32 max4(u32 m, v4u v) { return umax(umax(m, umax(v.x, v.y)), umax(v.z, v.w)); }
DI float rsum(float x) {
  auto a = __builtin_amdgcn_permlane16_swap(f2u(x), f2u(x), false, false); x = u2f(a[0]) + u2f(a[1]);
  auto c = __builtin_amdgcn_permlane32_swap(f2u(x), f2u(x), false, false); return u2f(c[0]) + u2f(c[1]);
}
DI float rmax(float x) {
  auto a = __builtin_amdgcn_permlane16_swap(f2u(x), f2u(x), false, false); x = fmaxf(u2f(a[0]), u2f(a[1]));
  auto c = __builtin_amdgcn_permlane32_swap(f2u(x), f2u(x), false, false); return fmaxf(u2f(c[0]), u2f(c[1]));
}
#define SB() __builtin_amdgcn_sched_barrier(0)
DI void bar() { SB(); asm volatile("s_waitcnt lgkmcnt(0)\n\ts_barrier" ::: "memory"); SB(); }
#define VMW(n) asm volatile("s_waitcnt vmcnt(" #n ")" ::: "memory")
DI rsrc_t mkrs(const void* p, u32 bytes) { return __builtin_amdgcn_make_buffer_rsrc((void*)p, 0, (int)bytes, 0x00020000); }
DI v4u ldg(rsrc_t rs, u32 voff, u32 soff) { return __builtin_amdgcn_raw_buffer_load_b128(rs, (int)voff, (int)soff, SC1); }
DI void stg(rsrc_t rs, u32 voff, u32 soff, v4u v) { __builtin_amdgcn_raw_buffer_store_b128(v, rs, (int)(voff + soff), 0, SC1); }
DI void dma1(rsrc_t rs, u32 voff, u32 soff, u32 ldsaddr) {
  asm volatile("s_nop 4\n\ts_mov_b32 m0, %0\n\ts_nop 0\n\tbuffer_load_dwordx4 %1, %2, %3 offen sc1 lds"
               :: "s"(ldsaddr), "v"(voff), "s"(rs), "s"(soff) : "memory");
}
DI void dma4(rsrc_t rs, u32 voff, u32 soff, u32 ldsaddr) {
  asm volatile("s_nop 4\n\ts_mov_b32 m0, %0\n\ts_nop 0\n\t"
               "buffer_load_dwordx4 %1, %2, %3 offen sc1 lds\n\t"
               "buffer_load_dwordx4 %1, %2, %3 offen offset:1024 sc1 lds\n\t"
               "buffer_load_dwordx4 %1, %2, %3 offen offset:2048 sc1 lds\n\t"
               "buffer_load_dwordx4 %1, %2, %3 offen offset:3072 sc1 lds"
               :: "s"(ldsaddr), "v"(voff), "s"(rs), "s"(soff) : "memory");
}
DI float gelu(float x) {
  const float c0 = -2.0f * 0.7978845608028654f * 1.4426950408889634f, c1 = c0 * 0.044715f;
  float t = x * x;
  float e = __builtin_amdgcn_exp2f(x * fmaf(t, c1, c0));
  return x * __builtin_amdgcn_rcpf(1.0f + e);
}

struct Gather8 { u32 t[8]; };
DI void pt_issue(Gather8& q, rsrc_t rs, u32 vlo, u32 vhi, u32 stride, u32 hs) {
#pragma unroll
  for (int j = 0; j < 4; ++j) q.t[j] = __builtin_amdgcn_raw_buffer_load_b16(rs, (int)vlo, (int)(j * stride), 0);
#pragma unroll
  for (int j = 4; j < 8; ++j) q.t[j] = __builtin_amdgcn_raw_buffer_load_b16(rs, (int)vhi, (int)((j + hs) * stride), 0);
}
DI v4u pt_pack(const Gather8& q) {
  return v4u{q.t[0] | (q.t[1] << 16), q.t[2] | (q.t[3] << 16), q.t[4] | (q.t[5] << 16), q.t[6] | (q.t[7] << 16)};
}

DI void poll64(unsigned char* ws, u32 off, bool on, u64 want) {
#pragma clang loop unroll(disable)
  for (u32 sp = 0;; ++sp) {
    u64 v = want;
    if (on) v = __hip_atomic_load((GAS u64*)(ws + off), RLX_AGENT);
    if (__all(v == want)) break;
    if (sp > SPIN_CAP) { __hip_atomic_store((GAS u32*)(ws + OFF_TMO), 1u, RLX_AGENT); break; }
    __builtin_amdgcn_s_sleep(1);
  }
  asm volatile("" ::: "memory");
}

DI void hedgehog(f4 p0, f4 p1, bool pad, v4u& pos, v4u& neg) {
  const float L2E = 1.4426950408889634f;
  float mx = fmaxf(fmaxf(p0[0], p0[1]), fmaxf(p0[2], p0[3]));
  float mn = fminf(fminf(p0[0], p0[1]), fminf(p0[2], p0[3]));
  float mx1 = fmaxf(fmaxf(p1[0], p1[1]), fmaxf(p1[2], p1[3]));
  float mn1 = fminf(fminf(p1[0], p1[1]), fminf(p1[2], p1[3]));
  mx = pad ? mx : fmaxf(mx, mx1);
  mn = pad ? mn : fminf(mn, mn1);
  mx = rmax(mx);
  mn = -rmax(-mn);
  float a[8], c[8];
#pragma unroll
  for (int r = 0; r < 4; ++r) {
    a[r] = __builtin_amdgcn_exp2f((p0[r] - mx) * L2E);
    c[r] = __builtin_amdgcn_exp2f((mn - p0[r]) * L2E);
    float e1 = __builtin_amdgcn_exp2f((p1[r] - mx) * L2E), e2 = __builtin_amdgcn_exp2f((mn - p1[r]) * L2E);
    a[4 + r] = pad ? 0.f : e1;
    c[4 + r] = pad ? 0.f : e2;
  }
  float sa = ((a[0] + a[1]) + (a[2] + a[3])) + ((a[4] + a[5]) + (a[6] + a[7]));
  float sc = ((c[0] + c[1]) + (c[2] + c[3])) + ((c[4] + c[5]) + (c[6] + c[7]));
  sa = __builtin_amdgcn_rcpf(rsum(sa));
  sc = __builtin_amdgcn_rcpf(rsum(sc));
  pos = v4u{pk2(a[0] * sa, a[1] * sa), pk2(a[2] * sa, a[3] * sa), pk2(a[4] * sa, a[5] * sa), pk2(a[6] * sa, a[7] * sa)};
  neg = v4u{pk2(c[0] * sc, c[1] * sc), pk2(c[2] * sc, c[3] * sc), pk2(c[4] * sc, c[5] * sc), pk2(c[6] * sc, c[7] * sc)};
}
DI v4u pack8(f4 a, f4 b) { return v4u{pk2(a[0], a[1]), pk2(a[2], a[3]), pk2(b[0], b[1]), pk2(b[2], b[3])}; }
DI v4u to_chunk(v4u p) {
  auto x = __builtin_amdgcn_permlane16_swap(p.x, p.z, false, false);
  auto y = __builtin_amdgcn_permlane16_swap(p.y, p.w, false, false);
  return v4u{x[0], y[0], x[1], y[1]};
}
DI f2 fin_stats(f4 sq, f4 sm, int g, int i16) {
  int r = i16 & 3;
  float d = r == 0 ? sq[0] : r == 1 ? sq[1] : r == 2 ? sq[2] : sq[3];
  d = (g == (i16 >> 2)) ? d : 0.f;
  d = rsum(d);
  float mu = sm[0] * (1.0f / D_);
  float var = fmaxf(d * (1.0f / D_) - mu * mu, 0.f);
  return f2{mu, var};
}
DI v4u lds16(LAS unsigned char* L, u32 off) { return *(LAS v4u*)(L + off); }
DI v2u trread(u32 addr) {
  return __builtin_bit_cast(v2u, __builtin_amdgcn_ds_read_tr16_b64_v4i16((LAS s4v*)(uintptr_t)addr));
}

template <int NK>
DI void gather(rsrc_t rws, unsigned char* ws, LAS unsigned char* L, u32 lbase, u32 src, u32 dst, int lane, v4u (&fr)[NK]) {
  static_assert(NK % 4 == 0, "");
#pragma clang loop unroll(disable)
  for (u32 sp = 0;; ++sp) {
#pragma unroll
    for (int k = 0; k < NK; k += 4) dma4(rws, lane * 16, src + k * KB, lbase + dst + k * KB);
    VMW(0);
    u32 m = 0;
#pragma unroll
    for (int k = 0; k < NK; ++k) { fr[k] = lds16(L, dst + k * KB + lane * 16); m = max4(m, fr[k]); }
    if (__all(m != 0xffffffffu)) break;
    if (sp > SPIN_CAP) { __hip_atomic_store((GAS u32*)(ws + OFF_TMO), 2u, RLX_AGENT); break; }
    asm volatile("s_waitcnt lgkmcnt(0)" ::: "memory");
  }
}
DI void tile_stats(const v4u (&fr)[28], LAS unsigned char* L, int tile0, int g, int i16) {
  const v4u ones = {0x3f803f80u, 0x3f803f80u, 0x3f803f80u, 0x3f803f80u};
#pragma unroll
  for (int T = 0; T < 2; ++T) {
    f4 sq = {0, 0, 0, 0}, sm = {0, 0, 0, 0};
#pragma unroll
    for (int s = 0; s < 14; ++s) { sq = mfma(fr[T * 14 + s], fr[T * 14 + s], sq); sm = mfma(ones, fr[T * 14 + s], sm); }
    f2 st = fin_stats(sq, sm, g, i16);
    if (g == 0) *(LAS f2*)(L + L_ST + ((tile0 + T) * 16 + i16) * 8) = st;
  }
}

__global__ void __launch_bounds__(512, 2)
fused_kernel(const u16* __restrict__ x, const u16* __restrict__ wqkv, const u16* __restrict__ wphi, const u16* __restrict__ wo,
             const u16* __restrict__ w1, const u16* __restrict__ w2, const u16* __restrict__ w3, const u16* __restrict__ w4,
             u16* __restrict__ out, unsigned char* __restrict__ ws) {
  __shared__ __attribute__((aligned(1024))) unsigned char lds_raw[L_BYTES];
  LAS unsigned char* L = (LAS unsigned char*)lds_raw;
  const u32 lbase = (u32)(uintptr_t)L;
  const int tid = threadIdx.x, lane = tid & 63, w = __builtin_amdgcn_readfirstlane(tid >> 6), g = lane >> 4, i16 = lane & 15;
  const int b = blockIdx.x, xcd = b & 7, ii = b >> 3;
  const int tg = 8 * (xcd & 1) + (ii & 7), h = 4 * (xcd >> 1) + (ii >> 3);
  const int role = (w - 2 * (h & 3)) & 7;
  const int zg = xcd * 8 + (ii >> 2), js = ii & 3;
  const int yg = xcd * 4 + (ii >> 3), ms = ii & 7;
  const rsrc_t rws = mkrs(ws, WS_BYTES);
  const rsrc_t rx = mkrs(x, S_ * D_ * 2);
  const v4u SENT = {0xffffffffu, 0xffffffffu, 0xffffffffu, 0xffffffffu};
  const v4u ones = {0x3f803f80u, 0x3f803f80u, 0x3f803f80u, 0x3f803f80u};
  const u32 l16 = lane * 16;
  STAMP(0);

  u32 pfc = 0;
  if (role == 2) {
    u64 pc; asm volatile("s_getpc_b64 %0" : "=s"(pc));
    const u32 line = ii + 32 * lane;
    if (line < CODE_BYTES / 128) pfc = *(const GAS u32*)((pc & ~255ull) + (u64)line * 128);
  }
  const u64 tag = __hip_atomic_load((GAS u64*)(ws + OFF_E), RLX_AGENT) + 1;

  v4u xf[14];
  Gather8 gp0, gp1;
  const rsrc_t rphi = mkrs(wphi, 16 * 28 * 28 * 2);
  auto load_x = [&]() {
    const u32 xo = (u32)(tg * 128 + w * 16 + i16) * 896 + g * 16;
#pragma unroll
    for (int s = 0; s < 14; ++s) xf[s] = __builtin_amdgcn_raw_buffer_load_b128(rx, (int)(xo + s * 64), 0, 0);
    const u32 po = (u32)(h * 784 + g * 4 * 28 + i16) * 2;
    pt_issue(gp0, rphi, po, g == 3 ? OOB : po, 56, 12);
    pt_issue(gp1, rphi, i16 < 12 ? po + 32 : OOB, (g == 3 || i16 >= 12) ? OOB : po + 32, 56, 12);
  };
  Gather8 q2[4];
  u32 q2dst[4];
  if (role < 2) {
    const rsrc_t rq = mkrs(wqkv, D_ * 3 * D_ * 2);
    Gather8 q1[3];
    const bool ok = tg < 14 && (role == 0 || i16 < 12);
#pragma unroll
    for (int m = 0; m < 3; ++m) {
      const u32 mc = m == 0 ? 448 : m == 1 ? 896 : 0;
      const u32 vo = ok ? (u32)((tg * 32 + g * 8) * 1344 + mc + h * 28 + role * 16 + i16) * 2 : OOB;
      pt_issue(q1[m], rq, vo, vo, 2688, 0);
    }
    stg(rws, l16, OFF_KVP + ((h * 16 + tg) * 4 + role * 2) * KB, SENT);
    stg(rws, l16, OFF_KVP + ((h * 16 + tg) * 4 + role * 2 + 1) * KB, SENT);
    if (tg < 14) {
#pragma unroll
      for (int m = 0; m < 3; ++m) stg(rws, l16, OFF_WQKV + (h * 84 + m * 28 + tg * 2 + role) * KB, pt_pack(q1[m]));
    }
    if (tg == 0 && role == 0 && lane == 0) __hip_atomic_store((GAS u64*)(ws + OFF_CNT + h * 128), tag << 8, RLX_AGENT);
    VMW(0);
    if (lane == 0) __hip_atomic_store((GAS u64*)(ws + OFF_FLAG1 + ((h * 16 + tg) * 2 + role) * 64), tag, RLX_AGENT);
    load_x();
  } else {
    load_x();
    if (role > 2) {
#pragma unroll
      for (int sl = 0; sl < 4; ++sl) {
        const int f = sl * 1280 + 5 * b + (role - 3);
        const u16* base; u32 bytes, stride, hs, G, row0, n, dst; bool padk = false;
        if (f < 448) { const int st = f / 28; n = f % 28; base = wo; bytes = D_ * D_ * 2; stride = 896; hs = 12; G = 4; row0 = st * 28; dst = OFF_WO + (n * 16 + st) * KB; padk = true; }
        else if (f < 1232) { const int sec = f >= 840; const int ff = f - (sec ? 840 : 448); const int st = ff / 28; n = ff % 28; base = sec ? w2 : w1; bytes = D_ * D_ * 2;
          stride = 896; hs = 0; G = 8; row0 = st * 32; dst = (sec ? OFF_W2 : OFF_W1) + (n * 14 + st) * KB; }
        else if (f < 2800) { const int ff = f - 1232; const int st = ff / 112; n = ff % 112; base = w3; bytes = D_ * F_ * 2; stride = 3584; hs = 0; G = 8; row0 = st * 32;
          dst = OFF_W3 + (n * 14 + st) * KB; }
        else { const int ff = f - 2800; const int ks = ff / 28; n = ff % 28; base = w4; bytes = D_ * F_ * 2; stride = 896; hs = 12; G = 4; row0 = ks * 32;
          dst = OFF_W4 + (((ks / 7) * 28 + n) * 7 + ks % 7) * KB; }
        const bool ok = f < 4368;
        const u32 vo = ok ? (row0 + g * G) * stride + (n * 16 + i16) * 2 : OOB;
        pt_issue(q2[sl], mkrs(base, bytes), vo, (padk && g == 3) ? OOB : vo, stride, hs);
        q2dst[sl] = ok ? dst : OOB;
      }
    }
  }
  {
    f4 sq = {0, 0, 0, 0}, sm = {0, 0, 0, 0};
#pragma unroll
    for (int s = 0; s < 14; ++s) { sq = mfma(xf[s], xf[s], sq); sm = mfma(ones, xf[s], sm); }
    const f2 st = fin_stats(sq, sm, g, i16);
    const float rs = __builtin_amdgcn_rsqf(st[1] + 1e-5f), nb = -st[0] * rs;
#pragma unroll
    for (int s = 0; s < 14; ++s)
#pragma unroll
      for (int e = 0; e < 4; ++e) xf[s][e] = pk2(fmaf(blo(xf[s][e]), rs, nb), fmaf(bhi(xf[s][e]), rs, nb));
  }
  const v4u phA = pt_pack(gp0), phB = pt_pack(gp1);
  STAMP(1);
  if (role > 2) {
#pragma unroll
    for (int sl = 0; sl < 4; ++sl) stg(rws, l16 + q2dst[sl], 0, pt_pack(q2[sl]));
  }
  stg(rws, l16, OFF_O + ((tg * 8 + w) * 16 + h) * KB, SENT);
  if (w < 7) {
    stg(rws, l16, OFF_H + (b * 7 + w) * KB, SENT);
    stg(rws, l16, OFF_HS + (b * 7 + w) * KB, SENT);
    stg(rws, l16, OFF_X1 + (b * 7 + w) * KB, SENT);
  }
  if (role == 2) {
    asm volatile("" :: "v"(pfc));
    poll64(ws, OFF_FLAG1 + (h * 32 + lane) * 64, lane < 32, tag);
  }
  bar();
  STAMP(2);
#pragma unroll
  for (int k = 0; k < 11; ++k) {
    const int f = (k * 8 + w < 84) ? k * 8 + w : 83;
    dma1(rws, l16, OFF_WQKV + (h * 84 + f) * KB, lbase + L_W + f * KB);
  }
  VMW(7);
  bar();
  STAMP(3);
  const bool padg = g == 3;
  {
    f4 k0 = {0, 0, 0, 0}, k1 = {0, 0, 0, 0};
#pragma unroll
    for (int s = 0; s < 14; ++s) {
      k0 = mfma(lds16(L, L_W + (2 * s) * KB + l16), xf[s], k0);
      k1 = mfma(lds16(L, L_W + (2 * s + 1) * KB + l16), xf[s], k1);
    }
    VMW(4);
    bar();
    const v4u kb = pack8(k0, k1);
    const f4 z4 = {0, 0, 0, 0};
    v4u pos, neg;
    hedgehog(mfma(phA, kb, z4), mfma(phB, kb, z4), padg, pos, neg);
    f4 v0 = {0, 0, 0, 0}, v1 = {0, 0, 0, 0};
#pragma unroll
    for (int s = 0; s < 14; ++s) {
      v0 = mfma(lds16(L, L_W + (28 + 2 * s) * KB + l16), xf[s], v0);
      v1 = mfma(lds16(L, L_W + (28 + 2 * s + 1) * KB + l16), xf[s], v1);
    }
    if (padg) v1[0] = 1.0f;
    const v4u vb = pack8(v0, v1);
    LAS unsigned char* pr = L + L_PK + (w * 16 + i16) * PK_RS + g * 8;
    *(LAS v2u*)(pr) = v2u{pos.x, pos.y};
    *(LAS v2u*)(pr + 32) = v2u{pos.z, pos.w};
    *(LAS v2u*)(pr + 64) = v2u{neg.x, neg.y};
    *(LAS v2u*)(pr + 96) = v2u{neg.z, neg.w};
    LAS unsigned char* vr = L + L_V + (w * 16 + i16) * V_RS + g * 8;
    *(LAS v2u*)(vr) = v2u{vb.x, vb.y};
    *(LAS v2u*)(vr + 32) = v2u{vb.z, vb.w};
  }
  VMW(0);
  bar();
  STAMP(4);
  if (w < 4) {
    const int sg = w >> 1, e = w & 1, q = i16 >> 2, p = i16 & 3;
    const u32 pa = lbase + L_PK + (g * 8 + q) * PK_RS + sg * 64 + p * 8;
    const u32 va = lbase + L_V + (g * 8 + q) * V_RS + e * 32 + p * 8;
    f4 c0 = {0, 0, 0, 0}, c1 = {0, 0, 0, 0};
#pragma unroll
    for (int s = 0; s < 4; ++s) {
      const v2u a0 = trread(pa + s * 32 * PK_RS), a1 = trread(pa + (s * 32 + 4) * PK_RS);
      const v2u b0 = trread(pa + s * 32 * PK_RS + 32), b1 = trread(pa + (s * 32 + 4) * PK_RS + 32);
      const v2u d0 = trread(va + s * 32 * V_RS), d1 = trread(va + (s * 32 + 4) * V_RS);
      const v4u vf = {d0.x, d0.y, d1.x, d1.y};
      c0 = mfma(v4u{a0.x, a0.y, a1.x, a1.y}, vf, c0);
      c1 = mfma(v4u{b0.x, b0.y, b1.x, b1.y}, vf, c1);
    }
    stg(rws, l16, OFF_KVP + ((h * 16 + tg) * 4 + w) * KB, nosent4(pack8(c0, c1)));
    if (w == 0 && lane == 0) __hip_atomic_fetch_add((GAS u64*)(ws + OFF_CNT + h * 128), 1ull, RLX_AGENT);
  }
  v4u qpos, qneg;
  {
    f4 q0 = {0, 0, 0, 0}, q1 = {0, 0, 0, 0};
#pragma unroll
    for (int s = 0; s < 14; ++s) {
      q0 = mfma(lds16(L, L_W + (56 + 2 * s) * KB + l16), xf[s], q0);
      q1 = mfma(lds16(L, L_W + (56 + 2 * s + 1) * KB + l16), xf[s], q1);
    }
    const v4u qb = pack8(q0, q1);
    const f4 z4 = {0, 0, 0, 0};
    hedgehog(mfma(phA, qb, z4), mfma(phB, qb, z4), padg, qpos, qneg);
  }
  STAMP(5);
  if (w == 7) poll64(ws, OFF_CNT + lane * 128, lane < 16, (tag << 8) + 16);
  bar();
  STAMP(6);
  const int n = js * 7 + (w < 7 ? w : 0);
  v4u wA[16];
  v2u xr[2];
  {
    v4u kp[8];
    const u32 src = OFF_KVP + ((h * 16 + (w >> 2) * 8) * 4 + (w & 3)) * KB;
#pragma clang loop unroll(disable)
    for (u32 sp = 0;; ++sp) {
      u32 m = 0;
#pragma unroll
      for (int k = 0; k < 8; ++k) kp[k] = ldg(rws, l16, src + k * 4 * KB);
#pragma unroll
      for (int k = 0; k < 8; ++k) m = max4(m, kp[k]);
      if (__all(m != 0xffffffffu)) break;
      if (sp > SPIN_CAP) { __hip_atomic_store((GAS u32*)(ws + OFF_TMO), 3u, RLX_AGENT); break; }
    }
    asm volatile("" ::: "memory");
    if (w < 7) {
#pragma unroll
      for (int s = 0; s < 16; ++s) wA[s] = ldg(rws, l16, OFF_WO + (n * 16 + s) * KB);
#pragma unroll
      for (int T = 0; T < 2; ++T)
        xr[T] = __builtin_amdgcn_raw_buffer_load_b64(rx, (int)((zg * 32 + T * 16 + i16) * 896 + (n * 16 + g * 4) * 2), 0, 0);
    }
    asm volatile("" ::: "memory");
    v4u sum;
#pragma unroll
    for (int e = 0; e < 4; ++e) {
      float lo = 0.f, hi = 0.f;
#pragma unroll
      for (int k = 0; k < 8; ++k) { lo += blo(kp[k][e]); hi += bhi(kp[k][e]); }
      sum[e] = pk2(lo, hi);
    }
    *(LAS v4u*)(L + L_KVS + w * KB + l16) = sum;
  }
  bar();
  STAMP(7);
  {
    f4 n0 = {0, 0, 0, 0}, n1 = {0, 0, 0, 0};
#pragma unroll
    for (int hf = 0; hf < 2; ++hf) {
      n0 = mfma(lds16(L, L_KVS + (hf * 4 + 0) * KB + l16), qpos, n0);
      n1 = mfma(lds16(L, L_KVS + (hf * 4 + 1) * KB + l16), qpos, n1);
      n0 = mfma(lds16(L, L_KVS + (hf * 4 + 2) * KB + l16), qneg, n0);
      n1 = mfma(lds16(L, L_KVS + (hf * 4 + 3) * KB + l16), qneg, n1);
    }
    const float den = rsum(padg ? n1[0] : 0.f) + 1e-6f;
    const float rd = __builtin_amdgcn_rcpf(den);
    n0 *= rd; n1 *= rd;
    if (padg) n1 = f4{0, 0, 0, 0};
    stg(rws, l16, OFF_O + ((tg * 8 + w) * 16 + h) * KB, nosent4(pack8(n0, n1)));
  }
  bar();
  STAMP(8);

  const int th = (ii >> 2) & 1;
  if (w == 7) {
    auto post = [&](int st, int id) { if (lane == 0) __hip_atomic_store((GAS u64*)(ws + OFF_HINT + (st * 256 + id) * 64), tag, RLX_AGENT); };
    post(0, tg * 16 + h);
    poll64(ws, OFF_HINT + (0 * 256 + (zg >> 2) * 16 + lane) * 64, lane < 16, tag);
    STAMP(9);
    { v4u fr[32]; gather<32>(rws, ws, L, lbase, OFF_O + zg * 32 * KB, L_ACT, lane, fr); }
    bar();
    STAMP(10);
    bar();
    post(1, zg * 4 + js);
    poll64(ws, OFF_HINT + (1 * 256 + zg * 4 + lane) * 64, lane < 4, tag);
    STAMP(11);
    {
      v4u fr[28];
      gather<28>(rws, ws, L, lbase, OFF_H + zg * 28 * KB, L_ACT, lane, fr);
      bar();
      STAMP(12);
      tile_stats(fr, L, 0, g, i16);
    }
    bar();
    bar();
    post(2, zg * 4 + js);
    {
      const int nz = lane < 4 ? zg : ((lane & 1) ? zg + 1 : zg + 63) & 63;
      poll64(ws, OFF_HINT + (2 * 256 + nz * 4 + (lane & 3)) * 64, lane < 8, tag);
    }
    STAMP(13);
    { v4u fr[28]; gather<28>(rws, ws, L, lbase, OFF_HS + zg * 28 * KB, L_ACT, lane, fr); }
    bar();
    STAMP(14);
    bar();
    post(3, zg * 4 + js);
    poll64(ws, OFF_HINT + (3 * 256 + yg * 8 + lane) * 64, lane < 8, tag);
    STAMP(15);
    {
      v4u fr[28];
      gather<28>(rws, ws, L, lbase, OFF_X1 + (yg * 2 + (th ^ 1)) * 28 * KB, L_ACT, lane, fr);
      bar();
      STAMP(16);
      v4u fb[28];
      gather<28>(rws, ws, L, lbase, OFF_X1 + (yg * 2 + th) * 28 * KB, L_ACT + 28 * KB, lane, fb);
      tile_stats(fr, L, 0, g, i16);
      bar();
      tile_stats(fb, L, 2, g, i16);
    }
    bar();
    bar();
    bar();
    post(4, yg * 8 + ms);
    poll64(ws, OFF_HINT + (4 * 256 + yg * 8 + lane) * 64, lane < 8, tag);
    STAMP(17);
    bar();
    if (b == 0 && lane == 0) __hip_atomic_store((GAS u64*)(ws + OFF_E), tag, RLX_AGENT);
    return;
  }
  const u32 chunk_off = ((n >> 1) * KB) + (2 * (n & 1) + (g >> 1)) * 256;
  f4 hold0, hold1;
  v4u wB[14];
  bar();
#pragma unroll
  for (int s = 0; s < 14; ++s) wB[s] = ldg(rws, l16, OFF_W1 + (n * 14 + s) * KB);
  asm volatile("" ::: "memory");
  {
    f4 a0 = {blo(xr[0].x), bhi(xr[0].x), blo(xr[0].y), bhi(xr[0].y)};
    f4 a1 = {blo(xr[1].x), bhi(xr[1].x), blo(xr[1].y), bhi(xr[1].y)};
#pragma unroll
    for (int s = 0; s < 16; ++s) {
      a0 = mfma(wA[s], lds16(L, L_ACT + s * KB + l16), a0);
      a1 = mfma(wA[s], lds16(L, L_ACT + (16 + s) * KB + l16), a1);
    }
    const v4u p = nosent4(pack8(a0, a1));
    hold0 = f4{blo(p.x), bhi(p.x), blo(p.y), bhi(p.y)};
    hold1 = f4{blo(p.z), bhi(p.z), blo(p.w), bhi(p.w)};
    stg(rws, (zg * 2 + (g & 1)) * 14 * KB + chunk_off + i16 * 16, OFF_H, to_chunk(p));
  }
  bar();
  STAMP(9);
  f4 xb0, xb1;
  {
    f4 cs = {0, 0, 0, 0};
#pragma unroll
    for (int s = 0; s < 14; ++s) cs = mfma(wB[s], ones, cs);
    bar();
#pragma unroll
    for (int s = 0; s < 14; ++s) wA[s] = ldg(rws, l16, OFF_W2 + (n * 14 + s) * KB);
    asm volatile("" ::: "memory");
    f4 a0 = {0, 0, 0, 0}, a1 = {0, 0, 0, 0};
#pragma unroll
    for (int s = 0; s < 14; ++s) {
      a0 = mfma(wB[s], lds16(L, L_ACT + s * KB + l16), a0);
      a1 = mfma(wB[s], lds16(L, L_ACT + (14 + s) * KB + l16), a1);
    }
    bar();
    const f2 s0 = *(LAS f2*)(L + L_ST + i16 * 8), s1 = *(LAS f2*)(L + L_ST + (16 + i16) * 8);
    const float r0 = __builtin_amdgcn_rsqf(s0[1] + 1e-5f), r1 = __builtin_amdgcn_rsqf(s1[1] + 1e-5f);
    const float t0 = r0 * __builtin_amdgcn_rsqf(s0[1] * r0 * r0 + 1e-5f), t1 = r1 * __builtin_amdgcn_rsqf(s1[1] * r1 * r1 + 1e-5f);
#pragma unroll
    for (int r = 0; r < 4; ++r) {
      a0[r] = gelu((a0[r] - s0[0] * cs[r]) * t0);
      a1[r] = gelu((a1[r] - s1[0] * cs[r]) * t1);
      xb0[r] = (hold0[r] - s0[0]) * r0;
      xb1[r] = (hold1[r] - s1[0]) * r1;
    }
    v4u ch = to_chunk(nosent4(pack8(a0, a1)));
    const int sh = js == 0 ? 1 : js == 1 ? -1 : js == 2 ? 2 : -2;
    const int tk = zg * 32 + (g & 1) * 16 + i16 + sh;
    if (tk < 0 || tk >= S_) ch = v4u{0, 0, 0, 0};
    const int tw = tk & (S_ - 1);
    stg(rws, (tw >> 4) * 14 * KB + chunk_off + (tw & 15) * 16, OFF_HS, ch);
  }
  bar();
  STAMP(10);
  v4u w3f[28];
  bar();
#pragma unroll
  for (int s = 0; s < 28; ++s) w3f[s] = ldg(rws, l16, OFF_W3 + ((ms * 14 + w * 2) * 14 + s) * KB);
  asm volatile("" ::: "memory");
  {
#pragma unroll
    for (int s = 0; s < 14; ++s) {
      xb0 = mfma(wA[s], lds16(L, L_ACT + s * KB + l16), xb0);
      xb1 = mfma(wA[s], lds16(L, L_ACT + (14 + s) * KB + l16), xb1);
    }
    const v4u p = nosent4(pack8(xb0, xb1));
    hold0 += f4{blo(p.x), bhi(p.x), blo(p.y), bhi(p.y)};
    hold1 += f4{blo(p.z), bhi(p.z), blo(p.w), bhi(p.w)};
    stg(rws, (zg * 2 + (g & 1)) * 14 * KB + chunk_off + i16 * 16, OFF_X1, to_chunk(p));
  }
  bar();
  STAMP(11);
  v4u w4f[28];
  auto w4off = [&](int slot) { return OFF_W4 + ((ms * 28 + ((js + 1 + slot) & 3) * 7 + w) * 7) * KB; };
#pragma unroll
  for (int c = 0; c < 7; ++c) w4f[c] = ldg(rws, l16, w4off(0) + c * KB);
  f4 cs0 = {0, 0, 0, 0}, cs1 = {0, 0, 0, 0};
#pragma unroll
  for (int s = 0; s < 14; ++s) { cs0 = mfma(w3f[s], ones, cs0); cs1 = mfma(w3f[14 + s], ones, cs1); }
  f4 pa[4], pb[4];
#pragma unroll
  for (int t = 0; t < 4; ++t) { pa[t] = f4{0, 0, 0, 0}; pb[t] = f4{0, 0, 0, 0}; }
  auto act = [&](f4 (&p)[4], int tile0) {
#pragma unroll
    for (int T = 0; T < 2; ++T) {
      const f2 st = *(LAS f2*)(L + L_ST + ((tile0 + T) * 16 + i16) * 8);
      const float rs = __builtin_amdgcn_rsqf(st[1] + 1e-5f), nb = -st[0] * rs;
      f4 u0, u1;
#pragma unroll
      for (int r = 0; r < 4; ++r) {
        u0[r] = gelu(fmaf(nb, cs0[r], p[T * 2][r] * rs));
        u1[r] = gelu(fmaf(nb, cs1[r], p[T * 2 + 1][r] * rs));
      }
      *(LAS v4u*)(L + L_HID + ((tile0 + T) * 7 + w) * KB + l16) = pack8(u0, u1);
    }
  };
  bar();
  STAMP(12);
#pragma unroll
  for (int s = 0; s < 14; ++s) {
    const v4u f0 = lds16(L, L_ACT + s * KB + l16), f1 = lds16(L, L_ACT + (14 + s) * KB + l16);
    pa[0] = mfma(w3f[s], f0, pa[0]); pa[1] = mfma(w3f[14 + s], f0, pa[1]);
    pa[2] = mfma(w3f[s], f1, pa[2]); pa[3] = mfma(w3f[14 + s], f1, pa[3]);
  }
  bar();
  STAMP(13);
#pragma unroll
  for (int s = 0; s < 14; ++s) {
    const v4u f0 = lds16(L, L_ACT + (28 + s) * KB + l16), f1 = lds16(L, L_ACT + (42 + s) * KB + l16);
    pb[0] = mfma(w3f[s], f0, pb[0]); pb[1] = mfma(w3f[14 + s], f0, pb[1]);
    pb[2] = mfma(w3f[s], f1, pb[2]); pb[3] = mfma(w3f[14 + s], f1, pb[3]);
    if (2 * s + 7 < 28) w4f[2 * s + 7] = ldg(rws, l16, w4off((2 * s + 7) / 7) + ((2 * s + 7) % 7) * KB);
    if (2 * s + 8 < 28) w4f[2 * s + 8] = ldg(rws, l16, w4off((2 * s + 8) / 7) + ((2 * s + 8) % 7) * KB);
  }
  act(pa, 0);
  bar();
  STAMP(14);
  auto down = [&](int half, bool last) {
    v4u hf[14];
#pragma unroll
    for (int k = 0; k < 14; ++k) hf[k] = lds16(L, L_HID + (half * 14 + k) * KB + l16);
#pragma unroll
    for (int sl = 0; sl < 4; ++sl) {
      const bool own = last && sl == 3;
      f4 a0 = own ? hold0 : f4{0, 0, 0, 0}, a1 = own ? hold1 : f4{0, 0, 0, 0};
#pragma unroll
      for (int c = 0; c < 7; ++c) { a0 = mfma(w4f[sl * 7 + c], hf[c], a0); a1 = mfma(w4f[sl * 7 + c], hf[7 + c], a1); }
      if (own) { hold0 = a0; hold1 = a1; }
      else {
        const int dest = yg * 8 + (last ? th : th ^ 1) * 4 + ((js + 1 + sl) & 3);
        stg(rws, l16, OFF_RS + ((dest * 8 + ms) * 7 + w) * KB, pack8(a0, a1));
      }
    }
  };
  down(0, false);
  act(pb, 2);
  bar();
  STAMP(15);
  down(1, true);
  VMW(0);
  bar();
  STAMP(16);
  bar();
  {
    v4u pp[7];
#pragma unroll
    for (int k = 0; k < 7; ++k) pp[k] = ldg(rws, l16, OFF_RS + (((yg * 8 + ms) * 8 + ((ms + 1 + k) & 7)) * 7 + w) * KB);
#pragma unroll
    for (int k = 0; k < 7; ++k) {
      hold0 += f4{blo(pp[k].x), bhi(pp[k].x), blo(pp[k].y), bhi(pp[k].y)};
      hold1 += f4{blo(pp[k].z), bhi(pp[k].z), blo(pp[k].w), bhi(pp[k].w)};
    }
    const v4u ch = to_chunk(pack8(hold0, hold1));
    *(GAS v4u*)((unsigned char*)out + (size_t)(zg * 32 + (g & 1) * 16 + i16) * 896 + (n * 16 + (g >> 1) * 8) * 2) = ch;
  }
  STAMP(17);
}

extern "C" void kernel_launch(void* const* d_in, const int* in_sizes, int n_in, void* d_out, int out_size, void* d_ws, size_t ws_size,
                              hipStream_t stream) {
  fused_kernel<<<256, 512, 0, stream>>>((const u16*)d_in[0], (const u16*)d_in[1], (const u16*)d_in[2], (const u16*)d_in[3],
                                        (const u16*)d_in[5], (const u16*)d_in[6], (const u16*)d_in[7], (const u16*)d_in[8],
                                        (u16*)d_out, (unsigned char*)d_ws);
}
```

```cpp
#include <hip/hip_runtime.h>
#include <stdint.h>

typedef unsigned int u32;
typedef unsigned long long u64;
typedef unsigned short u16;
typedef u32 v4u __attribute__((ext_vector_type(4)));
typedef u32 v2u __attribute__((ext_vector_type(2)));
typedef float f4 __attribute__((ext_vector_type(4)));
typedef float f2 __attribute__((ext_vector_type(2)));
typedef __bf16 bf8 __attribute__((ext_vector_type(8)));
typedef __bf16 bf2 __attribute__((ext_vector_type(2)));
typedef short s4v __attribute__((ext_vector_type(4)));
typedef __amdgpu_buffer_rsrc_t rsrc_t;
#define GAS __attribute__((address_space(1)))
#define LAS __attribute__((address_space(3)))
#define DI __device__ __forceinline__
#define AI __attribute__((always_inline))
#define RLX_AGENT __ATOMIC_RELAXED, __HIP_MEMORY_SCOPE_AGENT

constexpr int S_ = 2048, D_ = 448, F_ = 1792;
constexpr u32 KB = 1024;
constexpr u32 OFF_E = 0, OFF_TMO = 64;
constexpr u32 OFF_HINT = 16 * KB;
constexpr u32 OFF_KVT = 192 * KB;
constexpr u32 OFF_WO = 256 * KB;
constexpr u32 OFF_W1 = OFF_WO + 448 * KB;
constexpr u32 OFF_W2 = OFF_W1 + 392 * KB;
constexpr u32 OFF_W3 = OFF_W2 + 392 * KB;
constexpr u32 OFF_W4 = OFF_W3 + 1568 * KB;
constexpr u32 OFF_KVP = OFF_W4 + 1568 * KB;
constexpr u32 OFF_O = OFF_KVP + 2048 * KB;
constexpr u32 OFF_H = OFF_O + 2048 * KB;
constexpr u32 OFF_HS = OFF_H + 1792 * KB;
constexpr u32 OFF_X1 = OFF_HS + 1792 * KB;
constexpr u32 OFF_RS = OFF_X1 + 1792 * KB;
constexpr u32 WS_BYTES = OFF_RS + 256 * 56 * KB;
constexpr u32 OFF_DBG = WS_BYTES;
constexpr u32 L_X = 0, X_RS = 928, SLOT = 16 * X_RS;
constexpr u32 L_VB = 0, L_QA = 28 * KB, L_QX = 0;
constexpr u32 L_K = 64 * X_RS;
constexpr u32 L_PK = L_K, PK_RS = 136;
constexpr u32 L_V0 = L_PK + 128 * PK_RS, V_RS = 72;
constexpr u32 L_QB = L_K + 28 * KB;
constexpr u32 L_VA = L_K + 56 * KB;
constexpr u32 L_U = L_VA;
constexpr u32 L_KX = L_VA + 28 * KB;
constexpr u32 L_V1 = L_KX;
constexpr u32 L_ST = L_KX + 9 * KB;
constexpr u32 L_SF = L_ST + 512, L_DUMP = L_ST + 768;
constexpr u32 L_PHI = L_ST + 1024;
constexpr u32 L_BYTES = L_PHI + 4096;
static_assert(L_BYTES <= 163840 && L_V0 + 128 * V_RS <= L_QB, "");
constexpr u32 L_ACT = 0;
constexpr u32 L_HID = 56 * KB;
constexpr u32 SPIN_CAP = 1u << 15;
constexpr u32 OOB = 0x80000000u;
constexpr int SC1 = 16;
constexpr u32 CODE_BYTES = 32768;
#define XDMA dma_in
#define LDELAY 3
#define DLYA 1
#define DLYB 1
#define DLY0 1
#define DLY1 3
#define HS1 2

#define SB() __builtin_amdgcn_sched_barrier(0)
#define PIN(x) asm volatile("" : "+v"(x))
#define PINS(x) do { x = __builtin_amdgcn_readfirstlane(x); asm volatile("" : "+s"(x)); } while (0)
#define DIAG 0
#define STAMP(k) do { } while (0)
#define NRETRY(k) do { } while (0)
#define STAMP_BEGIN() do { } while (0)
#define STAMP_END() do { } while (0)

DI f4 mfma(v4u a, v4u b, f4 c) {
  return __builtin_amdgcn_mfma_f32_16x16x32_bf16(__builtin_bit_cast(bf8, a), __builtin_bit_cast(bf8, b), c, 0, 0, 0);
}
DI u32 pk2(float a, float b) { f2 v = {a, b}; return __builtin_bit_cast(u32, __builtin_convertvector(v, bf2)); }
DI float blo(u32 u) { return __builtin_bit_cast(float, u << 16); }
DI float bhi(u32 u) { return __builtin_bit_cast(float, u & 0xffff0000u); }
DI u32 f2u(float x) { return __builtin_bit_cast(u32, x); }
DI float u2f(u32 x) { return __builtin_bit_cast(float, x); }
DI u32 nosent(u32 x) { return x < 0xfffffffeu ? x : 0xfffffffeu; }
DI v4u nosent4(v4u v) { return v4u{nosent(v.x), nosent(v.y), nosent(v.z), nosent(v.w)}; }
DI u32 umax(u32 a, u32 b) { return a > b ? a : b; }
DI u32 max4(u32 m, v4u v) { m = umax(umax(m, v.x), v.y); return umax(umax(m, v.z), v.w); }
DI float fmx(float a, float b) { return __builtin_elementwise_maximum(a, b); }
DI float fmn(float a, float b) { return __builtin_elementwise_minimum(a, b); }
DI float rsum(float x) {
  auto a = __builtin_amdgcn_permlane16_swap(f2u(x), f2u(x), false, false); x = u2f(a[0]) + u2f(a[1]);
  auto c = __builtin_amdgcn_permlane32_swap(f2u(x), f2u(x), false, false); return u2f(c[0]) + u2f(c[1]);
}
DI void bar() { SB(); asm volatile("s_waitcnt lgkmcnt(0)\n\ts_barrier" ::: "memory"); SB(); }
#define VMW(n) asm volatile("s_waitcnt vmcnt(" #n ")" ::: "memory")
DI rsrc_t mkrs(const void* p, u32 bytes) { return __builtin_amdgcn_make_buffer_rsrc((void*)p, 0, (int)bytes, 0x00020000); }
DI v4u ldg(rsrc_t rs, u32 voff, u32 soff) { return __builtin_amdgcn_raw_buffer_load_b128(rs, (int)voff, (int)soff, SC1); }
DI void stg(rsrc_t rs, u32 off, v4u v) { __builtin_amdgcn_raw_buffer_store_b128(v, rs, (int)off, 0, SC1); }
template <int N, int DL>
DI void dma_in(rsrc_t rs, u32 voff, u32 soff, u32 ds, u32 lds) {
  asm volatile("s_mov_b32 m0, %4\n\ts_nop 0\n\tbuffer_load_dwordx4 %1, %2, %0 offen lds\n\t"
               ".rept %c5\n\ts_add_u32 m0, m0, %c6\n\ts_add_u32 %0, %0, %3\n\tbuffer_load_dwordx4 %1, %2, %0 offen lds\n\t.endr"
               : "+s"(soff) : "v"(voff), "s"(rs), "s"(ds), "s"(lds), "n"(N - 1), "n"(DL) : "memory", "scc");
}
template <int N, int DL>
DI void dma_nt(rsrc_t rs, u32 voff, u32 soff, u32 ds, u32 lds) {
  asm volatile("s_mov_b32 m0, %4\n\ts_nop 0\n\tbuffer_load_dwordx4 %1, %2, %0 offen nt lds\n\t"
               ".rept %c5\n\ts_add_u32 m0, m0, %c6\n\ts_add_u32 %0, %0, %3\n\tbuffer_load_dwordx4 %1, %2, %0 offen nt lds\n\t.endr"
               : "+s"(soff) : "v"(voff), "s"(rs), "s"(ds), "s"(lds), "n"(N - 1), "n"(DL) : "memory", "scc");
}
DI void touch(rsrc_t rs, u32 voff, u32 lds) {
  asm volatile("s_mov_b32 m0, %2\n\ts_nop 0\n\tbuffer_load_dword %0, %1, 0 offen sc1 lds" :: "v"(voff), "s"(rs), "s"(lds) : "memory");
}
template <int N>
DI void dma_ws(rsrc_t rs, u32 voff, u32 soff, u32 lds) {
#define L4_ "buffer_load_dwordx4 %1, %2, %0 offen sc1 lds\n\tbuffer_load_dwordx4 %1, %2, %0 offen offset:1024 sc1 lds\n\t" \
            "buffer_load_dwordx4 %1, %2, %0 offen offset:2048 sc1 lds\n\tbuffer_load_dwordx4 %1, %2, %0 offen offset:3072 sc1 lds\n\t"
  asm volatile("s_mov_b32 m0, %3\n\ts_nop 0\n\t" L4_
               ".rept %c4\n\ts_add_u32 m0, m0, 0x1000\n\ts_add_u32 %0, %0, 0x1000\n\t" L4_ ".endr"
               : "+s"(soff) : "v"(voff), "s"(rs), "s"(lds), "n"(N - 1) : "memory", "scc");
}
DI void poll(rsrc_t rs, u32 voff, u64 want) {
  u64 t; u32 cnt = SPIN_CAP;
  asm volatile("1:\n\tbuffer_load_dwordx2 %0, %2, %3, 0 offen sc1\n\ts_waitcnt vmcnt(0)\n\tv_cmp_ne_u64 vcc, %4, %0\n\ts_cbranch_vccz 2f\n\t"
               "s_sleep 1\n\ts_sub_u32 %1, %1, 1\n\ts_cbranch_scc0 1b\n2:"
               : "=&v"(t), "+s"(cnt) : "v"(voff), "s"(rs), "s"(want) : "vcc", "scc", "memory");
}
DI void poll4(rsrc_t rs, u32 voff, u64 want) {
  u64 t0, t1, t2, t3, m; u32 cnt = SPIN_CAP;
  const u32 v2 = voff + 8192;
  asm volatile("1:\n\tbuffer_load_dwordx2 %0, %6, %8, 0 offen sc1\n\tbuffer_load_dwordx2 %1, %6, %8, %10 offen sc1\n\t"
               "buffer_load_dwordx2 %2, %7, %8, 0 offen sc1\n\tbuffer_load_dwordx2 %3, %7, %8, %10 offen sc1\n\ts_waitcnt vmcnt(0)\n\t"
               "v_cmp_ne_u64 %4, %9, %0\n\tv_cmp_ne_u64 vcc, %9, %1\n\ts_or_b64 %4, %4, vcc\n\tv_cmp_ne_u64 vcc, %9, %2\n\ts_or_b64 %4, %4, vcc\n\t"
               "v_cmp_ne_u64 vcc, %9, %3\n\ts_or_b64 %4, %4, vcc\n\ts_cbranch_scc0 2f\n\t"
               "s_sleep 1\n\ts_sub_u32 %5, %5, 1\n\ts_cbranch_scc0 1b\n2:"
               : "=&v"(t0), "=&v"(t1), "=&v"(t2), "=&v"(t3), "=&s"(m), "+s"(cnt) : "v"(voff), "v"(v2), "s"(rs), "s"(want), "s"(4096) : "vcc", "scc", "memory");
}
constexpr float GC0 = -2.0f * 0.7978845608028654f * 1.4426950408889634f, GC1 = GC0 * 0.044715f;
DI float gelu(float x) {
  const float e = __builtin_amdgcn_exp2f(x * fmaf(x * x, GC1, GC0));
  return x * __builtin_amdgcn_rcpf(1.0f + e);
}

DI void hedgehog(f4 p0, f4 p1, float keep, v4u& pos, v4u& neg) {
  const float L2E = 1.4426950408889634f;
  float mx = fmx(fmx(fmx(fmx(p0[0], p0[1]), p0[2]), fmx(fmx(p0[3], p1[0]), p1[1])), fmx(p1[2], p1[3]));
  float nm = -fmn(fmn(fmn(fmn(p0[0], p0[1]), p0[2]), fmn(fmn(p0[3], p1[0]), p1[1])), fmn(p1[2], p1[3]));
  {
    auto a = __builtin_amdgcn_permlane16_swap(f2u(mx), f2u(nm), false, false);
    float t = fmx(u2f(a[0]), u2f(a[1]));
    auto c = __builtin_amdgcn_permlane32_swap(f2u(t), f2u(t), false, false);
    t = fmx(u2f(c[0]), u2f(c[1])) * -L2E;
    auto d = __builtin_amdgcn_permlane16_swap(f2u(t), f2u(t), false, false);
    mx = u2f(d[0]); nm = u2f(d[1]);
  }
  float a[8], c[8];
#pragma unroll
  for (int r = 0; r < 4; ++r) {
    a[r] = __builtin_amdgcn_exp2f(fmaf(p0[r], L2E, mx));
    c[r] = __builtin_amdgcn_exp2f(fmaf(p0[r], -L2E, nm));
    a[4 + r] = __builtin_amdgcn_exp2f(fmaf(p1[r], L2E, mx));
    c[4 + r] = __builtin_amdgcn_exp2f(fmaf(p1[r], -L2E, nm));
  }
  float sa = fmaf((a[4] + a[5]) + (a[6] + a[7]), keep, (a[0] + a[1]) + (a[2] + a[3]));
  float sc = fmaf((c[4] + c[5]) + (c[6] + c[7]), keep, (c[0] + c[1]) + (c[2] + c[3]));
  {
    auto x = __builtin_amdgcn_permlane16_swap(f2u(sa), f2u(sc), false, false);
    float t = u2f(x[0]) + u2f(x[1]);
    auto y = __builtin_amdgcn_permlane32_swap(f2u(t), f2u(t), false, false);
    t = __builtin_amdgcn_rcpf(u2f(y[0]) + u2f(y[1]));
    auto d = __builtin_amdgcn_permlane16_swap(f2u(t), f2u(t), false, false);
    sa = u2f(d[0]); sc = u2f(d[1]);
  }
  const float sa1 = sa * keep, sc1 = sc * keep;
  pos = v4u{pk2(a[0] * sa, a[1] * sa), pk2(a[2] * sa, a[3] * sa), pk2(a[4] * sa1, a[5] * sa1), pk2(a[6] * sa1, a[7] * sa1)};
  neg = v4u{pk2(c[0] * sc, c[1] * sc), pk2(c[2] * sc, c[3] * sc), pk2(c[4] * sc1, c[5] * sc1), pk2(c[6] * sc1, c[7] * sc1)};
}
DI v4u pack8(f4 a, f4 b) { return v4u{pk2(a[0], a[1]), pk2(a[2], a[3]), pk2(b[0], b[1]), pk2(b[2], b[3])}; }
DI v4u to_chunk(v4u p) {
  auto x = __builtin_amdgcn_permlane16_swap(p.x, p.z, false, false);
  auto y = __builtin_amdgcn_permlane16_swap(p.y, p.w, false, false);
  return v4u{x[0], y[0], x[1], y[1]};
}
DI float diag(f4 q, int p4) { return p4 == 0 ? q[0] : p4 == 1 ? q[1] : p4 == 2 ? q[2] : q[3]; }
DI v4u ld16(u32 addr) { return *(LAS v4u*)(uintptr_t)addr; }
DI void st16(u32 addr, v4u v) { *(LAS v4u*)(uintptr_t)addr = v; }
DI void st8(u32 addr, u32 a, u32 b) { *(LAS v2u*)(uintptr_t)addr = v2u{a, b}; }
DI f2 ldf2(u32 addr) { return *(LAS f2*)(uintptr_t)addr; }
DI v2u trread(u32 addr) {
  return __builtin_bit_cast(v2u, __builtin_amdgcn_ds_read_tr16_b64_v4i16((LAS s4v*)(uintptr_t)addr));
}
DI float dot2(u32 a, u32 sel, float c) {
  return __builtin_amdgcn_fdot2_f32_bf16(__builtin_bit_cast(bf2, a), __builtin_bit_cast(bf2, sel), c, false);
}

DI void gemm_tr(u32 t0, u32 t1, const v4u (&xa)[7], const v4u (&xb)[7], f4& a0, f4& a1, f4& b0, f4& b1) {
  constexpr int P = 3;
  PIN(t0); PIN(t1);
  v2u r[P][4];
#pragma unroll
  for (int k = 0; k < P; ++k) { r[k][0] = trread(t0 + k * 4096); r[k][1] = trread(t0 + k * 4096 + 512); r[k][2] = trread(t1 + k * 4096); r[k][3] = trread(t1 + k * 4096 + 512); }
  SB();
  a0 = f4{0, 0, 0, 0}; a1 = a0; b0 = a0; b1 = a0;
#pragma unroll
  for (int s = 0; s < 7; ++s) {
    const int k = s % P;
    const v4u f0 = {r[k][0].x, r[k][0].y, r[k][1].x, r[k][1].y}, f1 = {r[k][2].x, r[k][2].y, r[k][3].x, r[k][3].y};
    a1 = mfma(f1, xa[s], a1); b1 = mfma(f1, xb[s], b1);
    a0 = mfma(f0, xa[s], a0); b0 = mfma(f0, xb[s], b0);
    SB();
    if (s + P < 7) {
      r[k][0] = trread(t0 + (s + P) * 4096); r[k][1] = trread(t0 + (s + P) * 4096 + 512);
      r[k][2] = trread(t1 + (s + P) * 4096); r[k][3] = trread(t1 + (s + P) * 4096 + 512);
      SB();
    }
  }
}
template <int NS, class PF>
DI void gemm_alds(u32 p, const v4u (&wf)[NS], f4& a0, f4& a1, PF pf) {
  constexpr int P = 4;
  v4u b0[P], b1[P];
#pragma unroll
  for (int k = 0; k < P; ++k) { b0[k] = ld16(p + 2 * k * KB); b1[k] = ld16(p + (2 * k + 1) * KB); }
  SB();
#pragma unroll
  for (int s = 0; s < NS; ++s) {
    a1 = mfma(wf[s], b1[s % P], a1);
    a0 = mfma(wf[s], b0[s % P], a0);
    SB();
    if (s + P < NS) { b0[s % P] = ld16(p + 2 * (s + P) * KB); b1[s % P] = ld16(p + (2 * (s + P) + 1) * KB); }
    pf(s);
    SB();
  }
}
template <int NK, int EXTRA, int NT>
DI u32 g_check(u32 p, v4u (&fr)[NK], f4 (&sm)[4]) {
  const v4u ones = {0x3f803f80u, 0x3f803f80u, 0x3f803f80u, 0x3f803f80u};
  u32 m = 0;
#pragma unroll
  for (int j = 0; j <= NK / 4; ++j) {
    if (j < NK / 4) {
      asm volatile("s_waitcnt vmcnt(%0)" :: "n"(EXTRA + NK - 4 - 4 * j) : "memory");
#pragma unroll
      for (int e = 0; e < 4; ++e) fr[4 * j + e] = ld16(p + (4 * j + e) * KB);
      SB();
    }
    if (j >= 1) {
#pragma unroll
      for (int e = 3; e >= 0; --e) {
        const int k = 4 * (j - 1) + e;
        m = max4(m, fr[k]);
        if (NT) sm[k % (NT ? NT : 1)] = mfma(ones, fr[k], sm[k % (NT ? NT : 1)]);
      }
      SB();
    }
  }
  return m;
}
#define RETRY_LOOP _Pragma("clang loop unroll(disable)") for (u32 sp = 0;; ++sp)
#define RETRY_END(k) NRETRY(k); if (sp > SPIN_CAP) break; asm volatile("s_waitcnt lgkmcnt(0)" ::: "memory")

__global__ void __launch_bounds__(512, 2)
fused_kernel(const u16* __restrict__ x, const u16* __restrict__ wqkv, const u16* __restrict__ wphi, const u16* __restrict__ wo,
             const u16* __restrict__ w1, const u16* __restrict__ w2, const u16* __restrict__ w3, const u16* __restrict__ w4,
             u16* __restrict__ out, unsigned char* __restrict__ ws) {
  __shared__ __attribute__((aligned(1024))) unsigned char lds_raw[L_BYTES];
  const u32 L = (u32)(uintptr_t)(LAS unsigned char*)lds_raw;
  const int tid = threadIdx.x, lane = tid & 63, w = __builtin_amdgcn_readfirstlane(tid >> 6), g = lane >> 4, i16 = lane & 15;
  const int b = blockIdx.x, xcd = b & 7, ii = b >> 3;
  const int zg = xcd * 8 + (ii >> 2), js = ii & 3;
  const int yg = xcd * 4 + (ii >> 3), mi = ii & 7, ms = (mi + (ii >> 3)) & 7;
  const int th = (ii >> 2) & 1;
  const u32 l16 = lane * 16;
  STAMP_BEGIN();
  const int u = w >> 2, hh = w & 1, T = (w & 2) + (hh ^ u), head = mi * 2 + hh;
  asm volatile("" :: "s"(x), "s"(wqkv), "s"(wphi), "s"(out), "s"(ws));
  const rsrc_t rx = mkrs(x, S_ * D_ * 2);
  const rsrc_t rq = mkrs(wqkv, D_ * 3 * D_ * 2);
  const u32 wc = (lane & 7) ^ ((lane >> 3) & 2) ^ ((w & 1) << 2);
  u32 wv = (((w & 3) * 8 + (lane >> 3)) * 2688 + mi * 112 + wc * 16) | (wc < 7 ? 0 : OOB);
  u32 wl = L + (w & 3) * KB;
  if (u == 0) {
    __builtin_amdgcn_s_setprio(2);
    if (lane < 56) {
      const u32 r0 = (mi & 1) * 8;
      XDMA<8, X_RS>(rx, l16, (yg * 64 + w * 16 + r0) * 896, 896, L + L_X + w * SLOT + r0 * X_RS);
      XDMA<8, X_RS>(rx, l16, (yg * 64 + w * 16 + (r0 ^ 8)) * 896, 896, L + L_X + w * SLOT + (r0 ^ 8) * X_RS);
    }
  } else {
    if (w == 4) {
      u64 pc; asm volatile("s_getpc_b64 %0" : "=s"(pc));
      touch(mkrs((const void*)(pc & ~255ull), CODE_BYTES), (ii + 32 * lane) * 128, L + L_DUMP);
    } else if (w == 5) {
      touch(rq, lane < 37 ? (b * 37 + lane) * 128 : OOB, L + L_DUMP);
    } else if (w == 6) {
      touch(mkrs(ws, WS_BYTES), lane < 4 ? OFF_HINT + (b * 4 + lane) * 128 : OOB, L + L_DUMP);
    }
    __builtin_amdgcn_s_sleep(LDELAY);
    dma_in<1, 0>(mkrs(wphi, 16 * 28 * 28 * 2), l16, mi * 3136 + (w - 4) * KB, 0, L + L_PHI + (w - 4) * KB);
    dma_in<14, 4 * KB>(rq, wv, 896, 32 * 2688, wl + L_K);
  }
  SB();
  STAMP(24);
  const u32 epoch = *(const __attribute__((address_space(4))) u32*)(uintptr_t)(ws + OFF_E);
  const u64 tag = (u64)epoch | (0x9e3779b9ull << 32);
  const rsrc_t rws = mkrs(ws, WS_BYTES);
  const v4u SENT = {0xffffffffu, 0xffffffffu, 0xffffffffu, 0xffffffffu};
  const v4u ones = {0x3f803f80u, 0x3f803f80u, 0x3f803f80u, 0x3f803f80u};
  const f4 z4 = {0, 0, 0, 0};
  const int q4 = i16 >> 2, p4 = i16 & 3;
  const bool padg = g == 3;
  u32 vz = lane == 0 ? 0 : OOB;
  PIN(vz); PIN(wv); PINS(wl);
  auto post = [&](u32 off) AI { __builtin_amdgcn_raw_buffer_store_b64(v2u{epoch, 0x9e3779b9u}, rws, (int)vz, (int)off, SC1); };
  const int n = js * 7 + (w < 7 ? w : 0);
  const u32 unit = u == 0 ? 4 * b + w : w != 4 ? 2000 : ((mi + 1) & 6) == 0 ? 1024 + yg * 2 + (mi & 1) : mi == 1 ? 1088 + yg : 2000;
  const bool hasu = unit < 1092;
  u32 u_ts = 0, u_dst = 0, u_lo = 0, u_hi = 0, u_keep = 0;
  v4u u_raw[4];
  if (hasu) {
    const u32 sec = ((unit + 1936) >> 11) + ((unit + 1838) >> 11) + ((unit + 1740) >> 11) + ((unit + 1348) >> 11);
    const u64 base = ((const __attribute__((address_space(4))) u64*)__builtin_amdgcn_kernarg_segment_ptr())[3 + sec];
    const u32 first = (u32)(0x2bc1340d2070000ull >> (sec * 12)) & 0xfffu, uu = unit - first;
    const u32 sh3 = sec == 3 ? 2 : 0, prm = (sec & 3) == 0 ? 2 : 0;
    const u32 u7 = (uu * 9363) >> 16, st = u7 >> sh3, cb = uu - ((st * 7) << sh3), m = sec == 4 ? (u7 * 9363) >> 16 : 0;
    const rsrc_t rs = mkrs((const void*)base, sec >= 3 ? D_ * F_ * 2 : D_ * D_ * 2);
    const u32 vo = (st * (sec == 0 ? 28 : 32) + (lane >> 3)) * (896 << sh3) + cb * 128 + (lane & 7) * 16;
#pragma unroll
    for (int d = 0; d < 4; ++d)
      asm volatile("buffer_load_dwordx4 %0, %1, %2, 0 offen nt" : "=v"(u_raw[d]) : "v"(vo + ((d * 8 * 896) << sh3)), "s"(rs) : "memory");
    u_ts = ((0x7eee0u >> (sec * 4)) & 15) * KB + (sec == 0 ? 16 * KB : 0);
    u_dst = l16 + OFF_WO + first * 4 * KB + cb * 4 * u_ts + (m * 189 + st) * KB;
    u_lo = L + L_U + w * 4 * KB + ((g << (3 - (prm >> 1))) + q4) * 128 + p4 * 8; u_hi = u_lo + (512 << prm);
    u_keep = (sec == 0 && padg) ? 0u : 0xffffffffu;
  }
  u32 u_sw = L + L_U + w * 4 * KB + l16;
  PIN(u_sw); PINS(u_ts); PIN(u_dst); PIN(u_lo); PIN(u_hi); PIN(u_keep);
  if (u == 1) dma_in<7, 4 * KB>(rq, wv, 1792, 32 * 2688, wl + L_VA);
  SB();
  STAMP(46);
  u32 kvp_off = OFF_KVP + ((mi * 8 + w) * 4 + g) * 8 * KB + yg * 256 + i16 * 16;
  u32 kvt_off = g == 0 ? OFF_KVT + (mi * 32 + yg) * 256 + i16 * 16 : OOB;
  u32 o_off = l16 + OFF_O + (((yg * 2 + (T >> 1)) * 16 + head) * 2 + (T & 1)) * KB;
  u32 sel_lo = 0x00003f80u, sel_hi = 0x3f800000u;
  float keep = padg ? 0.f : 1.f;
  const u32 trx = (((q4 >> 1) & 1) | ((g & 1) << 1)) * 32;
  u32 tr0 = L + (8 * g + q4) * 128 + ((56 * hh + 8 * p4) ^ trx);
  u32 tr1 = L + (8 * g + q4) * 128 + ((56 * hh + 32 + 8 * (p4 == 3 ? 2 : p4)) ^ trx);
  u32 kx_w = L + L_KX + (w ^ 4) * KB + l16, kx_r = L + L_KX + w * KB + l16;
  u32 pk_w = L + L_PK + (hh * 64 + T * 16 + i16) * PK_RS + g * 8;
  u32 va_w = L + (u ? L_V1 : L_V0) + (hh * 64 + T * 16 + i16) * V_RS + g * 8, vb_w = L + (u ? L_V1 : L_V0) + (hh * 64 + (T ^ 1) * 16 + i16) * V_RS + g * 8;
  u32 kv_a = L + L_PK + (u * 64 + g * 8 + q4) * PK_RS + ((w >> 1) & 1) * 64 + p4 * 8;
  u32 kv_b = L + L_V0 + (u * 64 + g * 8 + q4) * V_RS + (w & 1) * 32 + p4 * 8, kv_c = kv_b + (L_V1 - L_V0);
  u32 ph_a = L + L_PHI + hh * 1568 + (4 * g + q4) * 56 + p4 * 8;
  u32 xo_r = L + L_X + (T ^ 1) * SLOT + u * 7 * KB + l16;
  u32 h32 = OFF_HINT + (mi * 32 + (lane & 31)) * 64;
  const u32 cko = (2 * (n & 1) + (g >> 1)) * 256;
  u32 h_off = ((zg * 14 + (n >> 1)) * 2 + (g & 1)) * KB + cko + i16 * 16 + OFF_H;
  PIN(h_off);
  PIN(kvp_off); PIN(kvt_off); PIN(o_off); PIN(sel_lo); PIN(sel_hi); PIN(keep); PIN(tr0); PIN(tr1); PIN(kx_w); PIN(kx_r);
  PIN(pk_w); PIN(va_w); PIN(vb_w); PIN(kv_a); PIN(kv_b); PIN(kv_c); PIN(ph_a); PIN(xo_r); PIN(h32);
  SB();
  v4u xa[7], xb[7];
  if (u == 0) {
    u32 xp = L + L_X + w * SLOT + i16 * X_RS + g * 16, xw = L + L_X + w * SLOT + l16;
    PIN(xp); PIN(xw);
    VMW(4);
    STAMP(25);
#pragma unroll
    for (int s = 0; s < 7; ++s) xb[s] = ld16(xp + 448 + s * 64);
#pragma unroll
    for (int s = 0; s < 7; ++s) xa[s] = ld16(xp + s * 64);
    SB();
    f4 sq = z4, sm = z4;
#pragma unroll
    for (int s = 0; s < 7; ++s) { sq = mfma(xb[s], xb[s], sq); sm = mfma(ones, xb[s], sm); }
#pragma unroll
    for (int s = 0; s < 7; ++s) { sq = mfma(xa[s], xa[s], sq); sm = mfma(ones, xa[s], sm); }
    const float mu = sm[0] * (1.0f / D_);
    const float var = fmx(fmaf(-mu, mu, rsum(g == q4 ? diag(sq, p4) : 0.f) * (1.0f / D_)), 0.f);
    const float rs = __builtin_amdgcn_rsqf(var + 1e-5f), nb = -mu * rs;
    STAMP(26);
#pragma unroll
    for (int s = 0; s < 7; ++s) {
#pragma unroll
      for (int e = 0; e < 4; ++e) xb[s][e] = pk2(fmaf(blo(xb[s][e]), rs, nb), fmaf(bhi(xb[s][e]), rs, nb));
      st16(xw + (7 + s) * KB, xb[s]);
      SB();
    }
#pragma unroll
    for (int s = 0; s < 7; ++s) {
#pragma unroll
      for (int e = 0; e < 4; ++e) xa[s][e] = pk2(fmaf(blo(xa[s][e]), rs, nb), fmaf(bhi(xa[s][e]), rs, nb));
      st16(xw + s * KB, xa[s]);
      SB();
    }
    __builtin_amdgcn_s_setprio(0);
  } else {
    stg(rws, kvp_off, SENT); stg(rws, kvp_off - 4 * 32 * KB, SENT);
    stg(rws, o_off, SENT); stg(rws, o_off ^ KB, SENT);
    const int n4 = js * 7 + w - 4;
    const u32 h4 = ((zg * 14 + (n4 >> 1)) * 2 + (g & 1)) * KB + (2 * (n4 & 1) + (g >> 1)) * 256 + i16 * 16 + OFF_H;
    stg(rws, h4, SENT); stg(rws, w == 7 ? kvt_off : h4 + 4 * KB, SENT);
  }
  STAMP(27);
  bar();
  STAMP(18);
#pragma unroll
  for (int s = 0; s < 7; ++s) xb[s] = ld16(xo_r + s * KB);
  if (u == 1) {
    const u32 xm = L + L_X + T * SLOT + 7 * KB + l16;
#pragma unroll
    for (int s = 0; s < 7; ++s) xa[s] = ld16(xm + s * KB);
    VMW(13);
  }
  bar();
  STAMP(1);
  v4u wo_[16], w1_[14], w2_[14], w3f[28], w4f[28];
  u32 lw = w < 7 ? l16 : OOB;
  PIN(lw);
  auto pf = [&](int k) AI {
    if (k < 16) wo_[k] = ldg(rws, lw + (k & 3) * KB, OFF_WO + (n * 16 + (k & ~3)) * KB);
    else if (k < 30) w1_[k - 16] = ldg(rws, lw + (k & 3) * KB, OFF_W1 + (n * 14 + ((k - 16) & ~3)) * KB);
    else if (k < 44) w2_[k - 30] = ldg(rws, lw + ((k - 30) & 3) * KB, OFF_W2 + (n * 14 + ((k - 30) & ~3)) * KB);
    else if (k < 72) { const int t = (k - 44) & 1, st = (k - 44) >> 1;
      w3f[t * 14 + st] = ldg(rws, lw + (st & 3) * KB, OFF_W3 + ((ms * 14 + w * 2 + t) * 14 + (st & ~3)) * KB); }
    else if (k < 100) { const int sl = (k - 72) / 7, c = (k - 72) % 7;
      w4f[k - 72] = ldg(rws, lw + (c & 3) * KB, OFF_W4 + ((ms * 28 + ((js + 1 + sl) & 3) * 7 + w) * 7 + (c & ~3)) * KB); }
  };
#define PFR(lo, hi) do { _Pragma("unroll") for (int k_ = (lo); k_ < (hi); ++k_) pf(k_); asm volatile("" ::: "memory"); SB(); } while (0)
#define PFS(base, cnt) [&](int s_) AI { if ((s_ & 1) == 0 && (s_ >> 1) < (cnt)) pf((base) + (s_ >> 1)); }
  v4u phA, phB;
  {
    if (u == 1) dma_in<7, 4 * KB>(rq, wv, 1792 + 224 * 2688, 32 * 2688, wl + L_VB);
    STAMP(20);
    f4 a0, a1, b0, b1;
    gemm_tr(tr0 + L_K + u * 28 * KB, tr1 + L_K + u * 28 * KB, xa, xb, a0, a1, b0, b1);
    {
      const u32 a = ph_a, c = a + (p4 == 3 ? 24 : 32);
      const v2u l0 = trread(a), h0 = trread(a + 16 * 56), l1 = trread(c), h1 = trread(c + 16 * 56);
      st16(kx_w, pack8(b0, b1));
      phA = v4u{l0.x, l0.y, padg ? 0u : h0.x, padg ? 0u : h0.y};
      phB = v4u{l1.x, l1.y, padg ? 0u : h1.x, padg ? 0u : h1.y};
      PIN(phA); PIN(phB);
    }
    const v4u ka = pack8(a0, a1);
    f4 p0 = mfma(phA, ka, z4), p1 = mfma(phB, ka, z4);
    STAMP(2);
    if (u == 0) dma_in<7, 4 * KB>(rq, wv, 0, 32 * 2688, wl + L_QA);
    else VMW(7);
    STAMP(19);
    bar();
    STAMP(3);
    const v4u kb = ld16(kx_r);
    if (u == 0) dma_in<7, 4 * KB>(rq, wv, 224 * 2688, 32 * 2688, wl + L_QB);
    SB();
    p0 = mfma(phA, kb, p0); p1 = mfma(phB, kb, p1);
    PIN(p0); PIN(p1);
    const u32 vbase = u ? L_VB : L_VA;
#pragma clang loop unroll(disable)
    for (int ph = 0; ph < 2; ++ph) {
      if (ph == u) {
        gemm_tr(tr0 + vbase, tr1 + vbase, xa, xb, a0, a1, b0, b1);
        if (padg) { a1[0] = (float)(1 - u); b1[0] = a1[0]; }
        const v4u va = pack8(a0, a1), vb = pack8(b0, b1);
        st8(va_w, va.x, va.y); st8(va_w + 32, va.z, va.w);
        st8(vb_w, vb.x, vb.y); st8(vb_w + 32, vb.z, vb.w);
      } else {
        v4u pos, neg;
        hedgehog(p0, p1, keep, pos, neg);
        st8(pk_w, pos.x, pos.y); st8(pk_w + 32, pos.z, pos.w); st8(pk_w + 64, neg.x, neg.y); st8(pk_w + 96, neg.z, neg.w);
      }
      if (ph == 0) {
        STAMP(21);
        if (u == 1) VMW(0);
        bar();
        STAMP(22);
    if (hasu) {
      VMW(14);
#pragma unroll
      for (int d = 0; d < 4; ++d) st16(u_sw + d * KB, u_raw[d]);
      asm volatile("s_waitcnt lgkmcnt(0)" ::: "memory");
      v2u lo[4], hi[4];
#pragma unroll
      for (int t = 0; t < 4; ++t) { lo[t] = trread(u_lo + t * 32); hi[t] = trread(u_hi + t * 32); }
      SB();
#pragma unroll
      for (int t = 0; t < 4; ++t) stg(rws, u_dst + t * u_ts, v4u{lo[t].x, lo[t].y, hi[t].x & u_keep, hi[t].y & u_keep});
    }
    SB();

      }
    }
    if (hasu) VMW(0);
  }
  STAMP(23);
  bar();
  STAMP(4);
  if (w == 7) post(OFF_HINT + (5 * 256 + mi * 32 + yg) * 64);
  {
    v2u a0[2], a1[2], b0[2], b1[2], d0[4], d1[4];
#pragma unroll
    for (int s = 0; s < 2; ++s) {
      a0[s] = trread(kv_a + s * 32 * PK_RS); a1[s] = trread(kv_a + (s * 32 + 4) * PK_RS);
      d0[s] = trread(kv_b + s * 32 * V_RS); d1[s] = trread(kv_b + (s * 32 + 4) * V_RS);
      b0[s] = trread(kv_a + s * 32 * PK_RS + 32); b1[s] = trread(kv_a + (s * 32 + 4) * PK_RS + 32);
    }
#pragma unroll
    for (int s = 0; s < 2; ++s) { d0[2 + s] = trread(kv_c + s * 32 * V_RS); d1[2 + s] = trread(kv_c + (s * 32 + 4) * V_RS); }
    SB();
    f4 c0 = z4, c1 = z4;
#pragma unroll
    for (int s = 0; s < 4; ++s) {
      const v4u vf = {d0[s].x, d0[s].y, d1[s].x, d1[s].y};
      c0 = mfma(v4u{a0[s & 1].x, a0[s & 1].y, a1[s & 1].x, a1[s & 1].y}, vf, c0);
      c1 = mfma(v4u{b0[s & 1].x, b0[s & 1].y, b1[s & 1].x, b1[s & 1].y}, vf, c1);
    }
    stg(rws, kvp_off, nosent4(pack8(c0, c1)));
    SB();
  }
  STAMP(28);
  v2u xr[2];
  {
#pragma unroll
    for (int t = 0; t < 2; ++t)
      xr[t] = __builtin_amdgcn_raw_buffer_load_b64(rx, (int)((zg * 32 + t * 16 + i16) * 896 + (n * 16 + g * 4) * 2), 0, 0);
    SB();
    f4 a0, a1, b0, b1;
    const u32 qbase = u ? L_QB : L_QA;
    gemm_tr(tr0 + qbase, tr1 + qbase, xa, xb, a0, a1, b0, b1);
    st16(kx_w - (L_KX - L_QX), pack8(b0, b1));
    const v4u qa = pack8(a0, a1);
    f4 p0 = mfma(phA, qa, z4), p1 = mfma(phB, qa, z4);
    PIN(p0); PIN(p1);
    STAMP(5);
    v4u kp[8];
    if (w == 7) {
      poll4(rws, OFF_HINT + 5 * 256 * 64 + lane * 64, tag);
      __builtin_amdgcn_s_sleep(DLYA);
#pragma unroll
      for (int k = 0; k < 8; ++k) kp[k] = ldg(rws, l16 + (k & 3) * KB, OFF_KVP + ((mi * 32 + yg) * 8 + (k & 4)) * KB);
    }
    STAMP(6);
    bar();
    STAMP(7);
    const v4u qb = ld16(kx_r - (L_KX - L_QX));
    p0 = mfma(phA, qb, p0); p1 = mfma(phB, qb, p1);
    v4u qpos, qneg;
    if (w == 7) {
      float lo[4], hi[4];
      RETRY_LOOP {
#pragma unroll
        for (int e = 0; e < 4; ++e) { lo[e] = 0.f; hi[e] = 0.f; }
#pragma unroll
        for (int k = 0; k < 8; ++k) {
#pragma unroll
          for (int e = 0; e < 4; ++e) { lo[e] = dot2(kp[k][e], sel_lo, lo[e]); hi[e] = dot2(kp[k][e], sel_hi, hi[e]); }
          SB();
        }
        const float t = (lo[0] + lo[1]) + (lo[2] + lo[3]);
        if (__all(t == t)) break;
        u32 m = 0;
#pragma unroll
        for (int k = 0; k < 8; ++k) m = max4(m, kp[k]);
        if (__all(m != 0xffffffffu)) break;
        RETRY_END(1);
#pragma unroll
        for (int k = 0; k < 8; ++k) kp[k] = ldg(rws, l16 + (k & 3) * KB, OFF_KVP + ((mi * 32 + yg) * 8 + (k & 4)) * KB);
        SB();
      }
      post(OFF_HINT + (6 * 256 + mi * 32 + yg) * 64);
      v4u sum;
#pragma unroll
      for (int e = 0; e < 4; ++e) {
        auto a = __builtin_amdgcn_permlane16_swap(f2u(lo[e]), f2u(hi[e]), false, false);
        const float t = u2f(a[0]) + u2f(a[1]);
        auto c = __builtin_amdgcn_permlane32_swap(f2u(t), f2u(t), false, false);
        const float v = u2f(c[0]) + u2f(c[1]);
        auto d = __builtin_amdgcn_permlane16_swap(f2u(v), f2u(v), false, false);
        sum[e] = nosent(pk2(u2f(d[0]), u2f(d[1])));
      }
      stg(rws, kvt_off, sum);
      SB();
      STAMP(30);
    } else {
      PFR(0, 8);
    }
    hedgehog(p0, p1, keep, qpos, qneg);
    PIN(qpos); PIN(qneg);
    STAMP(31);
    if (w == 7) { poll(rws, h32 + 6 * 256 * 64, tag); __builtin_amdgcn_s_sleep(DLYB); }
    else VMW(0);
    STAMP(45);
    bar();
    v4u kf[4];
#pragma unroll
    for (int k = 0; k < 4; ++k) kf[k] = ldg(rws, l16 + k * KB, OFF_KVT + head * 4 * KB);
    SB();
    if (w == 2) post(OFF_HINT + (0 * 256 + yg * 8 + mi) * 64);
    SB();
    f4 n0, n1;
    RETRY_LOOP {
      n1 = mfma(kf[1], qpos, z4); n0 = mfma(kf[0], qpos, z4);
      n1 = mfma(kf[3], qneg, n1); n0 = mfma(kf[2], qneg, n0);
      u32 m = 0;
#pragma unroll
      for (int k = 0; k < 4; ++k) m = max4(m, kf[k]);
      if (__all(m != 0xffffffffu)) break;
      RETRY_END(2);
#pragma unroll
      for (int k = 0; k < 4; ++k) kf[k] = ldg(rws, l16 + k * KB, OFF_KVT + head * 4 * KB);
    }
    const float rd = __builtin_amdgcn_rcpf(rsum(padg ? n1[0] : 0.f) + 1e-6f);
    n0 *= rd; n1 *= rd;
    v4u ov = nosent4(pack8(n0, n1));
    if (padg) { ov.z = 0; ov.w = 0; }
    stg(rws, o_off, ov);
  }
  bar();
  STAMP(8);

  if (w == 7) {
    __builtin_amdgcn_s_setprio(3);
    const u32 P = L + L_ACT + l16;
    const u32 hme = OFF_HINT + (zg * 4 + js) * 64;
    u32 h8 = OFF_HINT + (yg * 8 + (lane & 7)) * 64;
    f4 sm[4];
    poll(rws, h8, tag);
    __builtin_amdgcn_s_sleep(DLY0);
    STAMP(9);
    {
      v4u fr[32];
      RETRY_LOOP {
        dma_ws<8>(rws, l16, OFF_O + zg * 32 * KB, L + L_ACT);
        if (sp == 0) bar();
        if (__all(g_check<32, 0, 0>(P, fr, sm) != 0xffffffffu)) break;
        RETRY_END(3);
      }
    }
    u32 hA = OFF_HINT + (1 * 256 + zg * 4 + (lane & 3)) * 64;
    u32 hN = OFF_HINT + (7 * 256 + (((js & 1) ? zg + 63 : zg + 1) & 63) * 4 + js) * 64;
    u32 hB = OFF_HINT + (2 * 256 + ((lane & 4) ? ((lane & 1) ? zg + 1 : zg + 63) & 63 : zg) * 4 + (lane & 3)) * 64;
    u32 st_w = L + L_ST + i16 * 8;
    bar();
    STAMP(10);
    post(hme + 1 * 256 * 64);
    PIN(hA); PIN(hB); PIN(hN); PIN(st_w);
    *(LAS u32*)(uintptr_t)(L + L_SF) = 0;
    poll(rws, hA, tag);
    __builtin_amdgcn_s_sleep(HS1);
    STAMP(32);
    bar();
    __builtin_amdgcn_s_sleep(DLY1);
    STAMP(11);
    {
      v4u fr[28];
      RETRY_LOOP {
        dma_ws<7>(rws, l16, OFF_H + zg * 28 * KB, L + L_ACT);
        if (sp == 0) bar();
        sm[0] = z4; sm[1] = z4;
        if (__all(g_check<28, 0, 2>(P, fr, sm) != 0xffffffffu)) break;
        RETRY_END(4);
      }
      bar();
      STAMP(12);
      v2u nm = __builtin_amdgcn_raw_buffer_load_b64(rws, (int)hN, 0, SC1);
      post(hme + 2 * 256 * 64);
      SB();
      f4 q0 = z4, q1 = z4;
#pragma unroll
      for (int s = 0; s < 14; ++s) { q0 = mfma(fr[2 * s], fr[2 * s], q0); q1 = mfma(fr[2 * s + 1], fr[2 * s + 1], q1); }
      const float m0 = sm[0][0] * (1.0f / D_), m1 = sm[1][0] * (1.0f / D_);
      const float v0 = fmx(fmaf(-m0, m0, diag(q0, p4) * (1.0f / D_)), 0.f), v1 = fmx(fmaf(-m1, m1, diag(q1, p4) * (1.0f / D_)), 0.f);
      if (g == q4) { st8(st_w, f2u(m0), f2u(v0)); st8(st_w + 128, f2u(m1), f2u(v1)); }
      PIN(nm);
      if (!__all(nm.x == epoch && nm.y == 0x9e3779b9u)) poll(rws, hN, tag);
    }
    STAMP(33);
    bar();
    poll(rws, hB, tag);
    STAMP(34);
    bar();
    __builtin_amdgcn_s_sleep(DLY1);
    STAMP(13);
    {
      v4u fr[28];
      RETRY_LOOP {
        dma_ws<7>(rws, l16, OFF_HS + zg * 28 * KB, L + L_ACT);
        if (sp == 0) bar();
        if (__all(g_check<28, 0, 0>(P, fr, sm) != 0xffffffffu)) break;
        RETRY_END(5);
      }
    }
    bar();
    STAMP(14);
    post(hme + 3 * 256 * 64);
    poll(rws, h8 + 3 * 256 * 64, tag);
    __builtin_amdgcn_s_sleep(HS1);
    STAMP(35);
    bar();
    __builtin_amdgcn_s_sleep(DLY1);
    STAMP(15);
    {
      v4u fr[28];
      f4 sq[4] = {z4, z4, z4, z4};
      RETRY_LOOP {
        dma_ws<14>(rws, l16, OFF_X1 + yg * 56 * KB, L + L_ACT);
        if (sp == 0) bar();
#pragma unroll
        for (int t = 0; t < 4; ++t) sm[t] = z4;
        if (__all(g_check<28, 28, 4>(P, fr, sm) != 0xffffffffu)) break;
        RETRY_END(6);
        VMW(0);
      }
      bar();
      STAMP(16);
      RETRY_LOOP {
        v4u fs[28];
        if (__all(g_check<28, 0, 0>(P + 28 * KB, fs, sm) != 0xffffffffu)) break;
        RETRY_END(7);
        dma_ws<7>(rws, l16, OFF_X1 + (yg * 56 + 28) * KB, L + L_ACT + 28 * KB);
      }
      bar();
      STAMP(36);
#pragma unroll
      for (int k = 0; k < 28; k += 4) {
#pragma unroll
        for (int t = 0; t < 4; ++t) sq[t] = mfma(fr[k + t], fr[k + t], sq[t]);
        SB();
#pragma unroll
        for (int t = 0; t < 4; ++t) fr[k + t] = ld16(P + (28 + k + t) * KB);
        SB();
      }
#pragma unroll
      for (int k = 0; k < 28; ++k) { sq[k & 3] = mfma(fr[k], fr[k], sq[k & 3]); sm[k & 3] = mfma(ones, fr[k], sm[k & 3]); }
#pragma unroll
      for (int t = 0; t < 4; ++t) {
        const float m0 = sm[t][0] * (1.0f / D_);
        const float r0 = __builtin_amdgcn_rsqf(fmx(fmaf(-m0, m0, diag(sq[t], p4) * (1.0f / D_)), 0.f) + 1e-5f);
        if (g == q4) st8(st_w + t * 128, f2u(-m0), f2u(r0));
      }
      asm volatile("s_waitcnt lgkmcnt(0)" ::: "memory");
      *(volatile LAS u32*)(uintptr_t)(L + L_SF) = 1;
    }
    STAMP(37);
    bar();
    bar();
    post(hme + 4 * 256 * 64);
    poll(rws, h8 + 4 * 256 * 64, tag);
    STAMP(17);
    bar();
    if (b == 0 && lane == 0) __hip_atomic_store((GAS u32*)(ws + OFF_E), epoch + 1, RLX_AGENT);
    STAMP_END();
    return;
  }
  f4 hold0, hold1;
  const u32 A = L + L_ACT + l16;
  const int tk = zg * 32 + (g & 1) * 16 + i16 + ((js & 1) ? -1 - (js >> 1) : 1 + (js >> 1));
  u32 hs_off = ((((tk & (S_ - 1)) >> 5) * 14 + (n >> 1)) * 2 + ((tk >> 4) & 1)) * KB + cko + (tk & 15) * 16 + OFF_HS;
  u32 hs_keep = (tk < 0 || tk >= S_) ? 0u : 0xffffffffu;
  u32 x1_off = ((yg * 14 + (n >> 1)) * 4 + th * 2 + (g & 1)) * KB + cko + i16 * 16 + OFF_X1;
  u32 st_r = L + L_ST + i16 * 8;
  PFR(8, 11);
  PIN(hs_off); PIN(hs_keep); PIN(x1_off); PIN(st_r);
  f4 a0 = {blo(xr[0].x), bhi(xr[0].x), blo(xr[0].y), bhi(xr[0].y)};
  f4 a1 = {blo(xr[1].x), bhi(xr[1].x), blo(xr[1].y), bhi(xr[1].y)};
  PIN(a0); PIN(a1);
  VMW(0);
  bar();
  stg(rws, h_off + (OFF_HS - OFF_H), SENT);
  PFR(11, 16);
  bar();
  STAMP(32);
  {
    gemm_alds<16>(A, wo_, a0, a1, PFS(16, 8));
    STAMP(33);
    const v4u p = nosent4(pack8(a0, a1));
    stg(rws, h_off, to_chunk(p));
    SB();
    hold0 = f4{blo(p.x), bhi(p.x), blo(p.y), bhi(p.y)};
    hold1 = f4{blo(p.z), bhi(p.z), blo(p.w), bhi(p.w)};
  }
  bar();
  STAMP(9);
  f4 xb0, xb1;
  {
    PFR(24, 27);
    f4 cs = z4;
#pragma unroll
    for (int s = 0; s < 11; ++s) cs = mfma(w1_[s], ones, cs);
    PIN(cs);
    VMW(0);
    bar();
    if (w == 0) post(OFF_HINT + (7 * 256 + zg * 4 + js) * 64);
    stg(rws, x1_off, SENT);
    PFR(27, 30);
    bar();
    STAMP(34);
    a0 = z4; a1 = z4;
    gemm_alds<14>(A, w1_, a0, a1, PFS(30, 7));
#pragma unroll
    for (int s = 11; s < 14; ++s) cs = mfma(w1_[s], ones, cs);
    STAMP(35);
    bar();
    STAMP(36);
    const f2 s0 = ldf2(st_r), s1 = ldf2(st_r + 128);
    const float r0 = __builtin_amdgcn_rsqf(s0[1] + 1e-5f), r1 = __builtin_amdgcn_rsqf(s1[1] + 1e-5f);
    const float t0 = r0 * __builtin_amdgcn_rsqf(s0[1] * r0 * r0 + 1e-5f), t1 = r1 * __builtin_amdgcn_rsqf(s1[1] * r1 * r1 + 1e-5f);
#pragma unroll
    for (int r = 0; r < 4; ++r) {
      a0[r] = gelu(fmaf(-s0[0], cs[r], a0[r]) * t0);
      a1[r] = gelu(fmaf(-s1[0], cs[r], a1[r]) * t1);
    }
    stg(rws, hs_off, to_chunk(nosent4(pack8(a0, a1))) & hs_keep);
    bar();
#pragma unroll
    for (int r = 0; r < 4; ++r) { xb0[r] = (hold0[r] - s0[0]) * r0; xb1[r] = (hold1[r] - s1[0]) * r1; }
    PIN(xb0); PIN(xb1);
  }
  STAMP(10);
  PFR(37, 40);
  VMW(0);
  bar();
  PFR(40, 44);
  bar();
  STAMP(37);
  {
    gemm_alds<14>(A, w2_, xb0, xb1, PFS(44, 7));
    STAMP(38);
    const v4u p = nosent4(pack8(xb0, xb1));
    stg(rws, x1_off, to_chunk(p));
    bar();
    hold0 += f4{blo(p.x), bhi(p.x), blo(p.y), bhi(p.y)};
    hold1 += f4{blo(p.z), bhi(p.z), blo(p.w), bhi(p.w)};
    PIN(hold0); PIN(hold1);
  }
  STAMP(11);
  constexpr int W3E = 54;
  PFR(51, W3E);
  f4 cs[2] = {z4, z4};
#pragma unroll
  for (int s = 0; s < (W3E - 44) / 2; ++s) { cs[0] = mfma(w3f[s], ones, cs[0]); cs[1] = mfma(w3f[14 + s], ones, cs[1]); }
  PIN(cs[0]); PIN(cs[1]);
  u32 hid_w = L + L_HID + w * 2 * KB + l16, hid_r = L + L_HID + l16;
  u32 rs_w = l16 + OFF_RS + ((yg * 64 + mi) * 7 + w) * KB;
  u32 out_off = (zg * 32 + (g & 1) * 16 + i16) * 896 + (n * 16 + (g >> 1) * 8) * 2;
  PIN(hid_w); PIN(hid_r); PIN(rs_w); PIN(out_off);
  bar();
  PFR(W3E, W3E + 2);
  STAMP(44);
  bar();
  STAMP(12);
  f4 pu[8] = {z4, z4, z4, z4, z4, z4, z4, z4};
  {
    v4u bf[2][4];
#pragma unroll
    for (int t = 0; t < 4; ++t) { bf[0][t] = ld16(A + t * KB); }
#pragma unroll
    for (int t = 0; t < 4; ++t) { bf[1][t] = ld16(A + (4 + t) * KB); }
    SB();
#pragma unroll
    for (int s = 0; s < 14; ++s) {
      if (s == 3) { SB(); asm volatile("s_barrier" ::: "memory"); SB(); }
#pragma unroll
      for (int t = 3; t >= 0; --t) { pu[2 * t] = mfma(w3f[s], bf[s & 1][t], pu[2 * t]); pu[2 * t + 1] = mfma(w3f[14 + s], bf[s & 1][t], pu[2 * t + 1]); }
      if (s >= (W3E - 44) / 2) { cs[0] = mfma(w3f[s], ones, cs[0]); cs[1] = mfma(w3f[14 + s], ones, cs[1]); }
      SB();
      if (s + 2 < 14) {
#pragma unroll
        for (int t = 0; t < 4; ++t) bf[s & 1][t] = ld16(A + ((s + 2) * 4 + t) * KB);
      }
      pf(W3E + 2 + 2 * s); pf(W3E + 3 + 2 * s);
      SB();
    }
  }
  STAMP(39);
  for (u32 sp = 0; sp < (1u << 20) && *(volatile LAS u32*)(uintptr_t)(L + L_SF) == 0; ++sp) { }
  float fr0, fr1;
  {
    fr0 = ldf2(st_r + th * 256)[1]; fr1 = ldf2(st_r + th * 256 + 128)[1];
#pragma unroll
    for (int t = 0; t < 4; ++t) {
      const f2 st = ldf2(st_r + t * 128);
      const float k0 = GC0 * st[1], k1 = GC1 * st[1] * st[1] * st[1];
#pragma unroll
      for (int c = 0; c < 2; ++c)
#pragma unroll
        for (int r = 0; r < 4; ++r) {
          const float y = fmaf(st[0], cs[c][r], pu[2 * t + c][r]);
          pu[2 * t + c][r] = y * __builtin_amdgcn_rcpf(1.0f + __builtin_amdgcn_exp2f(y * fmaf(y * y, k1, k0)));
        }
      st16(hid_w + (t >> 1) * 14 * KB + (t & 1) * KB, pack8(pu[2 * t], pu[2 * t + 1]));
#pragma unroll
      for (int k = 0; k < 4; ++k) pf(W3E + 30 + 4 * t + k);
      SB();
    }
  }
  STAMP(40);
  bar();
  STAMP(14);
  f4 own0, own1;
  {
    v4u hf[14];
    const u32 hA = hid_r + (th ^ 1) * 14 * KB, hB = hid_r + th * 14 * KB;
#pragma unroll
    for (int k = 0; k < 14; ++k) hf[k] = ld16(hA + k * KB);
    SB();
#pragma unroll
    for (int hx = 0; hx < 2; ++hx) {
#pragma unroll
      for (int sl = 0; sl < 4; ++sl) {
        f4 c0 = z4, c1 = z4;
#pragma unroll
        for (int c = 0; c < 7; ++c) {
          c1 = mfma(w4f[sl * 7 + c], hf[2 * c + 1], c1); c0 = mfma(w4f[sl * 7 + c], hf[2 * c], c0);
          if (hx == 0 && sl == 3) { SB(); hf[2 * c] = ld16(hB + 2 * c * KB); hf[2 * c + 1] = ld16(hB + (2 * c + 1) * KB); SB(); }
          if (c == 1 && (hx | sl)) {
            SB();
            const int ph = sl == 0 ? hx - 1 : hx, ps = (sl + 3) & 3;
            stg(rws, rs_w + (((ph ? th : th ^ 1) * 4 + ((js + 1 + ps) & 3)) * 56) * KB, pack8(own0, own1));
            SB();
          }
        }
        own0 = c0; own1 = c1;
      }
    }
    PIN(own0); PIN(own1);
  }
  STAMP(42);
  VMW(0);
  bar();
  STAMP(16);
  u32 rs_r[7];
#pragma unroll
  for (int k = 0; k < 7; ++k) { rs_r[k] = OFF_RS + (((yg * 8 + mi) * 8 + ((mi + 1 + k) & 7)) * 7 + w) * KB; PINS(rs_r[k]); }
  bar();
  STAMP(43);
  {
    v4u pp[7];
#pragma unroll
    for (int k = 0; k < 7; ++k) pp[k] = ldg(rws, l16, rs_r[k]);
    SB();
#pragma unroll
    for (int k = 0; k < 7; ++k) {
      own0 = f4{dot2(pp[k].x, sel_lo, own0[0]), dot2(pp[k].x, sel_hi, own0[1]), dot2(pp[k].y, sel_lo, own0[2]), dot2(pp[k].y, sel_hi, own0[3])};
      own1 = f4{dot2(pp[k].z, sel_lo, own1[0]), dot2(pp[k].z, sel_hi, own1[1]), dot2(pp[k].w, sel_lo, own1[2]), dot2(pp[k].w, sel_hi, own1[3])};
      SB();
    }
#pragma unroll
    for (int r = 0; r < 4; ++r) { hold0[r] = fmaf(own0[r], fr0, hold0[r]); hold1[r] = fmaf(own1[r], fr1, hold1[r]); }
    *(GAS v4u*)((unsigned char*)out + out_off) = to_chunk(pack8(hold0, hold1));
  }
  STAMP(17);
  STAMP_END();
}

extern "C" void kernel_launch(void* const* d_in, const int* in_sizes, int n_in, void* d_out, int out_size, void* d_ws, size_t ws_size,
                              hipStream_t stream) {
  fused_kernel<<<256, 512, 0, stream>>>((const u16*)d_in[0], (const u16*)d_in[1], (const u16*)d_in[2], (const u16*)d_in[3],
                                        (const u16*)d_in[5], (const u16*)d_in[6], (const u16*)d_in[7], (const u16*)d_in[8],
                                        (u16*)d_out, (unsigned char*)d_ws);
}
```

```cpp
#include <hip/hip_runtime.h>
#include <stdint.h>

typedef unsigned int u32;
typedef unsigned long long u64;
typedef unsigned short u16;
typedef u32 v4u __attribute__((ext_vector_type(4)));
typedef u32 v2u __attribute__((ext_vector_type(2)));
typedef float f4 __attribute__((ext_vector_type(4)));
typedef float f2 __attribute__((ext_vector_type(2)));
typedef __bf16 bf8 __attribute__((ext_vector_type(8)));
typedef __bf16 bf2 __attribute__((ext_vector_type(2)));
typedef short s4v __attribute__((ext_vector_type(4)));
typedef __amdgpu_buffer_rsrc_t rsrc_t;
#define GAS __attribute__((address_space(1)))
#define LAS __attribute__((address_space(3)))
#define DI __device__ __forceinline__
#define AI __attribute__((always_inline))
#define RLX_AGENT __ATOMIC_RELAXED, __HIP_MEMORY_SCOPE_AGENT

constexpr int S_ = 2048, D_ = 448, F_ = 1792;
constexpr u32 KB = 1024;
constexpr u32 OFF_E = 0, OFF_TMO = 64;
constexpr u32 OFF_HINT = 16 * KB;
constexpr u32 OFF_KVT = 192 * KB;
constexpr u32 OFF_WO = 256 * KB;
constexpr u32 OFF_W1 = OFF_WO + 448 * KB;
constexpr u32 OFF_W2 = OFF_W1 + 392 * KB;
constexpr u32 OFF_W3 = OFF_W2 + 392 * KB;
constexpr u32 OFF_W4 = OFF_W3 + 1568 * KB;
constexpr u32 OFF_KVP = OFF_W4 + 1568 * KB;
constexpr u32 OFF_O = OFF_KVP + 2048 * KB;
constexpr u32 OFF_H = OFF_O + 2048 * KB;
constexpr u32 OFF_HS = OFF_H + 1792 * KB;
constexpr u32 OFF_X1 = OFF_HS + 1792 * KB;
constexpr u32 OFF_RS = OFF_X1 + 1792 * KB;
constexpr u32 WS_BYTES = OFF_RS + 256 * 56 * KB;
constexpr u32 OFF_DBG = WS_BYTES;
constexpr u32 L_X = 0, X_RS = 928, SLOT = 16 * X_RS;
constexpr u32 L_VB = 0, L_QA = 28 * KB, L_QX = 0;
constexpr u32 L_K = 64 * X_RS;
constexpr u32 L_PK = L_K, PK_RS = 136;
constexpr u32 L_V0 = L_PK + 128 * PK_RS, V_RS = 72;
constexpr u32 L_QB = L_K + 28 * KB;
constexpr u32 L_VA = L_K + 56 * KB;
constexpr u32 L_U = L_VA;
constexpr u32 L_KX = L_VA + 28 * KB;
constexpr u32 L_V1 = L_KX;
constexpr u32 L_ST = L_KX + 9 * KB;
constexpr u32 L_SF = L_ST + 512, L_DUMP = L_ST + 768;
constexpr u32 L_PHI = L_ST + 1024;
constexpr u32 L_BYTES = L_PHI + 4096;
static_assert(L_BYTES <= 163840 && L_V0 + 128 * V_RS <= L_QB, "");
constexpr u32 L_ACT = 0;
constexpr u32 L_HID = 56 * KB;
constexpr u32 SPIN_CAP = 1u << 15;
constexpr u32 OOB = 0x80000000u;
constexpr int SC1 = 16;
constexpr u32 CODE_BYTES = 32768;
#define XDMA dma_in
#define LDELAY 3
#define DLYA 1
#define DLYB 1
#define DLY0 1
#define DLY1 3
#define HS1 2

#define SB() __builtin_amdgcn_sched_barrier(0)
#define PIN(x) asm volatile("" : "+v"(x))
#define PINS(x) do { x = __builtin_amdgcn_readfirstlane(x); asm volatile("" : "+s"(x)); } while (0)
#define DIAG 0
#define STAMP(k) do { } while (0)
#define NRETRY(k) do { } while (0)
#define STAMP_BEGIN() do { } while (0)
#define STAMP_END() do { } while (0)

DI f4 mfma(v4u a, v4u b, f4 c) {
  return __builtin_amdgcn_mfma_f32_16x16x32_bf16(__builtin_bit_cast(bf8, a), __builtin_bit_cast(bf8, b), c, 0, 0, 0);
}
DI u32 pk2(float a, float b) { f2 v = {a, b}; return __builtin_bit_cast(u32, __builtin_convertvector(v, bf2)); }
DI float blo(u32 u) { return __builtin_bit_cast(float, u << 16); }
DI float bhi(u32 u) { return __builtin_bit_cast(float, u & 0xffff0000u); }
DI u32 f2u(float x) { return __builtin_bit_cast(u32, x); }
DI float u2f(u32 x) { return __builtin_bit_cast(float, x); }
DI u32 nosent(u32 x) { return x < 0xfffffffeu ? x : 0xfffffffeu; }
DI v4u nosent4(v4u v) { return v4u{nosent(v.x), nosent(v.y), nosent(v.z), nosent(v.w)}; }
DI u32 umax(u32 a, u32 b) { return a > b ? a : b; }
DI u32 max4(u32 m, v4u v) { m = umax(umax(m, v.x), v.y); return umax(umax(m, v.z), v.w); }
DI float fmx(float a, float b) { return __builtin_elementwise_maximum(a, b); }
DI float fmn(float a, float b) { return __builtin_elementwise_minimum(a, b); }
DI float rsum(float x) {
  auto a = __builtin_amdgcn_permlane16_swap(f2u(x), f2u(x), false, false); x = u2f(a[0]) + u2f(a[1]);
  auto c = __builtin_amdgcn_permlane32_swap(f2u(x), f2u(x), false, false); return u2f(c[0]) + u2f(c[1]);
}
DI void bar() { SB(); asm volatile("s_waitcnt lgkmcnt(0)\n\ts_barrier" ::: "memory"); SB(); }
#define VMW(n) asm volatile("s_waitcnt vmcnt(" #n ")" ::: "memory")
DI rsrc_t mkrs(const void* p, u32 bytes) { return __builtin_amdgcn_make_buffer_rsrc((void*)p, 0, (int)bytes, 0x00020000); }
DI v4u ldg(rsrc_t rs, u32 voff, u32 soff) { return __builtin_amdgcn_raw_buffer_load_b128(rs, (int)voff, (int)soff, SC1); }
DI void stg(rsrc_t rs, u32 off, v4u v) { __builtin_amdgcn_raw_buffer_store_b128(v, rs, (int)off, 0, SC1); }
template <int N, int DL>
DI void dma_in(rsrc_t rs, u32 voff, u32 soff, u32 ds, u32 lds) {
  asm volatile("s_mov_b32 m0, %4\n\ts_nop 0\n\tbuffer_load_dwordx4 %1, %2, %0 offen lds\n\t"
               ".rept %c5\n\ts_add_u32 m0, m0, %c6\n\ts_add_u32 %0, %0, %3\n\tbuffer_load_dwordx4 %1, %2, %0 offen lds\n\t.endr"
               : "+s"(soff) : "v"(voff), "s"(rs), "s"(ds), "s"(lds), "n"(N - 1), "n"(DL) : "memory", "scc");
}
template <int N, int DL>
DI void dma_nt(rsrc_t rs, u32 voff, u32 soff, u32 ds, u32 lds) {
  asm volatile("s_mov_b32 m0, %4\n\ts_nop 0\n\tbuffer_load_dwordx4 %1, %2, %0 offen nt lds\n\t"
               ".rept %c5\n\ts_add_u32 m0, m0, %c6\n\ts_add_u32 %0, %0, %3\n\tbuffer_load_dwordx4 %1, %2, %0 offen nt lds\n\t.endr"
               : "+s"(soff) : "v"(voff), "s"(rs), "s"(ds), "s"(lds), "n"(N - 1), "n"(DL) : "memory", "scc");
}
DI void touch(rsrc_t rs, u32 voff, u32 lds) {
  asm volatile("s_mov_b32 m0, %2\n\ts_nop 0\n\tbuffer_load_dword %0, %1, 0 offen sc1 lds" :: "v"(voff), "s"(rs), "s"(lds) : "memory");
}
template <int N>
DI void dma_ws(rsrc_t rs, u32 voff, u32 soff, u32 lds) {
#define L4_ "buffer_load_dwordx4 %1, %2, %0 offen sc1 lds\n\tbuffer_load_dwordx4 %1, %2, %0 offen offset:1024 sc1 lds\n\t" \
            "buffer_load_dwordx4 %1, %2, %0 offen offset:2048 sc1 lds\n\tbuffer_load_dwordx4 %1, %2, %0 offen offset:3072 sc1 lds\n\t"
  asm volatile("s_mov_b32 m0, %3\n\ts_nop 0\n\t" L4_
               ".rept %c4\n\ts_add_u32 m0, m0, 0x1000\n\ts_add_u32 %0, %0, 0x1000\n\t" L4_ ".endr"
               : "+s"(soff) : "v"(voff), "s"(rs), "s"(lds), "n"(N - 1) : "memory", "scc");
}
DI void poll(rsrc_t rs, u32 voff, u64 want) {
  u64 t; u32 cnt = SPIN_CAP;
  asm volatile("1:\n\tbuffer_load_dwordx2 %0, %2, %3, 0 offen sc1\n\ts_waitcnt vmcnt(0)\n\tv_cmp_ne_u64 vcc, %4, %0\n\ts_cbranch_vccz 2f\n\t"
               "s_sleep 1\n\ts_sub_u32 %1, %1, 1\n\ts_cbranch_scc0 1b\n2:"
               : "=&v"(t), "+s"(cnt) : "v"(voff), "s"(rs), "s"(want) : "vcc", "scc", "memory");
}
DI void poll4(rsrc_t rs, u32 voff, u64 want) {
  u64 t0, t1, t2, t3, m; u32 cnt = SPIN_CAP;
  const u32 v2 = voff + 8192;
  asm volatile("1:\n\tbuffer_load_dwordx2 %0, %6, %8, 0 offen sc1\n\tbuffer_load_dwordx2 %1, %6, %8, %10 offen sc1\n\t"
               "buffer_load_dwordx2 %2, %7, %8, 0 offen sc1\n\tbuffer_load_dwordx2 %3, %7, %8, %10 offen sc1\n\ts_waitcnt vmcnt(0)\n\t"
               "v_cmp_ne_u64 %4, %9, %0\n\tv_cmp_ne_u64 vcc, %9, %1\n\ts_or_b64 %4, %4, vcc\n\tv_cmp_ne_u64 vcc, %9, %2\n\ts_or_b64 %4, %4, vcc\n\t"
               "v_cmp_ne_u64 vcc, %9, %3\n\ts_or_b64 %4, %4, vcc\n\ts_cbranch_scc0 2f\n\t"
               "s_sleep 1\n\ts_sub_u32 %5, %5, 1\n\ts_cbranch_scc0 1b\n2:"
               : "=&v"(t0), "=&v"(t1), "=&v"(t2), "=&v"(t3), "=&s"(m), "+s"(cnt) : "v"(voff), "v"(v2), "s"(rs), "s"(want), "s"(4096) : "vcc", "scc", "memory");
}
constexpr float GC0 = -2.0f * 0.7978845608028654f * 1.4426950408889634f, GC1 = GC0 * 0.044715f;
DI float gelu(float x) {
  const float e = __builtin_amdgcn_exp2f(x * fmaf(x * x, GC1, GC0));
  return x * __builtin_amdgcn_rcpf(1.0f + e);
}

DI void hedgehog(f4 p0, f4 p1, float keep, v4u& pos, v4u& neg) {
  const float L2E = 1.4426950408889634f;
  float mx = fmx(fmx(fmx(fmx(p0[0], p0[1]), p0[2]), fmx(fmx(p0[3], p1[0]), p1[1])), fmx(p1[2], p1[3]));
  float nm = -fmn(fmn(fmn(fmn(p0[0], p0[1]), p0[2]), fmn(fmn(p0[3], p1[0]), p1[1])), fmn(p1[2], p1[3]));
  {
    auto a = __builtin_amdgcn_permlane16_swap(f2u(mx), f2u(nm), false, false);
    float t = fmx(u2f(a[0]), u2f(a[1]));
    auto c = __builtin_amdgcn_permlane32_swap(f2u(t), f2u(t), false, false);
    t = fmx(u2f(c[0]), u2f(c[1])) * -L2E;
    auto d = __builtin_amdgcn_permlane16_swap(f2u(t), f2u(t), false, false);
    mx = u2f(d[0]); nm = u2f(d[1]);
  }
  float a[8], c[8];
#pragma unroll
  for (int r = 0; r < 4; ++r) {
    a[r] = __builtin_amdgcn_exp2f(fmaf(p0[r], L2E, mx));
    c[r] = __builtin_amdgcn_exp2f(fmaf(p0[r], -L2E, nm));
    a[4 + r] = __builtin_amdgcn_exp2f(fmaf(p1[r], L2E, mx));
    c[4 + r] = __builtin_amdgcn_exp2f(fmaf(p1[r], -L2E, nm));
  }
  float sa = fmaf((a[4] + a[5]) + (a[6] + a[7]), keep, (a[0] + a[1]) + (a[2] + a[3]));
  float sc = fmaf((c[4] + c[5]) + (c[6] + c[7]), keep, (c[0] + c[1]) + (c[2] + c[3]));
  {
    auto x = __builtin_amdgcn_permlane16_swap(f2u(sa), f2u(sc), false, false);
    float t = u2f(x[0]) + u2f(x[1]);
    auto y = __builtin_amdgcn_permlane32_swap(f2u(t), f2u(t), false, false);
    t = __builtin_amdgcn_rcpf(u2f(y[0]) + u2f(y[1]));
    auto d = __builtin_amdgcn_permlane16_swap(f2u(t), f2u(t), false, false);
    sa = u2f(d[0]); sc = u2f(d[1]);
  }
  const float sa1 = sa * keep, sc1 = sc * keep;
  pos = v4u{pk2(a[0] * sa, a[1] * sa), pk2(a[2] * sa, a[3] * sa), pk2(a[4] * sa1, a[5] * sa1), pk2(a[6] * sa1, a[7] * sa1)};
  neg = v4u{pk2(c[0] * sc, c[1] * sc), pk2(c[2] * sc, c[3] * sc), pk2(c[4] * sc1, c[5] * sc1), pk2(c[6] * sc1, c[7] * sc1)};
}
DI v4u pack8(f4 a, f4 b) { return v4u{pk2(a[0], a[1]), pk2(a[2], a[3]), pk2(b[0], b[1]), pk2(b[2], b[3])}; }
DI v4u to_chunk(v4u p) {
  auto x = __builtin_amdgcn_permlane16_swap(p.x, p.z, false, false);
  auto y = __builtin_amdgcn_permlane16_swap(p.y, p.w, false, false);
  return v4u{x[0], y[0], x[1], y[1]};
}
DI float diag(f4 q, int p4) { return p4 == 0 ? q[0] : p4 == 1 ? q[1] : p4 == 2 ? q[2] : q[3]; }
DI v4u ld16(u32 addr) { return *(LAS v4u*)(uintptr_t)addr; }
DI void st16(u32 addr, v4u v) { *(LAS v4u*)(uintptr_t)addr = v; }
DI void st8(u32 addr, u32 a, u32 b) { *(LAS v2u*)(uintptr_t)addr = v2u{a, b}; }
DI f2 ldf2(u32 addr) { return *(LAS f2*)(uintptr_t)addr; }
DI v2u trread(u32 addr) {
  return __builtin_bit_cast(v2u, __builtin_amdgcn_ds_read_tr16_b64_v4i16((LAS s4v*)(uintptr_t)addr));
}
DI float dot2(u32 a, u32 sel, float c) {
  return __builtin_amdgcn_fdot2_f32_bf16(__builtin_bit_cast(bf2, a), __builtin_bit_cast(bf2, sel), c, false);
}

DI void gemm_tr(u32 t0, u32 t1, const v4u (&xa)[7], const v4u (&xb)[7], f4& a0, f4& a1, f4& b0, f4& b1) {
  constexpr int P = 3;
  PIN(t0); PIN(t1);
  v2u r[P][4];
#pragma unroll
  for (int k = 0; k < P; ++k) { r[k][0] = trread(t0 + k * 4096); r[k][1] = trread(t0 + k * 4096 + 512); r[k][2] = trread(t1 + k * 4096); r[k][3] = trread(t1 + k * 4096 + 512); }
  SB();
  a0 = f4{0, 0, 0, 0}; a1 = a0; b0 = a0; b1 = a0;
#pragma unroll
  for (int s = 0; s < 7; ++s) {
    const int k = s % P;
    const v4u f0 = {r[k][0].x, r[k][0].y, r[k][1].x, r[k][1].y}, f1 = {r[k][2].x, r[k][2].y, r[k][3].x, r[k][3].y};
    a1 = mfma(f1, xa[s], a1); b1 = mfma(f1, xb[s], b1);
    a0 = mfma(f0, xa[s], a0); b0 = mfma(f0, xb[s], b0);
    SB();
    if (s + P < 7) {
      r[k][0] = trread(t0 + (s + P) * 4096); r[k][1] = trread(t0 + (s + P) * 4096 + 512);
      r[k][2] = trread(t1 + (s + P) * 4096); r[k][3] = trread(t1 + (s + P) * 4096 + 512);
      SB();
    }
  }
}
template <int NS, class PF>
DI void gemm_alds(u32 p, const v4u (&wf)[NS], f4& a0, f4& a1, PF pf) {
  constexpr int P = 4;
  v4u b0[P], b1[P];
#pragma unroll
  for (int k = 0; k < P; ++k) { b0[k] = ld16(p + 2 * k * KB); b1[k] = ld16(p + (2 * k + 1) * KB); }
  SB();
#pragma unroll
  for (int s = 0; s < NS; ++s) {
    a1 = mfma(wf[s], b1[s % P], a1);
    a0 = mfma(wf[s], b0[s % P], a0);
    SB();
    if (s + P < NS) { b0[s % P] = ld16(p + 2 * (s + P) * KB); b1[s % P] = ld16(p + (2 * (s + P) + 1) * KB); }
    pf(s);
    SB();
  }
}
template <int NK, int EXTRA, int NT>
DI u32 g_check(u32 p, v4u (&fr)[NK], f4 (&sm)[4]) {
  const v4u ones = {0x3f803f80u, 0x3f803f80u, 0x3f803f80u, 0x3f803f80u};
  u32 m = 0;
#pragma unroll
  for (int j = 0; j <= NK / 4; ++j) {
    if (j < NK / 4) {
      asm volatile("s_waitcnt vmcnt(%0)" :: "n"(EXTRA + NK - 4 - 4 * j) : "memory");
#pragma unroll
      for (int e = 0; e < 4; ++e) fr[4 * j + e] = ld16(p + (4 * j + e) * KB);
      SB();
    }
    if (j >= 1) {
#pragma unroll
      for (int e = 3; e >= 0; --e) {
        const int k = 4 * (j - 1) + e;
        m = max4(m, fr[k]);
        if (NT) sm[k % (NT ? NT : 1)] = mfma(ones, fr[k], sm[k % (NT ? NT : 1)]);
      }
      SB();
    }
  }
  return m;
}
#define RETRY_LOOP _Pragma("clang loop unroll(disable)") for (u32 sp = 0;; ++sp)
#define RETRY_END(k) NRETRY(k); if (sp > SPIN_CAP) break; asm volatile("s_waitcnt lgkmcnt(0)" ::: "memory")

__global__ void __launch_bounds__(512, 2)
fused_kernel(const u16* __restrict__ x, const u16* __restrict__ wqkv, const u16* __restrict__ wphi, const u16* __restrict__ wo,
             const u16* __restrict__ w1, const u16* __restrict__ w2, const u16* __restrict__ w3, const u16* __restrict__ w4,
             u16* __restrict__ out, unsigned char* __restrict__ ws) {
  __shared__ __attribute__((aligned(1024))) unsigned char lds_raw[L_BYTES];
  const u32 L = (u32)(uintptr_t)(LAS unsigned char*)lds_raw;
  const int tid = threadIdx.x, lane = tid & 63, w = __builtin_amdgcn_readfirstlane(tid >> 6), g = lane >> 4, i16 = lane & 15;
  const int b = blockIdx.x, xcd = b & 7, ii = b >> 3;
  const int zg = xcd * 8 + (ii >> 2), js = ii & 3;
  const int yg = xcd * 4 + (ii >> 3), mi = ii & 7, ms = (mi + (ii >> 3)) & 7;
  const int th = (ii >> 2) & 1;
  const u32 l16 = lane * 16;
  STAMP_BEGIN();
  const u64 pr0 = __builtin_amdgcn_s_memrealtime();
  const int u = w >> 2, hh = w & 1, T = (w & 2) + (hh ^ u), head = mi * 2 + hh;
  asm volatile("" :: "s"(x), "s"(wqkv), "s"(wphi), "s"(out), "s"(ws));
  const rsrc_t rx = mkrs(x, S_ * D_ * 2);
  const rsrc_t rq = mkrs(wqkv, D_ * 3 * D_ * 2);
  const u32 wc = (lane & 7) ^ ((lane >> 3) & 2) ^ ((w & 1) << 2);
  u32 wv = (((w & 3) * 8 + (lane >> 3)) * 2688 + mi * 112 + wc * 16) | (wc < 7 ? 0 : OOB);
  u32 wl = L + (w & 3) * KB;
  if (u == 0) {
    __builtin_amdgcn_s_setprio(2);
    if (lane < 56) {
      const u32 r0 = (mi & 1) * 8;
      XDMA<8, X_RS>(rx, l16, (yg * 64 + w * 16 + r0) * 896, 896, L + L_X + w * SLOT + r0 * X_RS);
      XDMA<8, X_RS>(rx, l16, (yg * 64 + w * 16 + (r0 ^ 8)) * 896, 896, L + L_X + w * SLOT + (r0 ^ 8) * X_RS);
    }
  } else {
    if (w == 4) {
      u64 pc; asm volatile("s_getpc_b64 %0" : "=s"(pc));
      touch(mkrs((const void*)(pc & ~255ull), CODE_BYTES), (ii + 32 * lane) * 128, L + L_DUMP);
    } else if (w == 5) {
      touch(rq, lane < 37 ? (b * 37 + lane) * 128 : OOB, L + L_DUMP);
    } else if (w == 6) {
      touch(mkrs(ws, WS_BYTES), lane < 4 ? OFF_HINT + (b * 4 + lane) * 128 : OOB, L + L_DUMP);
    }
    __builtin_amdgcn_s_sleep(LDELAY);
    dma_in<1, 0>(mkrs(wphi, 16 * 28 * 28 * 2), l16, mi * 3136 + (w - 4) * KB, 0, L + L_PHI + (w - 4) * KB);
    dma_in<14, 4 * KB>(rq, wv, 896, 32 * 2688, wl + L_K);
  }
  SB();
  STAMP(24);
  const u32 epoch = *(const __attribute__((address_space(4))) u32*)(uintptr_t)(ws + OFF_E);
  const u64 tag = (u64)epoch | (0x9e3779b9ull << 32);
  const rsrc_t rws = mkrs(ws, WS_BYTES);
  const v4u SENT = {0xffffffffu, 0xffffffffu, 0xffffffffu, 0xffffffffu};
  const v4u ones = {0x3f803f80u, 0x3f803f80u, 0x3f803f80u, 0x3f803f80u};
  const f4 z4 = {0, 0, 0, 0};
  const int q4 = i16 >> 2, p4 = i16 & 3;
  const bool padg = g == 3;
  u32 vz = lane == 0 ? 0 : OOB;
  PIN(vz); PIN(wv); PINS(wl);
  auto post = [&](u32 off) AI { __builtin_amdgcn_raw_buffer_store_b64(v2u{epoch, 0x9e3779b9u}, rws, (int)vz, (int)off, SC1); };
  const int n = js * 7 + (w < 7 ? w : 0);
  const u32 unit = u == 0 ? 4 * b + w : w != 4 ? 2000 : ((mi + 1) & 6) == 0 ? 1024 + yg * 2 + (mi & 1) : mi == 1 ? 1088 + yg : 2000;
  const bool hasu = unit < 1092;
  u32 u_ts = 0, u_dst = 0, u_lo = 0, u_hi = 0, u_keep = 0;
  v4u u_raw[4];
  if (hasu) {
    const u32 sec = ((unit + 1936) >> 11) + ((unit + 1838) >> 11) + ((unit + 1740) >> 11) + ((unit + 1348) >> 11);
    const u64 base = ((const __attribute__((address_space(4))) u64*)__builtin_amdgcn_kernarg_segment_ptr())[3 + sec];
    const u32 first = (u32)(0x2bc1340d2070000ull >> (sec * 12)) & 0xfffu, uu = unit - first;
    const u32 sh3 = sec == 3 ? 2 : 0, prm = (sec & 3) == 0 ? 2 : 0;
    const u32 u7 = (uu * 9363) >> 16, st = u7 >> sh3, cb = uu - ((st * 7) << sh3), m = sec == 4 ? (u7 * 9363) >> 16 : 0;
    const rsrc_t rs = mkrs((const void*)base, sec >= 3 ? D_ * F_ * 2 : D_ * D_ * 2);
    const u32 vo = (st * (sec == 0 ? 28 : 32) + (lane >> 3)) * (896 << sh3) + cb * 128 + (lane & 7) * 16;
#pragma unroll
    for (int d = 0; d < 4; ++d)
      asm volatile("buffer_load_dwordx4 %0, %1, %2, 0 offen nt" : "=v"(u_raw[d]) : "v"(vo + ((d * 8 * 896) << sh3)), "s"(rs) : "memory");
    u_ts = ((0x7eee0u >> (sec * 4)) & 15) * KB + (sec == 0 ? 16 * KB : 0);
    u_dst = l16 + OFF_WO + first * 4 * KB + cb * 4 * u_ts + (m * 189 + st) * KB;
    u_lo = L + L_U + w * 4 * KB + ((g << (3 - (prm >> 1))) + q4) * 128 + p4 * 8; u_hi = u_lo + (512 << prm);
    u_keep = (sec == 0 && padg) ? 0u : 0xffffffffu;
  }
  u32 u_sw = L + L_U + w * 4 * KB + l16;
  PIN(u_sw); PINS(u_ts); PIN(u_dst); PIN(u_lo); PIN(u_hi); PIN(u_keep);
  if (u == 1) dma_in<7, 4 * KB>(rq, wv, 1792, 32 * 2688, wl + L_VA);
  SB();
  STAMP(46);
  u32 kvp_off = OFF_KVP + ((mi * 8 + w) * 4 + g) * 8 * KB + yg * 256 + i16 * 16;
  u32 kvt_off = g == 0 ? OFF_KVT + (mi * 32 + yg) * 256 + i16 * 16 : OOB;
  u32 o_off = l16 + OFF_O + (((yg * 2 + (T >> 1)) * 16 + head) * 2 + (T & 1)) * KB;
  u32 sel_lo = 0x00003f80u, sel_hi = 0x3f800000u;
  float keep = padg ? 0.f : 1.f;
  const u32 trx = (((q4 >> 1) & 1) | ((g & 1) << 1)) * 32;
  u32 tr0 = L + (8 * g + q4) * 128 + ((56 * hh + 8 * p4) ^ trx);
  u32 tr1 = L + (8 * g + q4) * 128 + ((56 * hh + 32 + 8 * (p4 == 3 ? 2 : p4)) ^ trx);
  u32 kx_w = L + L_KX + (w ^ 4) * KB + l16, kx_r = L + L_KX + w * KB + l16;
  u32 pk_w = L + L_PK + (hh * 64 + T * 16 + i16) * PK_RS + g * 8;
  u32 va_w = L + (u ? L_V1 : L_V0) + (hh * 64 + T * 16 + i16) * V_RS + g * 8, vb_w = L + (u ? L_V1 : L_V0) + (hh * 64 + (T ^ 1) * 16 + i16) * V_RS + g * 8;
  u32 kv_a = L + L_PK + (u * 64 + g * 8 + q4) * PK_RS + ((w >> 1) & 1) * 64 + p4 * 8;
  u32 kv_b = L + L_V0 + (u * 64 + g * 8 + q4) * V_RS + (w & 1) * 32 + p4 * 8, kv_c = kv_b + (L_V1 - L_V0);
  u32 ph_a = L + L_PHI + hh * 1568 + (4 * g + q4) * 56 + p4 * 8;
  u32 xo_r = L + L_X + (T ^ 1) * SLOT + u * 7 * KB + l16;
  u32 h32 = OFF_HINT + (mi * 32 + (lane & 31)) * 64;
  const u32 cko = (2 * (n & 1) + (g >> 1)) * 256;
  u32 h_off = ((zg * 14 + (n >> 1)) * 2 + (g & 1)) * KB + cko + i16 * 16 + OFF_H;
  PIN(h_off);
  PIN(kvp_off); PIN(kvt_off); PIN(o_off); PIN(sel_lo); PIN(sel_hi); PIN(keep); PIN(tr0); PIN(tr1); PIN(kx_w); PIN(kx_r);
  PIN(pk_w); PIN(va_w); PIN(vb_w); PIN(kv_a); PIN(kv_b); PIN(kv_c); PIN(ph_a); PIN(xo_r); PIN(h32);
  SB();
  v4u xa[7], xb[7];
  if (u == 0) {
    u32 xp = L + L_X + w * SLOT + i16 * X_RS + g * 16, xw = L + L_X + w * SLOT + l16;
    PIN(xp); PIN(xw);
    VMW(4);
    STAMP(25);
#pragma unroll
    for (int s = 0; s < 7; ++s) xb[s] = ld16(xp + 448 + s * 64);
#pragma unroll
    for (int s = 0; s < 7; ++s) xa[s] = ld16(xp + s * 64);
    SB();
    f4 sq = z4, sm = z4;
#pragma unroll
    for (int s = 0; s < 7; ++s) { sq = mfma(xb[s], xb[s], sq); sm = mfma(ones, xb[s], sm); }
#pragma unroll
    for (int s = 0; s < 7; ++s) { sq = mfma(xa[s], xa[s], sq); sm = mfma(ones, xa[s], sm); }
    const float mu = sm[0] * (1.0f / D_);
    const float var = fmx(fmaf(-mu, mu, rsum(g == q4 ? diag(sq, p4) : 0.f) * (1.0f / D_)), 0.f);
    const float rs = __builtin_amdgcn_rsqf(var + 1e-5f), nb = -mu * rs;
    STAMP(26);
#pragma unroll
    for (int s = 0; s < 7; ++s) {
#pragma unroll
      for (int e = 0; e < 4; ++e) xb[s][e] = pk2(fmaf(blo(xb[s][e]), rs, nb), fmaf(bhi(xb[s][e]), rs, nb));
      st16(xw + (7 + s) * KB, xb[s]);
      SB();
    }
#pragma unroll
    for (int s = 0; s < 7; ++s) {
#pragma unroll
      for (int e = 0; e < 4; ++e) xa[s][e] = pk2(fmaf(blo(xa[s][e]), rs, nb), fmaf(bhi(xa[s][e]), rs, nb));
      st16(xw + s * KB, xa[s]);
      SB();
    }
    __builtin_amdgcn_s_setprio(0);
  } else {
    stg(rws, kvp_off, SENT); stg(rws, kvp_off - 4 * 32 * KB, SENT);
    stg(rws, o_off, SENT); stg(rws, o_off ^ KB, SENT);
    const int n4 = js * 7 + w - 4;
    const u32 h4 = ((zg * 14 + (n4 >> 1)) * 2 + (g & 1)) * KB + (2 * (n4 & 1) + (g >> 1)) * 256 + i16 * 16 + OFF_H;
    stg(rws, h4, SENT); stg(rws, w == 7 ? kvt_off : h4 + 4 * KB, SENT);
  }
  STAMP(27);
  bar();
  STAMP(18);
#pragma unroll
  for (int s = 0; s < 7; ++s) xb[s] = ld16(xo_r + s * KB);
  if (u == 1) {
    const u32 xm = L + L_X + T * SLOT + 7 * KB + l16;
#pragma unroll
    for (int s = 0; s < 7; ++s) xa[s] = ld16(xm + s * KB);
    VMW(13);
  }
  bar();
  STAMP(1);
  v4u wo_[16], w1_[14], w2_[14], w3f[28], w4f[28];
  u32 lw = w < 7 ? l16 : OOB;
  PIN(lw);
  auto pf = [&](int k) AI {
    if (k < 16) wo_[k] = ldg(rws, lw + (k & 3) * KB, OFF_WO + (n * 16 + (k & ~3)) * KB);
    else if (k < 30) w1_[k - 16] = ldg(rws, lw + (k & 3) * KB, OFF_W1 + (n * 14 + ((k - 16) & ~3)) * KB);
    else if (k < 44) w2_[k - 30] = ldg(rws, lw + ((k - 30) & 3) * KB, OFF_W2 + (n * 14 + ((k - 30) & ~3)) * KB);
    else if (k < 72) { const int t = (k - 44) & 1, st = (k - 44) >> 1;
      w3f[t * 14 + st] = ldg(rws, lw + (st & 3) * KB, OFF_W3 + ((ms * 14 + w * 2 + t) * 14 + (st & ~3)) * KB); }
    else if (k < 100) { const int sl = (k - 72) / 7, c = (k - 72) % 7;
      w4f[k - 72] = ldg(rws, lw + (c & 3) * KB, OFF_W4 + ((ms * 28 + ((js + 1 + sl) & 3) * 7 + w) * 7 + (c & ~3)) * KB); }
  };
#define PFR(lo, hi) do { _Pragma("unroll") for (int k_ = (lo); k_ < (hi); ++k_) pf(k_); asm volatile("" ::: "memory"); SB(); } while (0)
#define PFS(base, cnt) [&](int s_) AI { if ((s_ & 1) == 0 && (s_ >> 1) < (cnt)) pf((base) + (s_ >> 1)); }
  v4u phA, phB;
  {
    if (u == 1) dma_in<7, 4 * KB>(rq, wv, 1792 + 224 * 2688, 32 * 2688, wl + L_VB);
    STAMP(20);
    f4 a0, a1, b0, b1;
    gemm_tr(tr0 + L_K + u * 28 * KB, tr1 + L_K + u * 28 * KB, xa, xb, a0, a1, b0, b1);
    {
      const u32 a = ph_a, c = a + (p4 == 3 ? 24 : 32);
      const v2u l0 = trread(a), h0 = trread(a + 16 * 56), l1 = trread(c), h1 = trread(c + 16 * 56);
      st16(kx_w, pack8(b0, b1));
      phA = v4u{l0.x, l0.y, padg ? 0u : h0.x, padg ? 0u : h0.y};
      phB = v4u{l1.x, l1.y, padg ? 0u : h1.x, padg ? 0u : h1.y};
      PIN(phA); PIN(phB);
    }
    const v4u ka = pack8(a0, a1);
    f4 p0 = mfma(phA, ka, z4), p1 = mfma(phB, ka, z4);
    STAMP(2);
    if (u == 0) dma_in<7, 4 * KB>(rq, wv, 0, 32 * 2688, wl + L_QA);
    else VMW(7);
    STAMP(19);
    bar();
    STAMP(3);
    const v4u kb = ld16(kx_r);
    if (u == 0) dma_in<7, 4 * KB>(rq, wv, 224 * 2688, 32 * 2688, wl + L_QB);
    SB();
    p0 = mfma(phA, kb, p0); p1 = mfma(phB, kb, p1);
    PIN(p0); PIN(p1);
    const u32 vbase = u ? L_VB : L_VA;
#pragma clang loop unroll(disable)
    for (int ph = 0; ph < 2; ++ph) {
      if (ph == u) {
        gemm_tr(tr0 + vbase, tr1 + vbase, xa, xb, a0, a1, b0, b1);
        if (padg) { a1[0] = (float)(1 - u); b1[0] = a1[0]; }
        const v4u va = pack8(a0, a1), vb = pack8(b0, b1);
        st8(va_w, va.x, va.y); st8(va_w + 32, va.z, va.w);
        st8(vb_w, vb.x, vb.y); st8(vb_w + 32, vb.z, vb.w);
      } else {
        v4u pos, neg;
        hedgehog(p0, p1, keep, pos, neg);
        st8(pk_w, pos.x, pos.y); st8(pk_w + 32, pos.z, pos.w); st8(pk_w + 64, neg.x, neg.y); st8(pk_w + 96, neg.z, neg.w);
      }
      if (ph == 0) {
        STAMP(21);
        if (u == 1) VMW(0);
        bar();
        STAMP(22);
    if (hasu) {
      VMW(14);
#pragma unroll
      for (int d = 0; d < 4; ++d) st16(u_sw + d * KB, u_raw[d]);
      asm volatile("s_waitcnt lgkmcnt(0)" ::: "memory");
      v2u lo[4], hi[4];
#pragma unroll
      for (int t = 0; t < 4; ++t) { lo[t] = trread(u_lo + t * 32); hi[t] = trread(u_hi + t * 32); }
      SB();
#pragma unroll
      for (int t = 0; t < 4; ++t) stg(rws, u_dst + t * u_ts, v4u{lo[t].x, lo[t].y, hi[t].x & u_keep, hi[t].y & u_keep});
    }
    SB();

      }
    }
    if (hasu) VMW(0);
  }
  STAMP(23);
  bar();
  STAMP(4);
  if (w == 7) post(OFF_HINT + (5 * 256 + mi * 32 + yg) * 64);
  {
    v2u a0[2], a1[2], b0[2], b1[2], d0[4], d1[4];
#pragma unroll
    for (int s = 0; s < 2; ++s) {
      a0[s] = trread(kv_a + s * 32 * PK_RS); a1[s] = trread(kv_a + (s * 32 + 4) * PK_RS);
      d0[s] = trread(kv_b + s * 32 * V_RS); d1[s] = trread(kv_b + (s * 32 + 4) * V_RS);
      b0[s] = trread(kv_a + s * 32 * PK_RS + 32); b1[s] = trread(kv_a + (s * 32 + 4) * PK_RS + 32);
    }
#pragma unroll
    for (int s = 0; s < 2; ++s) { d0[2 + s] = trread(kv_c + s * 32 * V_RS); d1[2 + s] = trread(kv_c + (s * 32 + 4) * V_RS); }
    SB();
    f4 c0 = z4, c1 = z4;
#pragma unroll
    for (int s = 0; s < 4; ++s) {
      const v4u vf = {d0[s].x, d0[s].y, d1[s].x, d1[s].y};
      c0 = mfma(v4u{a0[s & 1].x, a0[s & 1].y, a1[s & 1].x, a1[s & 1].y}, vf, c0);
      c1 = mfma(v4u{b0[s & 1].x, b0[s & 1].y, b1[s & 1].x, b1[s & 1].y}, vf, c1);
    }
    stg(rws, kvp_off, nosent4(pack8(c0, c1)));
    SB();
  }
  STAMP(28);
  v2u xr[2];
  {
#pragma unroll
    for (int t = 0; t < 2; ++t)
      xr[t] = __builtin_amdgcn_raw_buffer_load_b64(rx, (int)((zg * 32 + t * 16 + i16) * 896 + (n * 16 + g * 4) * 2), 0, 0);
    SB();
    f4 a0, a1, b0, b1;
    const u32 qbase = u ? L_QB : L_QA;
    gemm_tr(tr0 + qbase, tr1 + qbase, xa, xb, a0, a1, b0, b1);
    st16(kx_w - (L_KX - L_QX), pack8(b0, b1));
    const v4u qa = pack8(a0, a1);
    f4 p0 = mfma(phA, qa, z4), p1 = mfma(phB, qa, z4);
    PIN(p0); PIN(p1);
    STAMP(5);
    v4u kp[8];
    if (w == 7) {
      poll4(rws, OFF_HINT + 5 * 256 * 64 + lane * 64, tag);
      __builtin_amdgcn_s_sleep(DLYA);
#pragma unroll
      for (int k = 0; k < 8; ++k) kp[k] = ldg(rws, l16 + (k & 3) * KB, OFF_KVP + ((mi * 32 + yg) * 8 + (k & 4)) * KB);
    }
    STAMP(6);
    bar();
    STAMP(7);
    const v4u qb = ld16(kx_r - (L_KX - L_QX));
    p0 = mfma(phA, qb, p0); p1 = mfma(phB, qb, p1);
    v4u qpos, qneg;
    if (w == 7) {
      float lo[4], hi[4];
      RETRY_LOOP {
#pragma unroll
        for (int e = 0; e < 4; ++e) { lo[e] = 0.f; hi[e] = 0.f; }
#pragma unroll
        for (int k = 0; k < 8; ++k) {
#pragma unroll
          for (int e = 0; e < 4; ++e) { lo[e] = dot2(kp[k][e], sel_lo, lo[e]); hi[e] = dot2(kp[k][e], sel_hi, hi[e]); }
          SB();
        }
        const float t = (lo[0] + lo[1]) + (lo[2] + lo[3]);
        if (__all(t == t)) break;
        u32 m = 0;
#pragma unroll
        for (int k = 0; k < 8; ++k) m = max4(m, kp[k]);
        if (__all(m != 0xffffffffu)) break;
        RETRY_END(1);
#pragma unroll
        for (int k = 0; k < 8; ++k) kp[k] = ldg(rws, l16 + (k & 3) * KB, OFF_KVP + ((mi * 32 + yg) * 8 + (k & 4)) * KB);
        SB();
      }
      post(OFF_HINT + (6 * 256 + mi * 32 + yg) * 64);
      v4u sum;
#pragma unroll
      for (int e = 0; e < 4; ++e) {
        auto a = __builtin_amdgcn_permlane16_swap(f2u(lo[e]), f2u(hi[e]), false, false);
        const float t = u2f(a[0]) + u2f(a[1]);
        auto c = __builtin_amdgcn_permlane32_swap(f2u(t), f2u(t), false, false);
        const float v = u2f(c[0]) + u2f(c[1]);
        auto d = __builtin_amdgcn_permlane16_swap(f2u(v), f2u(v), false, false);
        sum[e] = nosent(pk2(u2f(d[0]), u2f(d[1])));
      }
      stg(rws, kvt_off, sum);
      SB();
      STAMP(30);
    } else {
      PFR(0, 8);
    }
    hedgehog(p0, p1, keep, qpos, qneg);
    PIN(qpos); PIN(qneg);
    STAMP(31);
    if (w == 7) { poll(rws, h32 + 6 * 256 * 64, tag); __builtin_amdgcn_s_sleep(DLYB); }
    else VMW(0);
    STAMP(45);
    bar();
    v4u kf[4];
#pragma unroll
    for (int k = 0; k < 4; ++k) kf[k] = ldg(rws, l16 + k * KB, OFF_KVT + head * 4 * KB);
    SB();
    if (w == 2) post(OFF_HINT + (0 * 256 + yg * 8 + mi) * 64);
    SB();
    f4 n0, n1;
    RETRY_LOOP {
      n1 = mfma(kf[1], qpos, z4); n0 = mfma(kf[0], qpos, z4);
      n1 = mfma(kf[3], qneg, n1); n0 = mfma(kf[2], qneg, n0);
      u32 m = 0;
#pragma unroll
      for (int k = 0; k < 4; ++k) m = max4(m, kf[k]);
      if (__all(m != 0xffffffffu)) break;
      RETRY_END(2);
#pragma unroll
      for (int k = 0; k < 4; ++k) kf[k] = ldg(rws, l16 + k * KB, OFF_KVT + head * 4 * KB);
    }
    const float rd = __builtin_amdgcn_rcpf(rsum(padg ? n1[0] : 0.f) + 1e-6f);
    n0 *= rd; n1 *= rd;
    v4u ov = nosent4(pack8(n0, n1));
    if (padg) { ov.z = 0; ov.w = 0; }
    stg(rws, o_off, ov);
  }
  bar();
  STAMP(8);

  if (w == 7) {
    __builtin_amdgcn_s_setprio(3);
    const u32 P = L + L_ACT + l16;
    const u32 hme = OFF_HINT + (zg * 4 + js) * 64;
    u32 h8 = OFF_HINT + (yg * 8 + (lane & 7)) * 64;
    f4 sm[4];
    poll(rws, h8, tag);
    __builtin_amdgcn_s_sleep(DLY0);
    STAMP(9);
    {
      v4u fr[32];
      RETRY_LOOP {
        dma_ws<8>(rws, l16, OFF_O + zg * 32 * KB, L + L_ACT);
        if (sp == 0) bar();
        if (__all(g_check<32, 0, 0>(P, fr, sm) != 0xffffffffu)) break;
        RETRY_END(3);
      }
    }
    u32 hA = OFF_HINT + (1 * 256 + zg * 4 + (lane & 3)) * 64;
    u32 hN = OFF_HINT + (7 * 256 + (((js & 1) ? zg + 63 : zg + 1) & 63) * 4 + js) * 64;
    u32 hB = OFF_HINT + (2 * 256 + ((lane & 4) ? ((lane & 1) ? zg + 1 : zg + 63) & 63 : zg) * 4 + (lane & 3)) * 64;
    u32 st_w = L + L_ST + i16 * 8;
    bar();
    STAMP(10);
    post(hme + 1 * 256 * 64);
    PIN(hA); PIN(hB); PIN(hN); PIN(st_w);
    *(LAS u32*)(uintptr_t)(L + L_SF) = 0;
    poll(rws, hA, tag);
    __builtin_amdgcn_s_sleep(HS1);
    STAMP(32);
    bar();
    __builtin_amdgcn_s_sleep(DLY1);
    STAMP(11);
    {
      v4u fr[28];
      RETRY_LOOP {
        dma_ws<7>(rws, l16, OFF_H + zg * 28 * KB, L + L_ACT);
        if (sp == 0) bar();
        sm[0] = z4; sm[1] = z4;
        if (__all(g_check<28, 0, 2>(P, fr, sm) != 0xffffffffu)) break;
        RETRY_END(4);
      }
      bar();
      STAMP(12);
      v2u nm = __builtin_amdgcn_raw_buffer_load_b64(rws, (int)hN, 0, SC1);
      post(hme + 2 * 256 * 64);
      SB();
      f4 q0 = z4, q1 = z4;
#pragma unroll
      for (int s = 0; s < 14; ++s) { q0 = mfma(fr[2 * s], fr[2 * s], q0); q1 = mfma(fr[2 * s + 1], fr[2 * s + 1], q1); }
      const float m0 = sm[0][0] * (1.0f / D_), m1 = sm[1][0] * (1.0f / D_);
      const float v0 = fmx(fmaf(-m0, m0, diag(q0, p4) * (1.0f / D_)), 0.f), v1 = fmx(fmaf(-m1, m1, diag(q1, p4) * (1.0f / D_)), 0.f);
      if (g == q4) { st8(st_w, f2u(m0), f2u(v0)); st8(st_w + 128, f2u(m1), f2u(v1)); }
      PIN(nm);
      if (!__all(nm.x == epoch && nm.y == 0x9e3779b9u)) poll(rws, hN, tag);
    }
    STAMP(33);
    bar();
    poll(rws, hB, tag);
    STAMP(34);
    bar();
    __builtin_amdgcn_s_sleep(DLY1);
    STAMP(13);
    {
      v4u fr[28];
      RETRY_LOOP {
        dma_ws<7>(rws, l16, OFF_HS + zg * 28 * KB, L + L_ACT);
        if (sp == 0) bar();
        if (__all(g_check<28, 0, 0>(P, fr, sm) != 0xffffffffu)) break;
        RETRY_END(5);
      }
    }
    bar();
    STAMP(14);
    post(hme + 3 * 256 * 64);
    poll(rws, h8 + 3 * 256 * 64, tag);
    __builtin_amdgcn_s_sleep(HS1);
    STAMP(35);
    bar();
    __builtin_amdgcn_s_sleep(DLY1);
    STAMP(15);
    {
      v4u fr[28];
      f4 sq[4] = {z4, z4, z4, z4};
      RETRY_LOOP {
        dma_ws<14>(rws, l16, OFF_X1 + yg * 56 * KB, L + L_ACT);
        if (sp == 0) bar();
#pragma unroll
        for (int t = 0; t < 4; ++t) sm[t] = z4;
        if (__all(g_check<28, 28, 4>(P, fr, sm) != 0xffffffffu)) break;
        RETRY_END(6);
        VMW(0);
      }
      bar();
      STAMP(16);
      RETRY_LOOP {
        v4u fs[28];
        if (__all(g_check<28, 0, 0>(P + 28 * KB, fs, sm) != 0xffffffffu)) break;
        RETRY_END(7);
        dma_ws<7>(rws, l16, OFF_X1 + (yg * 56 + 28) * KB, L + L_ACT + 28 * KB);
      }
      bar();
      STAMP(36);
#pragma unroll
      for (int k = 0; k < 28; k += 4) {
#pragma unroll
        for (int t = 0; t < 4; ++t) sq[t] = mfma(fr[k + t], fr[k + t], sq[t]);
        SB();
#pragma unroll
        for (int t = 0; t < 4; ++t) fr[k + t] = ld16(P + (28 + k + t) * KB);
        SB();
      }
#pragma unroll
      for (int k = 0; k < 28; ++k) { sq[k & 3] = mfma(fr[k], fr[k], sq[k & 3]); sm[k & 3] = mfma(ones, fr[k], sm[k & 3]); }
#pragma unroll
      for (int t = 0; t < 4; ++t) {
        const float m0 = sm[t][0] * (1.0f / D_);
        const float r0 = __builtin_amdgcn_rsqf(fmx(fmaf(-m0, m0, diag(sq[t], p4) * (1.0f / D_)), 0.f) + 1e-5f);
        if (g == q4) st8(st_w + t * 128, f2u(-m0), f2u(r0));
      }
      asm volatile("s_waitcnt lgkmcnt(0)" ::: "memory");
      *(volatile LAS u32*)(uintptr_t)(L + L_SF) = 1;
    }
    STAMP(37);
    bar();
    bar();
    post(hme + 4 * 256 * 64);
    poll(rws, h8 + 4 * 256 * 64, tag);
    STAMP(17);
    bar();
    if (b == 0 && lane == 0) __hip_atomic_store((GAS u32*)(ws + OFF_E), epoch + 1, RLX_AGENT);
    STAMP_END();
    return;
  }
  f4 hold0, hold1;
  const u32 A = L + L_ACT + l16;
  const int tk = zg * 32 + (g & 1) * 16 + i16 + ((js & 1) ? -1 - (js >> 1) : 1 + (js >> 1));
  u32 hs_off = ((((tk & (S_ - 1)) >> 5) * 14 + (n >> 1)) * 2 + ((tk >> 4) & 1)) * KB + cko + (tk & 15) * 16 + OFF_HS;
  u32 hs_keep = (tk < 0 || tk >= S_) ? 0u : 0xffffffffu;
  u32 x1_off = ((yg * 14 + (n >> 1)) * 4 + th * 2 + (g & 1)) * KB + cko + i16 * 16 + OFF_X1;
  u32 st_r = L + L_ST + i16 * 8;
  PFR(8, 11);
  PIN(hs_off); PIN(hs_keep); PIN(x1_off); PIN(st_r);
  f4 a0 = {blo(xr[0].x), bhi(xr[0].x), blo(xr[0].y), bhi(xr[0].y)};
  f4 a1 = {blo(xr[1].x), bhi(xr[1].x), blo(xr[1].y), bhi(xr[1].y)};
  PIN(a0); PIN(a1);
  VMW(0);
  bar();
  stg(rws, h_off + (OFF_HS - OFF_H), SENT);
  PFR(11, 16);
  bar();
  STAMP(32);
  {
    gemm_alds<16>(A, wo_, a0, a1, PFS(16, 8));
    STAMP(33);
    const v4u p = nosent4(pack8(a0, a1));
    stg(rws, h_off, to_chunk(p));
    SB();
    hold0 = f4{blo(p.x), bhi(p.x), blo(p.y), bhi(p.y)};
    hold1 = f4{blo(p.z), bhi(p.z), blo(p.w), bhi(p.w)};
  }
  bar();
  STAMP(9);
  f4 xb0, xb1;
  {
    PFR(24, 27);
    f4 cs = z4;
#pragma unroll
    for (int s = 0; s < 11; ++s) cs = mfma(w1_[s], ones, cs);
    PIN(cs);
    VMW(0);
    bar();
    if (w == 0) post(OFF_HINT + (7 * 256 + zg * 4 + js) * 64);
    stg(rws, x1_off, SENT);
    PFR(27, 30);
    bar();
    STAMP(34);
    a0 = z4; a1 = z4;
    gemm_alds<14>(A, w1_, a0, a1, PFS(30, 7));
#pragma unroll
    for (int s = 11; s < 14; ++s) cs = mfma(w1_[s], ones, cs);
    STAMP(35);
    bar();
    STAMP(36);
    const f2 s0 = ldf2(st_r), s1 = ldf2(st_r + 128);
    const float r0 = __builtin_amdgcn_rsqf(s0[1] + 1e-5f), r1 = __builtin_amdgcn_rsqf(s1[1] + 1e-5f);
    const float t0 = r0 * __builtin_amdgcn_rsqf(s0[1] * r0 * r0 + 1e-5f), t1 = r1 * __builtin_amdgcn_rsqf(s1[1] * r1 * r1 + 1e-5f);
#pragma unroll
    for (int r = 0; r < 4; ++r) {
      a0[r] = gelu(fmaf(-s0[0], cs[r], a0[r]) * t0);
      a1[r] = gelu(fmaf(-s1[0], cs[r], a1[r]) * t1);
    }
    stg(rws, hs_off, to_chunk(nosent4(pack8(a0, a1))) & hs_keep);
    bar();
#pragma unroll
    for (int r = 0; r < 4; ++r) { xb0[r] = (hold0[r] - s0[0]) * r0; xb1[r] = (hold1[r] - s1[0]) * r1; }
    PIN(xb0); PIN(xb1);
  }
  STAMP(10);
  PFR(37, 40);
  VMW(0);
  bar();
  PFR(40, 44);
  bar();
  STAMP(37);
  {
    gemm_alds<14>(A, w2_, xb0, xb1, PFS(44, 7));
    STAMP(38);
    const v4u p = nosent4(pack8(xb0, xb1));
    stg(rws, x1_off, to_chunk(p));
    bar();
    hold0 += f4{blo(p.x), bhi(p.x), blo(p.y), bhi(p.y)};
    hold1 += f4{blo(p.z), bhi(p.z), blo(p.w), bhi(p.w)};
    PIN(hold0); PIN(hold1);
  }
  STAMP(11);
  constexpr int W3E = 54;
  PFR(51, W3E);
  f4 cs[2] = {z4, z4};
#pragma unroll
  for (int s = 0; s < (W3E - 44) / 2; ++s) { cs[0] = mfma(w3f[s], ones, cs[0]); cs[1] = mfma(w3f[14 + s], ones, cs[1]); }
  PIN(cs[0]); PIN(cs[1]);
  u32 hid_w = L + L_HID + w * 2 * KB + l16, hid_r = L + L_HID + l16;
  u32 rs_w = l16 + OFF_RS + ((yg * 64 + mi) * 7 + w) * KB;
  u32 out_off = (zg * 32 + (g & 1) * 16 + i16) * 896 + (n * 16 + (g >> 1) * 8) * 2;
  PIN(hid_w); PIN(hid_r); PIN(rs_w); PIN(out_off);
  bar();
  PFR(W3E, W3E + 2);
  STAMP(44);
  bar();
  STAMP(12);
  f4 pu[8] = {z4, z4, z4, z4, z4, z4, z4, z4};
  {
    v4u bf[2][4];
#pragma unroll
    for (int t = 0; t < 4; ++t) { bf[0][t] = ld16(A + t * KB); }
#pragma unroll
    for (int t = 0; t < 4; ++t) { bf[1][t] = ld16(A + (4 + t) * KB); }
    SB();
#pragma unroll
    for (int s = 0; s < 14; ++s) {
      if (s == 3) { SB(); asm volatile("s_barrier" ::: "memory"); SB(); }
#pragma unroll
      for (int t = 3; t >= 0; --t) { pu[2 * t] = mfma(w3f[s], bf[s & 1][t], pu[2 * t]); pu[2 * t + 1] = mfma(w3f[14 + s], bf[s & 1][t], pu[2 * t + 1]); }
      if (s >= (W3E - 44) / 2) { cs[0] = mfma(w3f[s], ones, cs[0]); cs[1] = mfma(w3f[14 + s], ones, cs[1]); }
      SB();
      if (s + 2 < 14) {
#pragma unroll
        for (int t = 0; t < 4; ++t) bf[s & 1][t] = ld16(A + ((s + 2) * 4 + t) * KB);
      }
      pf(W3E + 2 + 2 * s); pf(W3E + 3 + 2 * s);
      SB();
    }
  }
  STAMP(39);
  for (u32 sp = 0; sp < (1u << 20) && *(volatile LAS u32*)(uintptr_t)(L + L_SF) == 0; ++sp) { }
  float fr0, fr1;
  {
    fr0 = ldf2(st_r + th * 256)[1]; fr1 = ldf2(st_r + th * 256 + 128)[1];
#pragma unroll
    for (int t = 0; t < 4; ++t) {
      const f2 st = ldf2(st_r + t * 128);
      const float k0 = GC0 * st[1], k1 = GC1 * st[1] * st[1] * st[1];
#pragma unroll
      for (int c = 0; c < 2; ++c)
#pragma unroll
        for (int r = 0; r < 4; ++r) {
          const float y = fmaf(st[0], cs[c][r], pu[2 * t + c][r]);
          pu[2 * t + c][r] = y * __builtin_amdgcn_rcpf(1.0f + __builtin_amdgcn_exp2f(y * fmaf(y * y, k1, k0)));
        }
      st16(hid_w + (t >> 1) * 14 * KB + (t & 1) * KB, pack8(pu[2 * t], pu[2 * t + 1]));
#pragma unroll
      for (int k = 0; k < 4; ++k) pf(W3E + 30 + 4 * t + k);
      SB();
    }
  }
  STAMP(40);
  bar();
  STAMP(14);
  f4 own0, own1;
  {
    v4u hf[14];
    const u32 hA = hid_r + (th ^ 1) * 14 * KB, hB = hid_r + th * 14 * KB;
#pragma unroll
    for (int k = 0; k < 14; ++k) hf[k] = ld16(hA + k * KB);
    SB();
#pragma unroll
    for (int hx = 0; hx < 2; ++hx) {
#pragma unroll
      for (int sl = 0; sl < 4; ++sl) {
        f4 c0 = z4, c1 = z4;
#pragma unroll
        for (int c = 0; c < 7; ++c) {
          c1 = mfma(w4f[sl * 7 + c], hf[2 * c + 1], c1); c0 = mfma(w4f[sl * 7 + c], hf[2 * c], c0);
          if (hx == 0 && sl == 3) { SB(); hf[2 * c] = ld16(hB + 2 * c * KB); hf[2 * c + 1] = ld16(hB + (2 * c + 1) * KB); SB(); }
          if (c == 1 && (hx | sl)) {
            SB();
            const int ph = sl == 0 ? hx - 1 : hx, ps = (sl + 3) & 3;
            stg(rws, rs_w + (((ph ? th : th ^ 1) * 4 + ((js + 1 + ps) & 3)) * 56) * KB, pack8(own0, own1));
            SB();
          }
        }
        own0 = c0; own1 = c1;
      }
    }
    PIN(own0); PIN(own1);
  }
  STAMP(42);
  VMW(0);
  bar();
  STAMP(16);
  u32 rs_r[7];
#pragma unroll
  for (int k = 0; k < 7; ++k) { rs_r[k] = OFF_RS + (((yg * 8 + mi) * 8 + ((mi + 1 + k) & 7)) * 7 + w) * KB; PINS(rs_r[k]); }
  bar();
  STAMP(43);
  {
    v4u pp[7];
#pragma unroll
    for (int k = 0; k < 7; ++k) pp[k] = ldg(rws, l16, rs_r[k]);
    SB();
#pragma unroll
    for (int k = 0; k < 7; ++k) {
      own0 = f4{dot2(pp[k].x, sel_lo, own0[0]), dot2(pp[k].x, sel_hi, own0[1]), dot2(pp[k].y, sel_lo, own0[2]), dot2(pp[k].y, sel_hi, own0[3])};
      own1 = f4{dot2(pp[k].z, sel_lo, own1[0]), dot2(pp[k].z, sel_hi, own1[1]), dot2(pp[k].w, sel_lo, own1[2]), dot2(pp[k].w, sel_hi, own1[3])};
      SB();
    }
#pragma unroll
    for (int r = 0; r < 4; ++r) { hold0[r] = fmaf(own0[r], fr0, hold0[r]); hold1[r] = fmaf(own1[r], fr1, hold1[r]); }
    *(GAS v4u*)((unsigned char*)out + out_off) = to_chunk(pack8(hold0, hold1));
  }
  if (w == 6 && lane == 0) { u64* q = (u64*)(ws + (200u << 20)) + b * 2; q[0] = pr0; q[1] = __builtin_amdgcn_s_memrealtime(); }
}

__global__ void k_probe(u64* q, int mode) {
  u64 lo = q[threadIdx.x * 2], hi = q[threadIdx.x * 2 + 1], l2 = lo;
  __shared__ u64 a[256], c[256], e[256];
  a[threadIdx.x] = lo; c[threadIdx.x] = hi; e[threadIdx.x] = l2; __syncthreads();
  for (int i = 0; i < 256; ++i) { lo = a[i] < lo ? a[i] : lo; hi = c[i] > hi ? c[i] : hi; l2 = e[i] > l2 ? e[i] : l2; }
  u64 ticks = (mode == 0 ? hi - lo : l2 - lo) * 10;
  if (ticks > 100000) ticks = 100000;
  u64 r1 = __builtin_amdgcn_s_memrealtime();
  while (__builtin_amdgcn_s_memrealtime() - r1 < ticks) { }
}
extern "C" void kernel_launch(void* const* d_in, const int* in_sizes, int n_in, void* d_out, int out_size, void* d_ws, size_t ws_size,
                              hipStream_t stream) {
  fused_kernel<<<256, 512, 0, stream>>>((const u16*)d_in[0], (const u16*)d_in[1], (const u16*)d_in[2], (const u16*)d_in[3],
                                        (const u16*)d_in[5], (const u16*)d_in[6], (const u16*)d_in[7], (const u16*)d_in[8],
                                        (u16*)d_out, (unsigned char*)d_ws);
  k_probe<<<1, 256, 0, stream>>>((u64*)((char*)d_ws + (200u << 20)), 1);
}
```

```cpp
#include <hip/hip_runtime.h>
#include <stdint.h>

typedef unsigned int u32;
typedef unsigned long long u64;
typedef unsigned short u16;
typedef u32 v4u __attribute__((ext_vector_type(4)));
typedef u32 v2u __attribute__((ext_vector_type(2)));
typedef float f4 __attribute__((ext_vector_type(4)));
typedef float f2 __attribute__((ext_vector_type(2)));
typedef __bf16 bf8 __attribute__((ext_vector_type(8)));
typedef __bf16 bf2 __attribute__((ext_vector_type(2)));
typedef short s4v __attribute__((ext_vector_type(4)));
typedef __amdgpu_buffer_rsrc_t rsrc_t;
#define GAS __attribute__((address_space(1)))
#define LAS __attribute__((address_space(3)))
#define DI __device__ __forceinline__
#define AI __attribute__((always_inline))
#define RLX_AGENT __ATOMIC_RELAXED, __HIP_MEMORY_SCOPE_AGENT

constexpr int S_ = 2048, D_ = 448, F_ = 1792;
constexpr u32 KB = 1024;
constexpr u32 OFF_E = 0, OFF_TMO = 64;
constexpr u32 OFF_HINT = 16 * KB;
constexpr u32 OFF_KVT = 192 * KB;
constexpr u32 OFF_WO = 256 * KB;
constexpr u32 OFF_W1 = OFF_WO + 448 * KB;
constexpr u32 OFF_W2 = OFF_W1 + 392 * KB;
constexpr u32 OFF_W3 = OFF_W2 + 392 * KB;
constexpr u32 OFF_W4 = OFF_W3 + 1568 * KB;
constexpr u32 OFF_KVP = OFF_W4 + 1568 * KB;
constexpr u32 OFF_O = OFF_KVP + 2048 * KB;
constexpr u32 OFF_H = OFF_O + 2048 * KB;
constexpr u32 OFF_HS = OFF_H + 1792 * KB;
constexpr u32 OFF_X1 = OFF_HS + 1792 * KB;
constexpr u32 OFF_RS = OFF_X1 + 1792 * KB;
constexpr u32 WS_BYTES = OFF_RS + 256 * 56 * KB;
constexpr u32 OFF_DBG = WS_BYTES;
constexpr u32 L_X = 0, X_RS = 928, SLOT = 16 * X_RS;
constexpr u32 L_VB = 0, L_QA = 28 * KB, L_QX = 0;
constexpr u32 L_K = 64 * X_RS;
constexpr u32 L_PK = L_K, PK_RS = 136;
constexpr u32 L_V0 = L_PK + 128 * PK_RS, V_RS = 72;
constexpr u32 L_QB = L_K + 28 * KB;
constexpr u32 L_VA = L_K + 56 * KB;
constexpr u32 L_U = L_VA;
constexpr u32 L_KX = L_VA + 28 * KB;
constexpr u32 L_V1 = L_KX;
constexpr u32 L_ST = L_KX + 9 * KB;
constexpr u32 L_SF = L_ST + 512, L_DUMP = L_ST + 768;
constexpr u32 L_PHI = L_ST + 1024;
constexpr u32 L_U0 = L_PHI + 4096;
constexpr u32 L_BYTES = L_U0 + 4096;
constexpr u32 L_KP = 8 * KB;
constexpr u32 L_KF = 16 * KB;
static_assert(L_BYTES <= 163840 && L_V0 + 128 * V_RS <= L_QB, "");
constexpr u32 L_ACT = 0;
constexpr u32 L_HID = 56 * KB;
constexpr u32 L_S5 = 84 * KB;
constexpr u32 SPIN_CAP = 1u << 15;
constexpr u32 OOB = 0x80000000u;
constexpr int SC1 = 16;
constexpr u32 CODE_BYTES = 39936;
#define XDMA dma_in
#define DT0 10
#define DT1 10
#define DT2 14
#define R1 3
#define PA 9
#define QPF 3
#define R20 7
#define PA2 13
#define DTX 26
#define PB PA2
#define R2 3
#define NL 7
#define PC (PB + R1 + R20)
#define PD (PC + NL + R1 + R2)
#define PE (PD + NL + R1 + R2)
static_assert(PC >= 16 && PC + NL + R1 >= 30 && PE >= 44, "");
#define FA 16
#define FB 14
#define NOW4 0
#define NORS 0
#define GG 2
#define OT 0
#define RF 7
#define LDELAY 3
#define DLYA 1
#define DLYB 5
#define DLY0 1
#define DLY1 3
#define HS1 2

#define SB() __builtin_amdgcn_sched_barrier(0)
#define PIN(x) asm volatile("" : "+v"(x))
#define PINS(x) do { x = __builtin_amdgcn_readfirstlane(x); asm volatile("" : "+s"(x)); } while (0)
#define DIAG 0
#define STAMP(k) do { } while (0)
#define NRETRY(k) do { } while (0)
#define STAMP_BEGIN() do { } while (0)
#define STAMP_END() do { } while (0)

template <int V> struct IC { static constexpr int v = V; };
template <int I, int N, class F> DI void sfor(F f) { if constexpr (I < N) { f(IC<I>{}); sfor<I + 1, N>(f); } }
DI f4 mfma(v4u a, v4u b, f4 c) {
  return __builtin_amdgcn_mfma_f32_16x16x32_bf16(__builtin_bit_cast(bf8, a), __builtin_bit_cast(bf8, b), c, 0, 0, 0);
}
DI u32 pk2(float a, float b) { f2 v = {a, b}; return __builtin_bit_cast(u32, __builtin_convertvector(v, bf2)); }
DI float blo(u32 u) { return __builtin_bit_cast(float, u << 16); }
DI float bhi(u32 u) { return __builtin_bit_cast(float, u & 0xffff0000u); }
DI u32 f2u(float x) { return __builtin_bit_cast(u32, x); }
DI float u2f(u32 x) { return __builtin_bit_cast(float, x); }
DI u32 nosent(u32 x) { return x < 0xfffffffeu ? x : 0xfffffffeu; }
DI v4u nosent4(v4u v) { return v4u{nosent(v.x), nosent(v.y), nosent(v.z), nosent(v.w)}; }
DI u32 umax(u32 a, u32 b) { return a > b ? a : b; }
DI u32 max4(u32 m, v4u v) { m = umax(umax(m, v.x), v.y); return umax(umax(m, v.z), v.w); }
DI float fmx(float a, float b) { return __builtin_elementwise_maximum(a, b); }
DI float fmn(float a, float b) { return __builtin_elementwise_minimum(a, b); }
DI float rsum(float x) {
  auto a = __builtin_amdgcn_permlane16_swap(f2u(x), f2u(x), false, false); x = u2f(a[0]) + u2f(a[1]);
  auto c = __builtin_amdgcn_permlane32_swap(f2u(x), f2u(x), false, false); return u2f(c[0]) + u2f(c[1]);
}
DI void bar() { SB(); asm volatile("s_waitcnt lgkmcnt(0)\n\ts_barrier" ::: "memory"); SB(); }
#define VMW(n) asm volatile("s_waitcnt vmcnt(" #n ")" ::: "memory")
DI rsrc_t mkrs(u64 p, u32 bytes) { return __builtin_amdgcn_make_buffer_rsrc((GAS void*)p, 0, (int)bytes, 0x00020000); }
DI v4u ldg(rsrc_t rs, u32 voff, u32 soff) { return __builtin_amdgcn_raw_buffer_load_b128(rs, (int)voff, (int)soff, SC1); }
DI void stg(rsrc_t rs, u32 off, v4u v) { __builtin_amdgcn_raw_buffer_store_b128(v, rs, (int)off, 0, SC1); }
template <int N, int DL>
DI void dma_in(rsrc_t rs, u32 voff, u32 soff, u32 ds, u32 lds) {
  asm volatile("s_mov_b32 m0, %4\n\ts_nop 0\n\tbuffer_load_dwordx4 %1, %2, %0 offen lds\n\t"
               ".rept %c5\n\ts_add_u32 m0, m0, %c6\n\ts_add_u32 %0, %0, %3\n\tbuffer_load_dwordx4 %1, %2, %0 offen lds\n\t.endr"
               : "+s"(soff) : "v"(voff), "s"(rs), "s"(ds), "s"(lds), "n"(N - 1), "n"(DL) : "memory", "scc");
}
template <int N, int DL>
DI void dma_nt(rsrc_t rs, u32 voff, u32 soff, u32 ds, u32 lds) {
  asm volatile("s_mov_b32 m0, %4\n\ts_nop 0\n\tbuffer_load_dwordx4 %1, %2, %0 offen nt lds\n\t"
               ".rept %c5\n\ts_add_u32 m0, m0, %c6\n\ts_add_u32 %0, %0, %3\n\tbuffer_load_dwordx4 %1, %2, %0 offen nt lds\n\t.endr"
               : "+s"(soff) : "v"(voff), "s"(rs), "s"(ds), "s"(lds), "n"(N - 1), "n"(DL) : "memory", "scc");
}
DI void touch(rsrc_t rs, u32 voff, u32 lds) {
  asm volatile("s_mov_b32 m0, %2\n\ts_nop 0\n\tbuffer_load_dword %0, %1, 0 offen sc1 lds" :: "v"(voff), "s"(rs), "s"(lds) : "memory");
}
template <int N>
DI void dma_ws(rsrc_t rs, u32 voff, u32 soff, u32 lds) {
#define L4_ "buffer_load_dwordx4 %1, %2, %0 offen sc1 lds\n\tbuffer_load_dwordx4 %1, %2, %0 offen offset:1024 sc1 lds\n\t" \
            "buffer_load_dwordx4 %1, %2, %0 offen offset:2048 sc1 lds\n\tbuffer_load_dwordx4 %1, %2, %0 offen offset:3072 sc1 lds\n\t"
  asm volatile("s_mov_b32 m0, %3\n\ts_nop 0\n\t" L4_
               ".rept %c4\n\ts_add_u32 m0, m0, 0x1000\n\ts_add_u32 %0, %0, 0x1000\n\t" L4_ ".endr"
               : "+s"(soff) : "v"(voff), "s"(rs), "s"(lds), "n"(N - 1) : "memory", "scc");
}
DI void poll(rsrc_t rs, u32 voff, u64 want) {
  u64 t; u32 cnt = SPIN_CAP;
  asm volatile("1:\n\tbuffer_load_dwordx2 %0, %2, %3, 0 offen sc1\n\ts_waitcnt vmcnt(0)\n\tv_cmp_ne_u64 vcc, %4, %0\n\ts_cbranch_vccz 2f\n\t"
               "s_sleep 1\n\ts_sub_u32 %1, %1, 1\n\ts_cbranch_scc0 1b\n2:"
               : "=&v"(t), "+s"(cnt) : "v"(voff), "s"(rs), "s"(want) : "vcc", "scc", "memory");
}
DI u32 now() { u64 t; asm volatile("s_memrealtime %0\n\ts_waitcnt lgkmcnt(0)" : "=s"(t) :: "memory"); return (u32)t; }
DI void peek_issue(rsrc_t rs, u32 voff) {
  asm volatile("buffer_load_dwordx4 v[248:251], %0, %1, 0 offen sc1" :: "v"(voff), "s"(rs) : "v248", "v249", "v250", "v251", "memory");
}
template <int N>
DI u32 peek_take(rsrc_t rs, u32 voff, u64 want) {
  v4u t; u32 cnt = SPIN_CAP;
  asm volatile("s_waitcnt vmcnt(%5)\n1:\n\tv_cmp_ne_u64 vcc, %4, v[248:249]\n\ts_cbranch_vccz 2f\n\t"
               "buffer_load_dwordx4 v[248:251], %2, %3, 0 offen sc1\n\ts_waitcnt vmcnt(0)\n\ts_sub_u32 %1, %1, 1\n\ts_cbranch_scc0 1b\n2:"
               : "=&{v[248:251]}"(t), "+s"(cnt) : "v"(voff), "s"(rs), "s"(want), "n"(N) : "vcc", "scc", "memory");
  return t.z;
}
template <int N, int DT>
DI void pace(rsrc_t rs, u32 voff, u64 want, u32 own) {
  u32 cnt = SPIN_CAP, c2 = 256, a;
  asm volatile("s_memrealtime s[90:91]\n\ts_waitcnt vmcnt(%7)\n1:\n\tv_cmp_ne_u64 vcc, %5, v[248:249]\n\ts_cbranch_vccz 2f\n\t"
               "buffer_load_dwordx4 v[248:251], %3, %4, 0 offen sc1\n\ts_waitcnt vmcnt(0)\n\ts_sub_u32 %0, %0, 1\n\ts_cbranch_scc0 1b\n2:\n\t"
               "v_readlane_b32 %2, v250, %6\n\ts_waitcnt lgkmcnt(0)\n\ts_sub_u32 %2, s90, %2\n\ts_add_u32 %2, %2, %8\n\tv_add_u32 v250, %2, v250\n"
               "3:\n\ts_memrealtime s[90:91]\n\ts_waitcnt lgkmcnt(0)\n\tv_sub_u32 v251, s90, v250\n\tv_cmp_gt_i32 vcc, 0, v251\n\ts_cbranch_vccz 4f\n\t"
               "s_sub_u32 %1, %1, 1\n\ts_cbranch_scc0 3b\n4:"
               : "+s"(cnt), "+s"(c2), "=&s"(a) : "v"(voff), "s"(rs), "s"(want), "s"(own), "n"(N), "n"(DT)
               : "v248", "v249", "v250", "v251", "s90", "s91", "vcc", "scc", "memory");
}
DI void wait_until(u32 when) {
  u64 t; u32 d, cnt = 256;
  asm volatile("1:\n\ts_memrealtime %0\n\ts_waitcnt lgkmcnt(0)\n\tv_sub_u32 %1, s90, %3\n\tv_cmp_gt_i32 vcc, 0, %1\n\ts_cbranch_vccz 2f\n\t"
               "s_sub_u32 %2, %2, 1\n\ts_cbranch_scc0 1b\n2:"
               : "=&{s[90:91]}"(t), "=&v"(d), "+s"(cnt) : "v"(when) : "vcc", "scc", "memory");
}
#define P4L_ "buffer_load_dwordx2 v[244:245], %[a], %[r], 0 offen sc1\n\tbuffer_load_dwordx2 v[246:247], %[a], %[r], %[k] offen sc1\n\t" \
             "buffer_load_dwordx2 v[248:249], %[b], %[r], 0 offen sc1\n\tbuffer_load_dwordx2 v[250:251], %[b], %[r], %[k] offen sc1\n\t"
#define P4C_ "v244", "v245", "v246", "v247", "v248", "v249", "v250", "v251"
DI void peek4_issue(rsrc_t rs, u32 voff) {
  asm volatile(P4L_ :: [a] "v"(voff), [b] "v"(voff + 8192), [r] "s"(rs), [k] "s"(4096) : P4C_, "memory");
}
DI void peek4_take(rsrc_t rs, u32 voff, u64 want) {
  u64 m; u32 cnt = SPIN_CAP;
  asm volatile("1:\n\tv_cmp_ne_u64 %[m], %[w], v[244:245]\n\tv_cmp_ne_u64 vcc, %[w], v[246:247]\n\ts_or_b64 %[m], %[m], vcc\n\t"
               "v_cmp_ne_u64 vcc, %[w], v[248:249]\n\ts_or_b64 %[m], %[m], vcc\n\tv_cmp_ne_u64 vcc, %[w], v[250:251]\n\ts_or_b64 %[m], %[m], vcc\n\t"
               "s_cbranch_scc0 2f\n\t" P4L_ "s_waitcnt vmcnt(0)\n\ts_sub_u32 %[c], %[c], 1\n\ts_cbranch_scc0 1b\n2:"
               : [m] "=&s"(m), [c] "+s"(cnt) : [a] "v"(voff), [b] "v"(voff + 8192), [r] "s"(rs), [k] "s"(4096), [w] "s"(want) : P4C_, "vcc", "scc", "memory");
}
constexpr float GC0 = -2.0f * 0.7978845608028654f * 1.4426950408889634f, GC1 = GC0 * 0.044715f;
DI float gelu(float x) {
  const float e = __builtin_amdgcn_exp2f(x * fmaf(x * x, GC1, GC0));
  return x * __builtin_amdgcn_rcpf(1.0f + e);
}

DI void hedgehog(f4 p0, f4 p1, float keep, v4u& pos, v4u& neg) {
  const float L2E = 1.4426950408889634f;
  float mx = fmx(fmx(fmx(fmx(p0[0], p0[1]), p0[2]), fmx(fmx(p0[3], p1[0]), p1[1])), fmx(p1[2], p1[3]));
  float nm = -fmn(fmn(fmn(fmn(p0[0], p0[1]), p0[2]), fmn(fmn(p0[3], p1[0]), p1[1])), fmn(p1[2], p1[3]));
  {
    auto a = __builtin_amdgcn_permlane16_swap(f2u(mx), f2u(nm), false, false);
    float t = fmx(u2f(a[0]), u2f(a[1]));
    auto c = __builtin_amdgcn_permlane32_swap(f2u(t), f2u(t), false, false);
    t = fmx(u2f(c[0]), u2f(c[1])) * -L2E;
    auto d = __builtin_amdgcn_permlane16_swap(f2u(t), f2u(t), false, false);
    mx = u2f(d[0]); nm = u2f(d[1]);
  }
  float a[8], c[8];
#pragma unroll
  for (int r = 0; r < 4; ++r) {
    a[r] = __builtin_amdgcn_exp2f(fmaf(p0[r], L2E, mx));
    c[r] = __builtin_amdgcn_exp2f(fmaf(p0[r], -L2E, nm));
    a[4 + r] = __builtin_amdgcn_exp2f(fmaf(p1[r], L2E, mx));
    c[4 + r] = __builtin_amdgcn_exp2f(fmaf(p1[r], -L2E, nm));
  }
  float sa = fmaf((a[4] + a[5]) + (a[6] + a[7]), keep, (a[0] + a[1]) + (a[2] + a[3]));
  float sc = fmaf((c[4] + c[5]) + (c[6] + c[7]), keep, (c[0] + c[1]) + (c[2] + c[3]));
  {
    auto x = __builtin_amdgcn_permlane16_swap(f2u(sa), f2u(sc), false, false);
    float t = u2f(x[0]) + u2f(x[1]);
    auto y = __builtin_amdgcn_permlane32_swap(f2u(t), f2u(t), false, false);
    t = __builtin_amdgcn_rcpf(u2f(y[0]) + u2f(y[1]));
    auto d = __builtin_amdgcn_permlane16_swap(f2u(t), f2u(t), false, false);
    sa = u2f(d[0]); sc = u2f(d[1]);
  }
  const float sa1 = sa * keep, sc1 = sc * keep;
  pos = v4u{pk2(a[0] * sa, a[1] * sa), pk2(a[2] * sa, a[3] * sa), pk2(a[4] * sa1, a[5] * sa1), pk2(a[6] * sa1, a[7] * sa1)};
  neg = v4u{pk2(c[0] * sc, c[1] * sc), pk2(c[2] * sc, c[3] * sc), pk2(c[4] * sc1, c[5] * sc1), pk2(c[6] * sc1, c[7] * sc1)};
}
DI v4u pack8(f4 a, f4 b) { return v4u{pk2(a[0], a[1]), pk2(a[2], a[3]), pk2(b[0], b[1]), pk2(b[2], b[3])}; }
DI v4u to_chunk(v4u p) {
  auto x = __builtin_amdgcn_permlane16_swap(p.x, p.z, false, false);
  auto y = __builtin_amdgcn_permlane16_swap(p.y, p.w, false, false);
  return v4u{x[0], y[0], x[1], y[1]};
}
DI float diag(f4 q, int p4) {
  const float a = q[0], b = q[1], c = q[2], d = q[3];
  const float t0 = (p4 & 1) ? b : a, t1 = (p4 & 1) ? d : c;
  return (p4 & 2) ? t1 : t0;
}
DI v4u ld16(u32 addr) { return *(LAS v4u*)(uintptr_t)addr; }
DI void st16(u32 addr, v4u v) { *(LAS v4u*)(uintptr_t)addr = v; }
DI void st8(u32 addr, u32 a, u32 b) { *(LAS v2u*)(uintptr_t)addr = v2u{a, b}; }
DI f2 ldf2(u32 addr) { return *(LAS f2*)(uintptr_t)addr; }
DI v2u trread(u32 addr) {
  return __builtin_bit_cast(v2u, __builtin_amdgcn_ds_read_tr16_b64_v4i16((LAS s4v*)(uintptr_t)addr));
}
DI float dot2(u32 a, u32 sel, float c) {
  return __builtin_amdgcn_fdot2_f32_bf16(__builtin_bit_cast(bf2, a), __builtin_bit_cast(bf2, sel), c, false);
}

template <class PF>
DI void gemm_tr(u32 t0, u32 t1, const v4u (&xa)[7], const v4u (&xb)[7], f4& a0, f4& a1, f4& b0, f4& b1, PF pf) {
  constexpr int P = 3;
  PIN(t0); PIN(t1);
  v2u r[P][4];
#pragma unroll
  for (int k = 0; k < P; ++k) { r[k][0] = trread(t0 + k * 4096); r[k][1] = trread(t0 + k * 4096 + 512); r[k][2] = trread(t1 + k * 4096); r[k][3] = trread(t1 + k * 4096 + 512); }
  SB();
  a0 = f4{0, 0, 0, 0}; a1 = a0; b0 = a0; b1 = a0;
#pragma unroll
  for (int s = 0; s < 7; ++s) {
    const int k = s % P;
    const v4u f0 = {r[k][0].x, r[k][0].y, r[k][1].x, r[k][1].y}, f1 = {r[k][2].x, r[k][2].y, r[k][3].x, r[k][3].y};
    a1 = mfma(f1, xa[s], a1); b1 = mfma(f1, xb[s], b1);
    a0 = mfma(f0, xa[s], a0); b0 = mfma(f0, xb[s], b0);
    SB();
    if (s + P < 7) {
      r[k][0] = trread(t0 + (s + P) * 4096); r[k][1] = trread(t0 + (s + P) * 4096 + 512);
      r[k][2] = trread(t1 + (s + P) * 4096); r[k][3] = trread(t1 + (s + P) * 4096 + 512);
    }
    pf(s);
    SB();
  }
}
template <int NS, class PF>
DI void gemm_alds(u32 p, const v4u (&wf)[NS], f4& a0, f4& a1, PF pf) {
  constexpr int P = 6;
  v4u b0[P], b1[P];
#pragma unroll
  for (int k = 0; k < P; ++k) { b0[k] = ld16(p + 2 * k * KB); b1[k] = ld16(p + (2 * k + 1) * KB); }
  SB();
#pragma unroll
  for (int s = 0; s < NS; ++s) {
    a1 = mfma(wf[s], b1[s % P], a1);
    a0 = mfma(wf[s], b0[s % P], a0);
    SB();
    if (s == 2) {
      asm volatile("s_barrier" ::: "memory");
#pragma unroll
      for (int k = 0; k < 3; ++k) { b0[k] = ld16(p + 2 * (k + P) * KB); b1[k] = ld16(p + (2 * (k + P) + 1) * KB); }
    }
    if (s > 2 && s + P < NS) { b0[s % P] = ld16(p + 2 * (s + P) * KB); b1[s % P] = ld16(p + (2 * (s + P) + 1) * KB); }
    pf(s);
    SB();
  }
}
template <int J0, int J1, int EXTRA, bool ST>
DI bool g_check(u32 p, f4 (&sm)[4]) {
  const v4u ones = {0x3f803f80u, 0x3f803f80u, 0x3f803f80u, 0x3f803f80u};
  constexpr int LA = 2;
  v4u fr[4 * (J1 - J0)];
  u32 m = 0;
  sfor<J0, J1 + LA>([&](auto J) AI {
    constexpr int j = J.v;
    if constexpr (j < J1) {
      if constexpr (EXTRA >= 0) asm volatile("s_waitcnt vmcnt(%0)" :: "n"(EXTRA + 4 * (J1 - 1 - j)) : "memory");
#pragma unroll
      for (int e = 0; e < 4; ++e) fr[4 * (j - J0) + e] = ld16(p + (4 * j + e) * KB);
      SB();
    }
    constexpr int d = J0 == 0 ? 1 : LA;
    if constexpr (j >= J0 + d && j < J1 + d) {
#pragma unroll
      for (int e = 3; e >= 0; --e) {
        const v4u f = fr[4 * (j - d - J0) + e];
        if (ST) { sm[e & 1] = mfma(ones, f, sm[e & 1]); sm[2 + (e & 1)] = mfma(f, f, sm[2 + (e & 1)]); }
        else m = max4(m, f);
      }
      SB();
    }
  });
  if (ST) {
    const float t = sm[0][0] + sm[1][0];
    if (__all(t == t)) return true;
#pragma unroll
    for (int k = 0; k < 4 * (J1 - J0); ++k) m = max4(m, fr[k]);
  }
  return __all(m != 0xffffffffu);
}
#define RETRY_LOOP _Pragma("clang loop unroll(disable)") for (u32 sp = 0;; ++sp)
#define RETRY_END(k) NRETRY(k); if (sp > SPIN_CAP) break; asm volatile("s_waitcnt lgkmcnt(0)" ::: "memory")

__global__ void __launch_bounds__(512, 2)
fused_kernel(const u16* x_, const u16* wqkv_, const u16* wphi_, const u16* wo_a, const u16* w1_a, const u16* w2_a, const u16* w3_a,
             const u16* w4_a, u16* out_, unsigned char* ws_) {
  u64 p_x, p_q, p_phi, p_out, p_ws;
  asm volatile("s_load_dwordx2 %0, %5, 0\n\ts_load_dwordx2 %1, %5, 8\n\ts_load_dwordx2 %4, %5, 0x48\n\ts_load_dwordx2 %2, %5, 0x10\n\ts_load_dwordx2 %3, %5, 0x40"
               : "=&s"(p_x), "=&s"(p_q), "=&s"(p_phi), "=&s"(p_out), "=&s"(p_ws) : "s"(__builtin_amdgcn_kernarg_segment_ptr()));
  __shared__ __attribute__((aligned(1024))) unsigned char lds_raw[L_BYTES];
  const u32 L = (u32)(uintptr_t)(LAS unsigned char*)lds_raw;
  const int tid = threadIdx.x, lane = tid & 63, w = __builtin_amdgcn_readfirstlane(tid >> 6), g = lane >> 4, i16 = lane & 15;
  const int b = blockIdx.x, xcd = b & 7, ii = b >> 3;
  const int zg = xcd * 8 + (ii >> 2), js = ii & 3;
  const int yg = xcd * 4 + (ii >> 3), mi = ii & 7, ms = (mi + (ii >> 3)) & 7;
  const int th = (ii >> 2) & 1;
  const u32 l16 = lane * 16;
  STAMP_BEGIN();
  const int u = w >> 2, hh = w & 1, T = (w & 2) + (hh ^ u), head = mi * 2 + hh;
  if (w == 4) {
    u64 pc; asm volatile("s_getpc_b64 %0" : "=s"(pc));
    touch(mkrs(pc & ~255ull, CODE_BYTES), (ii + 32 * lane) * 128, L + L_DUMP);
  }
  u32 xs0 = (yg * 64 + w * 16 + (mi & 1) * 8) * 896, xl0 = L + L_X + w * SLOT + (mi & 1) * 8 * X_RS;
  u32 xs1 = (yg * 64 + w * 16 + (~mi & 1) * 8) * 896, xl1 = L + L_X + w * SLOT + (~mi & 1) * 8 * X_RS;
  const u32 tn = w == 5 ? 37 : 4, tb = w == 5 ? 0 : OFF_HINT;
  u32 tv = (tb + (b * tn + lane) * 128) | ((u32)lane < tn ? 0 : OOB);
  PINS(xs0); PINS(xl0); PINS(xs1); PINS(xl1); PIN(tv);
  SB();
  asm volatile("s_waitcnt lgkmcnt(0)" : "+s"(p_x), "+s"(p_q), "+s"(p_phi), "+s"(p_out), "+s"(p_ws));
  const rsrc_t rx = mkrs(p_x, S_ * D_ * 2);
  const rsrc_t rq = mkrs(p_q, D_ * 3 * D_ * 2);
  const rsrc_t rws = mkrs(p_ws, WS_BYTES);
  if (u == 0) {
    __builtin_amdgcn_s_setprio(2);
    asm volatile("s_bfm_b64 exec, 56, 0" ::: "memory");
    XDMA<8, X_RS>(rx, l16, xs0, 896, xl0);
    XDMA<8, X_RS>(rx, l16, xs1, 896, xl1);
    asm volatile("s_mov_b64 exec, -1" ::: "memory");
  } else if (w == 5) {
    __builtin_amdgcn_s_setprio(3);
    touch(rq, tv, L + L_DUMP);
    __builtin_amdgcn_s_setprio(0);
  } else if (w == 6) {
    __builtin_amdgcn_s_setprio(3);
    touch(rws, tv, L + L_DUMP);
    __builtin_amdgcn_s_setprio(0);
  }
  SB();
  const u32 wc = (lane & 7) ^ ((lane >> 3) & 2) ^ ((w & 1) << 2);
  u32 wv = (((w & 3) * 8 + (lane >> 3)) * 2688 + mi * 112 + wc * 16) | (wc < 7 ? 0 : OOB);
  u32 wl = L + (w & 3) * KB;
  PIN(wv); PINS(wl);
  SB();
  STAMP(24);
  u32 epoch;
  asm volatile("s_load_dword %0, %1, 0" : "=s"(epoch) : "s"(p_ws));
  const v4u SENT = {0xffffffffu, 0xffffffffu, 0xffffffffu, 0xffffffffu};
  const v4u ones = {0x3f803f80u, 0x3f803f80u, 0x3f803f80u, 0x3f803f80u};
  const f4 z4 = {0, 0, 0, 0};
  const int q4 = i16 >> 2, p4 = i16 & 3;
  const bool padg = g == 3;
  u32 vz = lane == 0 ? 0 : OOB;
  PIN(vz);
  auto post = [&](u32 off) AI { __builtin_amdgcn_raw_buffer_store_b64(v2u{epoch, 0x9e3779b9u}, rws, (int)vz, (int)off, SC1); };
  auto post_t = [&](u32 off) AI { const u32 t = now(); stg(rws, vz + off, v4u{epoch, 0x9e3779b9u, t, 0}); return t; };
  const int n = js * 7 + (w < 7 ? w : 0);
  const u32 unit = u == 1 ? 4 * b + (w - 4) : w != 0 ? 2000 : ((mi + 1) & 6) == 0 ? 1024 + yg * 2 + (mi & 1) : mi == 1 ? 1088 + yg : 2000;
  const bool hasu = unit < 1092;
  u32 u_ts = 0, u_dst = 0, u_lo = 0, u_hi = 0, u_keep = 0;
  if (hasu) {
    const u32 sec = ((unit + 1936) >> 11) + ((unit + 1838) >> 11) + ((unit + 1740) >> 11) + ((unit + 1348) >> 11);
    const u64 base = ((const __attribute__((address_space(4))) u64*)__builtin_amdgcn_kernarg_segment_ptr())[3 + sec];
    const u32 first = (u32)(0x2bc1340d2070000ull >> (sec * 12)) & 0xfffu, uu = unit - first;
    const u32 sh3 = sec == 3 ? 2 : 0;
    const bool prm = (sec & 3) == 0;
    const u32 u7 = (uu * 9363) >> 16, st = u7 >> sh3, cb = uu - ((st * 7) << sh3), m = sec == 4 ? (u7 * 9363) >> 16 : 0;
    const rsrc_t rs = mkrs(base, sec >= 3 ? D_ * F_ * 2 : D_ * D_ * 2);
    const u32 vo = (st * (sec == 0 ? 28 : 32) + (lane >> 3)) * (896 << sh3) + cb * 128 + (lane & 7) * 16;
    if (u == 1) dma_nt<4, 4 * KB>(rs, vo, 0, (8 * 896) << sh3, wl + L_VA);
    else dma_nt<4, KB>(rs, vo, 0, (8 * 896) << sh3, L + L_U0);
    const u32 psh = u == 1 ? 12 : 10, r0 = (prm ? 4 * g : 8 * g) + q4;
    u_ts = ((0x7eee0u >> (sec * 4)) & 15) * KB + (sec == 0 ? 16 * KB : 0);
    u_dst = l16 + OFF_WO + first * 4 * KB + cb * 4 * u_ts + (m * 189 + st) * KB;
    u_lo = (u == 1 ? wl + L_VA : L + L_U0) + ((r0 >> 3) << psh) + (r0 & 7) * 128 + p4 * 8;
    u_hi = u_lo + (prm ? 2u << psh : 512);
    u_keep = (sec == 0 && padg) ? 0u : 0xffffffffu;
  }
  PINS(u_ts); PIN(u_dst); PIN(u_lo); PIN(u_hi); PIN(u_keep);
  if (u == 1) {
    dma_in<1, 0>(mkrs(p_phi, 16 * 28 * 28 * 2), l16, mi * 3136 + (w - 4) * KB, 0, L + L_PHI + (w - 4) * KB);
    dma_in<4, 4 * KB>(rq, wv, 896, 32 * 2688, wl + L_K);
  }
  SB();
  STAMP(46);
  u32 kvp_off = OFF_KVP + ((mi * 8 + w) * 4 + g) * 8 * KB + yg * 256 + i16 * 16;
  u32 kvt_off = g == 0 ? OFF_KVT + (mi * 32 + yg) * 256 + i16 * 16 : OOB;
  u32 kvt8 = g < 2 ? OFF_KVT + (mi * 32 + yg) * 256 + i16 * 16 + g * 8 : OOB;
  PIN(kvt8);
  u32 o_off = l16 + OFF_O + (((yg * 2 + (T >> 1)) * 16 + head) * 2 + (T & 1)) * KB;
  float keep = padg ? 0.f : 1.f;
  const u32 trx = (((q4 >> 1) & 1) | ((g & 1) << 1)) * 32;
  u32 tr0 = L + (8 * g + q4) * 128 + ((56 * hh + 8 * p4) ^ trx);
  u32 tr1 = L + (8 * g + q4) * 128 + ((56 * hh + 32 + 8 * (p4 == 3 ? 2 : p4)) ^ trx);
  u32 kx_w = L + L_KX + (w ^ 4) * KB + l16, kx_r = L + L_KX + w * KB + l16;
  u32 pk_w = L + L_PK + (hh * 64 + T * 16 + i16) * PK_RS + g * 8;
  u32 va_w = L + (u ? L_V1 : L_V0) + (hh * 64 + T * 16 + i16) * V_RS + g * 8, vb_w = L + (u ? L_V1 : L_V0) + (hh * 64 + (T ^ 1) * 16 + i16) * V_RS + g * 8;
  u32 kv_a = L + L_PK + (u * 64 + g * 8 + q4) * PK_RS + ((w >> 1) & 1) * 64 + p4 * 8;
  u32 kv_b = L + L_V0 + (u * 64 + g * 8 + q4) * V_RS + (w & 1) * 32 + p4 * 8, kv_c = kv_b + (L_V1 - L_V0);
  u32 ph_a = L + L_PHI + hh * 1568 + (4 * g + q4) * 56 + p4 * 8;
  u32 xr_r = L + L_KX + i16 * 256 + ((((w < 7 ? w : 0) * 2 + (g >> 1)) ^ i16) * 16) + (g & 1) * 8;
  PIN(xr_r);
  u32 xo_r = L + L_X + (T ^ 1) * SLOT + u * 7 * KB + l16;
  u32 h32 = OFF_HINT + (mi * 32 + (lane & 31)) * 64, uf_off = OFF_HINT + 4 * 256 * 64 + 32 + lane * 64;
  PIN(uf_off);
  const u32 cko = (2 * (n & 1) + (g >> 1)) * 256;
  u32 h_off = ((zg * 14 + (n >> 1)) * 2 + (g & 1)) * KB + cko + i16 * 16 + OFF_H;
  PIN(h_off);
  PIN(kvp_off); PIN(kvt_off); PIN(o_off); PIN(keep); PIN(tr0); PIN(tr1); PIN(kx_w); PIN(kx_r);
  PIN(pk_w); PIN(va_w); PIN(vb_w); PIN(kv_a); PIN(kv_b); PIN(kv_c); PIN(ph_a); PIN(xo_r); PIN(h32);
  SB();
  v4u xa[7], xb[7];
  if (u == 0) {
    u32 xp = L + L_X + w * SLOT + i16 * X_RS + g * 16, xw = L + L_X + w * SLOT + l16;
    PIN(xp); PIN(xw);
    if (hasu) VMW(4); else VMW(0);
    STAMP(25);
#pragma unroll
    for (int s = 0; s < 7; ++s) xb[s] = ld16(xp + 448 + s * 64);
#pragma unroll
    for (int s = 0; s < 7; ++s) xa[s] = ld16(xp + s * 64);
    SB();
    f4 sq = z4, sm = z4;
#pragma unroll
    for (int s = 0; s < 7; ++s) { sq = mfma(xb[s], xb[s], sq); sm = mfma(ones, xb[s], sm); }
#pragma unroll
    for (int s = 0; s < 7; ++s) { sq = mfma(xa[s], xa[s], sq); sm = mfma(ones, xa[s], sm); }
    const float mu = sm[0] * (1.0f / D_), dgv = diag(sq, p4);
    const float var = fmx(fmaf(-mu, mu, rsum(g == q4 ? dgv : 0.f) * (1.0f / D_)), 0.f);
    const float rs = __builtin_amdgcn_rsqf(var + 1e-5f), nb = -mu * rs;
    STAMP(26);
#pragma unroll
    for (int s = 0; s < 7; ++s) {
#pragma unroll
      for (int e = 0; e < 4; ++e) xb[s][e] = pk2(fmaf(blo(xb[s][e]), rs, nb), fmaf(bhi(xb[s][e]), rs, nb));
      st16(xw + (7 + s) * KB, xb[s]);
      SB();
    }
#pragma unroll
    for (int s = 0; s < 7; ++s) {
#pragma unroll
      for (int e = 0; e < 4; ++e) xa[s][e] = pk2(fmaf(blo(xa[s][e]), rs, nb), fmaf(bhi(xa[s][e]), rs, nb));
      st16(xw + s * KB, xa[s]);
      SB();
    }
    __builtin_amdgcn_s_setprio(0);
    VMW(0);
  } else {
    dma_in<10, 4 * KB>(rq, wv, 896 + 4 * 32 * 2688, 32 * 2688, wl + L_K + 16 * KB);
#pragma unroll
    for (int d = 0; d < 2; ++d) {
      const u32 c = i16 ^ ((w & 1) * 8 + d * 4 + g);
      dma_in<1, 0>(rx, (g * 896 + js * 224 + c * 16) | (c < 14 ? 0 : OOB), (zg * 32 + (w - 4) * 8 + d * 4) * 896, 0, L + L_KX + ((w - 4) * 2 + d) * KB);
    }
    STAMP(25);
    VMW(17);
  }
  if (hasu) {
    v2u lo[4], hi[4];
#pragma unroll
    for (int t = 0; t < 4; ++t) { lo[t] = trread(u_lo + t * 32); hi[t] = trread(u_hi + t * 32); }
    asm volatile("s_waitcnt lgkmcnt(0)" ::: "memory");
    SB();
    if (u == 1) dma_in<7, 4 * KB>(rq, wv, 1792, 32 * 2688, wl + L_VA);
    STAMP(47);
#pragma unroll
    for (int t = 0; t < 4; ++t) stg(rws, u_dst + t * u_ts, v4u{lo[t].x, lo[t].y, hi[t].x & u_keep, hi[t].y & u_keep});
    SB();
  }
  if (u == 1) VMW(11);
  STAMP(27);
  bar();
  asm volatile("" : "+s"(epoch));
  const u64 tag = (u64)epoch | (0x9e3779b9ull << 32);
  STAMP(18);
#pragma unroll
  for (int s = 0; s < 7; ++s) xb[s] = ld16(xo_r + s * KB);
  if (u == 1) {
    const u32 xm = L + L_X + T * SLOT + 7 * KB + l16;
#pragma unroll
    for (int s = 0; s < 7; ++s) xa[s] = ld16(xm + s * KB);
  }
  v2u xr[2];
  xr[0] = *(LAS v2u*)(uintptr_t)xr_r; xr[1] = *(LAS v2u*)(uintptr_t)(xr_r + 4 * KB);
  bar();
  STAMP(1);
  v4u wo_[16], w1_[14], w2_[14], w3f[28], w4f[28];
  u32 lw = w < 7 ? l16 : OOB;
  PIN(lw);
  auto pf = [&](int k) AI {
    if (k < 16) wo_[k] = ldg(rws, lw + (k & 3) * KB, OFF_WO + (n * 16 + (k & ~3)) * KB);
    else if (k < 30) w1_[k - 16] = ldg(rws, lw + (k & 3) * KB, OFF_W1 + (n * 14 + ((k - 16) & ~3)) * KB);
    else if (k < 44) w2_[k - 30] = ldg(rws, lw + ((k - 30) & 3) * KB, OFF_W2 + (n * 14 + ((k - 30) & ~3)) * KB);
    else if (k < 72) { const int t = (k - 44) & 1, st = (k - 44) >> 1;
      w3f[t * 14 + st] = ldg(rws, lw + (st & 3) * KB, OFF_W3 + ((ms * 14 + w * 2 + t) * 14 + (st & ~3)) * KB); }
    else if (k < 100) { const int sl = (k - 72) / 7, c = (k - 72) % 7;
      w4f[k - 72] = ldg(rws, (lw | NOW4) + (c & 3) * KB, OFF_W4 + ((ms * 28 + ((js + 1 + sl) & 3) * 7 + w) * 7 + (c & ~3)) * KB); }
  };
#define PFR(lo, hi) do { _Pragma("unroll") for (int k_ = (lo); k_ < (hi); ++k_) pf(k_); asm volatile("" ::: "memory"); SB(); } while (0)
#define PFS(base, cnt) [&](int s_) AI { if ((s_ & 1) == 0 && (s_ >> 1) < (cnt)) pf((base) + (s_ >> 1)); }
  v4u phA, phB;
  {
    if (u == 1) {
      stg(rws, kvp_off, SENT); stg(rws, kvp_off - 4 * 32 * KB, SENT);
      if (w == 7) stg(rws, kvt_off, SENT);
    } else {
      stg(rws, o_off, SENT); stg(rws, o_off ^ KB, SENT);
      stg(rws, h_off, SENT); stg(rws, w < 3 ? h_off + 4 * KB : OOB, SENT);
    }
    STAMP(20);
    f4 a0, a1, b0, b1;
    gemm_tr(tr0 + L_K + u * 28 * KB, tr1 + L_K + u * 28 * KB, xa, xb, a0, a1, b0, b1, [](int) {});
    {
      const u32 a = ph_a, c = a + (p4 == 3 ? 24 : 32);
      const v2u l0 = trread(a), h0 = trread(a + 16 * 56), l1 = trread(c), h1 = trread(c + 16 * 56);
      st16(kx_w, pack8(b0, b1));
      phA = v4u{l0.x, l0.y, padg ? 0u : h0.x, padg ? 0u : h0.y};
      phB = v4u{l1.x, l1.y, padg ? 0u : h1.x, padg ? 0u : h1.y};
      PIN(phA); PIN(phB);
    }
    const v4u ka = pack8(a0, a1);
    f4 p0 = mfma(phA, ka, z4), p1 = mfma(phB, ka, z4);
    STAMP(2);
    if (u == 0) { VMW(4); dma_in<7, 4 * KB>(rq, wv, 1792 + 224 * 2688, 32 * 2688, wl + L_VB); }
    else VMW(0);
    STAMP(19);
    bar();
    STAMP(3);
    const v4u kb = ld16(kx_r);
    if (w == 4) post(OFF_HINT + (4 * 256 + b) * 64 + 32);
    SB();
    p0 = mfma(phA, kb, p0); p1 = mfma(phB, kb, p1);
    PIN(p0); PIN(p1);
    const u32 vbase = u ? L_VB : L_VA;
#pragma clang loop unroll(disable)
    for (int ph = 0; ph < 2; ++ph) {
      if (ph == u) {
        u32 qs = u * 224 * 2688, ql = wl + (u ? L_QB : L_QA);
        gemm_tr(tr0 + vbase, tr1 + vbase, xa, xb, a0, a1, b0, b1, [&](int) AI {
          asm volatile("s_mov_b32 m0, %1\n\ts_add_u32 %1, %1, 0x1000\n\tbuffer_load_dwordx4 %2, %3, %0 offen lds\n\ts_add_u32 %0, %0, %4"
                       : "+s"(qs), "+s"(ql) : "v"(wv), "s"(rq), "n"(32 * 2688) : "memory", "scc"); });
        if (padg) { a1[0] = (float)(1 - u); b1[0] = a1[0]; }
        const v4u va = pack8(a0, a1), vb = pack8(b0, b1);
        st8(va_w, va.x, va.y); st8(va_w + 32, va.z, va.w);
        st8(vb_w, vb.x, vb.y); st8(vb_w + 32, vb.z, vb.w);
      } else {
        v4u pos, neg;
        hedgehog(p0, p1, keep, pos, neg);
        st8(pk_w, pos.x, pos.y); st8(pk_w + 32, pos.z, pos.w); st8(pk_w + 64, neg.x, neg.y); st8(pk_w + 96, neg.z, neg.w);
      }
      if (ph == 0) {
        STAMP(21);
        if (u == 0) { VMW(7); stg(rws, h_off + (OFF_HS - OFF_H), SENT); stg(rws, w < 3 ? h_off + (OFF_HS - OFF_H) + 4 * KB : OOB, SENT); }
        bar();
        STAMP(22);
        if (w == 0) post_t(OFF_HINT + (5 * 256 + mi * 32 + yg) * 64);
      }
    }
    if (w == 3) peek4_issue(rws, uf_off);
  }
  STAMP(23);
  bar();
  STAMP(4);
  if (w == 7) peek_issue(rws, h32 + 5 * 256 * 64);
  {
    v2u a0[2], a1[2], b0[2], b1[2], d0[4], d1[4];
#pragma unroll
    for (int s = 0; s < 2; ++s) {
      a0[s] = trread(kv_a + s * 32 * PK_RS); a1[s] = trread(kv_a + (s * 32 + 4) * PK_RS);
      d0[s] = trread(kv_b + s * 32 * V_RS); d1[s] = trread(kv_b + (s * 32 + 4) * V_RS);
      b0[s] = trread(kv_a + s * 32 * PK_RS + 32); b1[s] = trread(kv_a + (s * 32 + 4) * PK_RS + 32);
    }
#pragma unroll
    for (int s = 0; s < 2; ++s) { d0[2 + s] = trread(kv_c + s * 32 * V_RS); d1[2 + s] = trread(kv_c + (s * 32 + 4) * V_RS); }
    SB();
    f4 c0 = z4, c1 = z4;
#pragma unroll
    for (int s = 0; s < 4; ++s) {
      const v4u vf = {d0[s].x, d0[s].y, d1[s].x, d1[s].y};
      c0 = mfma(v4u{a0[s & 1].x, a0[s & 1].y, a1[s & 1].x, a1[s & 1].y}, vf, c0);
      c1 = mfma(v4u{b0[s & 1].x, b0[s & 1].y, b1[s & 1].x, b1[s & 1].y}, vf, c1);
    }
    stg(rws, kvp_off, nosent4(pack8(c0, c1)));
    SB();
  }
  STAMP(28);
  u32 t3 = 0;
  if (w == 7) t3 = now();
  VMW(1);
  if (w == 3) peek4_take(rws, uf_off, tag);
  bar();
  STAMP(29);
  if (w == 7) {
    __builtin_amdgcn_s_setprio(3);
    const u32 tl = peek_take<1>(rws, h32 + 5 * 256 * 64, tag);
    wait_until(tl + (t3 - __builtin_amdgcn_readlane(tl, yg) + DTX));
    dma_ws<2>(rws, l16, OFF_KVP + (mi * 32 + yg) * 8 * KB, L + L_KP);
    STAMP(6);
  }
  u32 h8 = OFF_HINT + (yg * 8 + (lane & 7)) * 64;
  PIN(h8);
  f4 pq0, pq1;
  {
    f4 a0, a1, b0, b1;
    const u32 qbase = u ? L_QB : L_QA;
    gemm_tr(tr0 + qbase, tr1 + qbase, xa, xb, a0, a1, b0, b1, [&](int s_) AI { if (s_ < QPF) pf(s_); });
    st16(kx_w - (L_KX - L_QX), pack8(b0, b1));
    const v4u qa = pack8(a0, a1);
    pq0 = mfma(phA, qa, z4); pq1 = mfma(phB, qa, z4);
    PIN(pq0); PIN(pq1);
  }
  STAMP(5);
  v4u qpos, qneg;
  auto phi_q = [&]() AI {
    const v4u qb = ld16(kx_r - (L_KX - L_QX));
    hedgehog(mfma(phA, qb, pq0), mfma(phB, qb, pq1), keep, qpos, qneg);
    PIN(qpos); PIN(qneg);
    SB();
  };
  auto make_o = [&]() AI {
    v4u kf[4];
#pragma unroll
    for (int k = 0; k < 4; ++k) kf[k] = ld16(L + L_KF + (hh * 4 + k) * KB + l16);
    SB();
    f4 n1 = mfma(kf[1], qpos, z4), n0 = mfma(kf[0], qpos, z4);
    n1 = mfma(kf[3], qneg, n1); n0 = mfma(kf[2], qneg, n0);
    const float rd = __builtin_amdgcn_rcpf(rsum(padg ? n1[0] : 0.f) + 1e-6f);
    n0 *= rd; n1 *= rd;
    v4u ov = nosent4(pack8(n0, n1));
    if (padg) { ov.z = 0; ov.w = 0; }
    stg(rws, o_off, ov);
    SB();
  };
  const u32 hme = OFF_HINT + (zg * 4 + js) * 64;
  if (w < 7) {
  bar();
  STAMP(7);
  PFR(QPF, PA);
  phi_q();
  STAMP(31);
  PFR(PA, PA2);
  bar();
  STAMP(53);
  if (!OT && w == 0) post(OFF_HINT + (0 * 256 + yg * 8 + mi) * 64);
  make_o();
  bar();
  STAMP(8);
  f4 hold0, hold1;
  const u32 A = L + L_ACT + l16;
  const int tk = zg * 32 + (g & 1) * 16 + i16 + ((js & 1) ? -1 - (js >> 1) : 1 + (js >> 1));
  u32 hs_off = ((((tk & (S_ - 1)) >> 5) * 14 + (n >> 1)) * 2 + ((tk >> 4) & 1)) * KB + cko + (tk & 15) * 16 + OFF_HS;
  u32 hs_keep = (tk < 0 || tk >= S_) ? 0u : 0xffffffffu;
  u32 x1_off = h_off + (OFF_X1 - OFF_H);
  u32 st_r = L + L_ST + i16 * 8;
  PFR(PB, PB + R1);
  PIN(hs_off); PIN(hs_keep); PIN(x1_off); PIN(st_r);
  f4 a0 = {blo(xr[0].x), bhi(xr[0].x), blo(xr[0].y), bhi(xr[0].y)};
  f4 a1 = {blo(xr[1].x), bhi(xr[1].x), blo(xr[1].y), bhi(xr[1].y)};
  bar();
  stg(rws, x1_off, SENT);
  PFR(PB + R1, PC);
  PIN(a0); PIN(a1);
  bar();
  if (w == 0) post_t(hme + 1 * 256 * 64);
  STAMP(32);
  {
    gemm_alds<16>(A, wo_, a0, a1, PFS(PC, NL));
    STAMP(33);
    const v4u p = nosent4(pack8(a0, a1));
    stg(rws, h_off, to_chunk(p));
    asm volatile("s_waitcnt vmcnt(%0)" :: "n"(NL + 1) : "memory");
    SB();
    hold0 = f4{blo(p.x), bhi(p.x), blo(p.y), bhi(p.y)};
    hold1 = f4{blo(p.z), bhi(p.z), blo(p.w), bhi(p.w)};
  }
  bar();
  STAMP(9);
  f4 xb0, xb1;
  {
    PFR(PC + NL, PC + NL + R1);
    bar();
    PFR(PC + NL + R1, PD);
    f4 cs = z4;
#pragma unroll
    for (int s = 0; s < 14; ++s) cs = mfma(w1_[s], ones, cs);
    PIN(cs);
    bar();
    if (w == 0) post_t(hme + 2 * 256 * 64);
    STAMP(34);
    a0 = z4; a1 = z4;
    gemm_alds<14>(A, w1_, a0, a1, PFS(PD, NL));
    STAMP(35);
    for (u32 sp = 0; sp < (1u << 20) && __builtin_amdgcn_readfirstlane(*(volatile LAS u32*)(uintptr_t)(L + L_SF)) == 0; ++sp) { }
    STAMP(36);
    const f2 s0 = ldf2(st_r), s1 = ldf2(st_r + 128), e0 = ldf2(st_r + 256), e1 = ldf2(st_r + 384);
#pragma unroll
    for (int r = 0; r < 4; ++r) {
      a0[r] = gelu(fmaf(a0[r], s0[1], s0[0] * cs[r]));
      a1[r] = gelu(fmaf(a1[r], s1[1], s1[0] * cs[r]));
    }
    stg(rws, hs_off, to_chunk(nosent4(pack8(a0, a1))) & hs_keep);
    bar();
#pragma unroll
    for (int r = 0; r < 4; ++r) { xb0[r] = fmaf(hold0[r], e0[1], e0[0]); xb1[r] = fmaf(hold1[r], e1[1], e1[0]); }
    PIN(xb0); PIN(xb1);
  }
  STAMP(10);
  PFR(PD + NL, PD + NL + R1);
  bar();
  PFR(PD + NL + R1, PE);
  bar();
  if (w == 0) post_t(hme + 3 * 256 * 64);
  STAMP(37);
  {
    gemm_alds<14>(A, w2_, xb0, xb1, PFS(PE, NL));
    STAMP(38);
    const v4u p = nosent4(pack8(xb0, xb1));
    stg(rws, x1_off, to_chunk(p));
    bar();
    hold0 += f4{blo(p.x), bhi(p.x), blo(p.y), bhi(p.y)};
    hold1 += f4{blo(p.z), bhi(p.z), blo(p.w), bhi(p.w)};
    PIN(hold0); PIN(hold1);
  }
  STAMP(11);
  constexpr int W3E = (PE + NL + R1) & ~1;
  PFR(PE + NL, W3E);
  u32 hid_w = L + L_HID + w * 2 * KB + l16, hid_r = L + L_HID + l16;
  u32 rs_w = l16 + OFF_RS + ((yg * 64 + mi) * 7 + w) * KB;
  u32 out_off = (zg * 32 + (g & 1) * 16 + i16) * 896 + (n * 16 + (g >> 1) * 8) * 2;
  PIN(hid_w); PIN(hid_r); PIN(rs_w); PIN(out_off);
  constexpr int CSE = (PE + NL - 44) / 2 < 14 ? (PE + NL - 44) / 2 : 14;
  f4 cs[2] = {z4, z4};
#pragma unroll
  for (int s = 0; s < CSE; ++s) { cs[0] = mfma(w3f[s], ones, cs[0]); cs[1] = mfma(w3f[14 + s], ones, cs[1]); }
  PIN(cs[0]); PIN(cs[1]);
  bar();
  dma_ws<1>(rws, l16, OFF_X1 + (w < 3 ? (zg ^ 1) * 28 + 16 + w * 4 : zg * 28 + w * 4) * KB, L + L_ACT + (w < 3 ? 16 + w * 4 : 28 + w * 4) * KB);
  STAMP(44);
  bar();
  STAMP(12);
  f4 pu[4][2] = {{z4, z4}, {z4, z4}, {z4, z4}, {z4, z4}};
  f4 sv[2];
  float fr0, fr1;
  u32 s5_r = L + L_S5 + i16 * 16;
  PIN(s5_r);
  float gy[2], gt[2];
  v4u gp;
  auto mop = [&](int t, int j) AI {
    const int pr = j / 17, o = j % 17, e = o & 1, c = pr >> 1, r = (pr & 1) * 2 + e;
    const f4& q = sv[t & 1];
    if (j == 68) st16(hid_w + (t >> 1) * 14 * KB + (t & 1) * KB, gp);
    else if (o < 2) gy[e] = fmaf(q[0], cs[c][r], pu[t][c][r]);
    else if (o < 4) gt[e] = gy[e] * gy[e];
    else if (o < 6) gt[e] = fmaf(gt[e], q[2], q[1]);
    else if (o < 8) gt[e] = gt[e] * gy[e];
    else if (o < 10) gt[e] = __builtin_amdgcn_exp2f(gt[e]);
    else if (o < 12) gt[e] = gt[e] + 1.0f;
    else if (o < 14) gt[e] = __builtin_amdgcn_rcpf(gt[e]);
    else if (o < 16) { gy[e] = gy[e] * gt[e]; if (e == 0) PIN(gy[0]); }
    else gp[pr] = pk2(gy[0], gy[1]);
  };
#define MOPS(T0, NOPS, m, NM) sfor<(m) * (NOPS) / (NM), ((m) + 1) * (NOPS) / (NM)>([&](auto J_) AI { mop((T0) + J_.v / 69, J_.v % 69); SB(); })
  constexpr int G0 = W3E, N3 = G0 < 72 ? 72 - G0 : 0, K3 = 4 + N3 * 3 / 2, GB = G0 + N3 + FA;
  v4u hf[14];
  {
    constexpr int P = 6;
    v4u bq[P];
    auto fa = [&](int k) AI { return A + (k < 12 ? k : k < 24 ? 28 + k - 12 : k < 32 ? 2 * (k - 18) : k < 40 ? 2 * (k - 26) + 1 : k) * KB; };
#pragma unroll
    for (int k = 0; k < P; ++k) bq[k] = ld16(fa(k));
    SB();
    sfor<0, 56>([&](auto K) AI {
      constexpr int k = K.v;
      constexpr int t = k < 12 ? k & 1 : k < 24 ? 2 + (k & 1) : k < 32 ? 0 : k < 40 ? 1 : 2 + (k & 1);
      constexpr int s = k < 12 ? k >> 1 : k < 24 ? (k - 12) >> 1 : k < 32 ? k - 18 : k < 40 ? k - 26 : (k - 28) >> 1;
      sfor<0, 2>([&](auto C) AI {
        constexpr int c = C.v;
        pu[t][c] = mfma(w3f[c * 14 + s], bq[k % P], pu[t][c]);
        SB();
        if constexpr (k >= 33 && k < 50) MOPS(0, 138, (k - 33) * 2 + c, 34);
      });
      if constexpr (t == 0 && s >= CSE) { cs[0] = mfma(w3f[s], ones, cs[0]); cs[1] = mfma(w3f[14 + s], ones, cs[1]); SB(); }
      if constexpr (k == 6 || k == 18) asm volatile("s_barrier" ::: "memory");
      if constexpr (k == 11) { asm volatile("s_waitcnt vmcnt(%0)\n\ts_barrier" :: "n"(N3 ? (K3 < 11 ? N3 + (11 - K3) * FA / (56 - K3) : 7 * 2 / 3) : 7 * FA / 52) : "memory"); }
      if constexpr (k == 24) STAMP(48);
      if constexpr (k == 31) { asm volatile("s_barrier" ::: "memory"); STAMP(49); sv[0] = __builtin_bit_cast(f4, ld16(s5_r)); sv[1] = __builtin_bit_cast(f4, ld16(s5_r + 256)); }
      if constexpr (k == 50) { STAMP(50); asm volatile("s_waitcnt lgkmcnt(0)\n\ts_barrier" ::: "memory"); }
      if constexpr (k + P < 56) bq[k % P] = ld16(fa(k + P));
      if constexpr (k >= 50) sfor<(k - 50) * 14 / 6, (k - 49) * 14 / 6>([&](auto E) AI { hf[E.v] = ld16(hid_r + E.v * KB); });
      if constexpr (k >= 4 && k < K3) sfor<(k - 4) * 2 / 3, (k - 3) * 2 / 3>([&](auto E) AI { pf(G0 + E.v); });
      if constexpr (k >= K3) sfor<(k - K3) * FA / (56 - K3), (k + 1 - K3) * FA / (56 - K3)>([&](auto E) AI { pf(G0 + N3 + E.v); });
      SB();
    });
  }
  STAMP(39);
  f4 own0, own1;
  {
    v4u pend[2];
    auto rs_off = [&](int rd) AI { return (rs_w | NORS) + ((((rd >> 2) ? th : th ^ 1) * 4 + ((js + 1 + rd) & 3)) * 56) * KB; };
    sv[0] = __builtin_bit_cast(f4, ld16(s5_r + 512)); sv[1] = __builtin_bit_cast(f4, ld16(s5_r + 768));
    fr0 = sv[0][3]; fr1 = sv[1][3];
    sfor<0, 8>([&](auto RD) AI {
      constexpr int rd = RD.v, sl = rd & 3;
      f4 c0 = z4, c1 = z4;
      sfor<0, 7>([&](auto C) AI {
        constexpr int c = C.v;
        c1 = mfma(w4f[sl * 7 + c], hf[2 * c + 1], c1);
        SB();
        if constexpr (rd < 3 && rd + c > 0) MOPS(2, 138, (rd * 7 + c) * 2 - 2, 40);
        c0 = mfma(w4f[sl * 7 + c], hf[2 * c], c0);
        SB();
        if constexpr (rd < 3 && rd + c > 0) MOPS(2, 138, (rd * 7 + c) * 2 - 1, 40);
        if constexpr (rd == 3 && c == 0) { STAMP(51); asm volatile("s_waitcnt lgkmcnt(0)\n\ts_barrier" ::: "memory"); }
        if constexpr (rd == 4 && c == 0) STAMP(52);
        if constexpr (rd == 3) { hf[2 * c] = ld16(hid_r + (14 + 2 * c) * KB); hf[2 * c + 1] = ld16(hid_r + (15 + 2 * c) * KB); }
        if constexpr (rd < 2) sfor<(rd * 7 + c) * (100 - GB) / 14, (rd * 7 + c + 1) * (100 - GB) / 14>([&](auto E) AI { pf(GB + E.v); });
        if constexpr (c == 1 && rd == 1) pend[0] = pack8(own0, own1);
        if constexpr (c == 1 && rd >= 2) stg(rws, rs_off(rd - 1), pack8(own0, own1));
        if constexpr (c == 4 && rd == 2) stg(rws, rs_off(0), pend[0]);
        if constexpr (rd == RF && c == 0) asm volatile("s_waitcnt vmcnt(%0)\n\ts_barrier" :: "n"(RF - 5) : "memory");
        SB();
      });
      own0 = c0; own1 = c1;
    });
    PIN(own0); PIN(own1);
  }
  static_assert(GB >= 86, "");
  STAMP(42);
  VMW(0);
  bar();
  STAMP(16);
  const u32 one = g != q4 ? 0u : (p4 & 1) ? 0x3f800000u : 0x00003f80u;
  v4u sl0 = {p4 < 2 ? one : 0u, p4 < 2 ? 0u : one, 0u, 0u}, sl1 = {0u, 0u, sl0.x, sl0.y};
  PIN(sl0); PIN(sl1);
  u32 rs_r[3];
#pragma unroll
  for (int k = 0; k < 3; ++k) { rs_r[k] = OFF_RS + (((yg * 8 + mi) * 8 + th * 4 + ((js + 1 + k) & 3)) * 7 + w) * KB; PINS(rs_r[k]); }
  bar();
  STAMP(43);
  {
    v4u pp[7];
#pragma unroll
    for (int k = 0; k < 3; ++k) pp[k] = ldg(rws, l16, rs_r[k]);
#pragma unroll
    for (int k = 0; k < 4; ++k) pp[3 + k] = ld16(A + (k * 7 + w) * KB);
    SB();
#pragma unroll
    for (int k = 6; k >= 3; --k) { own0 = mfma(sl0, pp[k], own0); own1 = mfma(sl1, pp[k], own1); }
    SB();
#pragma unroll
    for (int k = 0; k < 3; ++k) { own0 = mfma(sl0, pp[k], own0); own1 = mfma(sl1, pp[k], own1); }
#pragma unroll
    for (int r = 0; r < 4; ++r) { hold0[r] = fmaf(own0[r], fr0, hold0[r]); hold1[r] = fmaf(own1[r], fr1, hold1[r]); }
    *(GAS v4u*)(p_out + out_off) = to_chunk(pack8(hold0, hold1));
  }
  STAMP(17);
  STAMP_END();
  return;
  }
  {
    {
      const u32 one = (i16 & 1) ? 0x3f800000u : 0x00003f80u;
      v4u sel = {(i16 >> 1) == 0 ? one : 0u, (i16 >> 1) == 1 ? one : 0u, (i16 >> 1) == 2 ? one : 0u, (i16 >> 1) == 3 ? one : 0u};
      PIN(sel);
      f4 acc;
      RETRY_LOOP {
        VMW(0);
        v4u kp[8];
#pragma unroll
        for (int k = 0; k < 8; ++k) kp[k] = ld16(L + L_KP + k * KB + l16);
        SB();
        acc = z4;
        u32 m = 0;
#pragma unroll
        for (int k = 0; k < 8; ++k) { acc = mfma(sel, kp[k], acc); m = max4(m, kp[k]); }
        if (__all(m != 0xffffffffu)) break;
        RETRY_END(1);
        dma_ws<2>(rws, l16, OFF_KVP + (mi * 32 + yg) * 8 * KB, L + L_KP);
      }
      __builtin_amdgcn_raw_buffer_store_b64(v2u{nosent(pk2(acc[0], acc[1])), nosent(pk2(acc[2], acc[3]))}, rws, (int)kvt8, 0, SC1);
      STAMP(30);
      bar();
      __builtin_amdgcn_s_sleep(DLYB);
      dma_ws<2>(rws, l16, OFF_KVT + mi * 8 * KB, L + L_KF);
      if (OT) post_t(OFF_HINT + (0 * 256 + yg * 8 + mi) * 64);
      STAMP(45);
      phi_q();
      STAMP(31);
      f4 sm[4];
      RETRY_LOOP {
        if (OT) VMW(1); else VMW(0);
        if (g_check<0, 2, -1, false>(L + L_KF + l16, sm)) break;
        RETRY_END(2);
        dma_ws<2>(rws, l16, OFF_KVT + mi * 8 * KB, L + L_KF);
        if (OT) post_t(OFF_HINT + (0 * 256 + yg * 8 + mi) * 64);
      }
      bar();
      STAMP(53);
      if (OT) peek_issue(rws, h8);
      make_o();
      bar();
      STAMP(8);
    }
    const u32 P = L + L_ACT + l16;
    f4 sm[4];
#define GATHER(NK, ST, SRC, ID) \
      RETRY_LOOP { \
        dma_ws<NK / 4>(rws, l16, SRC, L + L_ACT); \
        if (sp == 0) bar(); \
        if (ST) { sm[0] = z4; sm[1] = z4; sm[2] = z4; sm[3] = z4; } \
        if (g_check<0, 3, NK - 12, ST>(P, sm)) break; \
        RETRY_END(ID); \
        VMW(0); \
      } \
      bar(); \
      RETRY_LOOP { \
        f4 sn[4] = {sm[0], sm[1], sm[2], sm[3]}; \
        const bool ok = g_check<3, NK / 4, 0, ST>(P, sn); \
        if (ok) { sm[0] = sn[0]; sm[1] = sn[1]; sm[2] = sn[2]; sm[3] = sn[3]; break; } \
        RETRY_END(ID); \
        dma_ws<NK / 4 - 3>(rws, l16, (SRC) + 12 * KB, L + L_ACT + 12 * KB); \
      } \
      bar()
    {
      if (OT) pace<1, DT0>(rws, h8, tag, mi);
      else poll(rws, h8, tag);
    }
    STAMP(9);
    { GATHER(32, false, OFF_O + zg * 32 * KB, 3); }
    STAMP(10);
    u32 hA = OFF_HINT + (1 * 256 + zg * 4 + (lane & 3)) * 64;
    u32 hB = OFF_HINT + (2 * 256 + ((lane & 4) ? ((lane & 1) ? zg + 1 : zg + 63) & 63 : zg) * 4 + (lane & 3)) * 64;
    u32 st_w = L + (g == q4 ? L_ST : L_QB) + i16 * 8;
    PIN(hA); PIN(hB); PIN(st_w);
    *(LAS u32*)(uintptr_t)(L + L_SF) = 0;
    poll(rws, OFF_HINT + (0 * 256 + (((js & 1) ? zg + 63 : zg + 1) & 63) * 4 + js) * 64, tag);
    peek_issue(rws, hA);
    STAMP(32);
    bar();
    pace<0, DT1>(rws, hA, tag, js);
    STAMP(11);
    {
      GATHER(28, true, OFF_H + zg * 28 * KB, 4);
        STAMP(12);
      const float m0 = sm[0][0] * (1.0f / D_), m1 = sm[1][0] * (1.0f / D_);
      const float v0 = fmx(fmaf(-m0, m0, diag(sm[2], p4) * (1.0f / D_)), 0.f), v1 = fmx(fmaf(-m1, m1, diag(sm[3], p4) * (1.0f / D_)), 0.f);
      const float r0 = __builtin_amdgcn_rsqf(v0 + 1e-5f), r1 = __builtin_amdgcn_rsqf(v1 + 1e-5f);
      const float t0 = r0 * __builtin_amdgcn_rsqf(v0 * r0 * r0 + 1e-5f), t1 = r1 * __builtin_amdgcn_rsqf(v1 * r1 * r1 + 1e-5f);
      { st8(st_w, f2u(-m0 * t0), f2u(t0)); st8(st_w + 128, f2u(-m1 * t1), f2u(t1)); st8(st_w + 256, f2u(-m0 * r0), f2u(r0)); st8(st_w + 384, f2u(-m1 * r1), f2u(r1)); }
      asm volatile("s_waitcnt lgkmcnt(0)" ::: "memory");
      *(volatile LAS u32*)(uintptr_t)(L + L_SF) = 1;
    }
    STAMP(33);
    peek_issue(rws, hB);
    bar();
    pace<0, DT2>(rws, hB, tag, js);
    STAMP(13);
    { GATHER(28, false, OFF_HS + zg * 28 * KB, 5); }
    STAMP(14);
    u32 h83 = h8 + 3 * 256 * 64;
    PIN(h83);
    peek_issue(rws, h83);
    bar();
    pace<0, DT1>(rws, h83, tag, mi);
    STAMP(15);
    {
      const u32 s5_w = L + (g == q4 ? L_S5 : L_QB + 4 * KB) + i16 * 16;
      auto stats = [&](u32 dst) AI {
        const float m0 = sm[0][0] * (1.0f / D_), m1 = sm[1][0] * (1.0f / D_);
        const float r0 = __builtin_amdgcn_rsqf(fmx(fmaf(-m0, m0, diag(sm[2], p4) * (1.0f / D_)), 0.f) + 1e-5f);
        const float r1 = __builtin_amdgcn_rsqf(fmx(fmaf(-m1, m1, diag(sm[3], p4) * (1.0f / D_)), 0.f) + 1e-5f);
        {
          st16(dst, v4u{f2u(-m0), f2u(GC0 * r0), f2u(GC1 * r0 * r0 * r0), f2u(r0)});
          st16(dst + 256, v4u{f2u(-m1), f2u(GC0 * r1), f2u(GC1 * r1 * r1 * r1), f2u(r1)});
        }
      };
      const u32 xa_ = OFF_X1 + (zg ^ 1) * 28 * KB, xb_ = OFF_X1 + zg * 28 * KB;
      RETRY_LOOP {
        dma_ws<3>(rws, l16, xa_, L + L_ACT);
        dma_ws<3>(rws, l16, xb_, L + L_ACT + 28 * KB);
        dma_ws<1>(rws, l16, xa_ + 12 * KB, L + L_ACT + 12 * KB);
        if (sp == 0) bar();
        if (g_check<0, 3, 16, false>(P, sm)) break;
        RETRY_END(6);
        VMW(0);
      }
      bar();
      RETRY_LOOP {
        if (g_check<7, 10, 4, false>(P, sm)) break;
        RETRY_END(6);
        dma_ws<3>(rws, l16, xb_, L + L_ACT + 28 * KB);
        VMW(0);
      }
      bar();
      STAMP(16);
      sm[0] = z4; sm[1] = z4; sm[2] = z4; sm[3] = z4;
      g_check<0, 3, -1, true>(P, sm);
      VMW(0);
      bar();
      RETRY_LOOP {
        const bool o1 = g_check<3, 7, -1, false>(P, sm), o2 = g_check<10, 14, -1, false>(P, sm);
        if (o1 && o2) break;
        RETRY_END(7);
        dma_ws<4>(rws, l16, xa_ + 12 * KB, L + L_ACT + 12 * KB);
        dma_ws<4>(rws, l16, xb_ + 12 * KB, L + L_ACT + 40 * KB);
        VMW(0);
      }
      STAMP(35);
      bar();
      g_check<3, 7, -1, true>(P, sm);
      stats(s5_w);
      STAMP(36);
      bar();
      sm[0] = z4; sm[1] = z4; sm[2] = z4; sm[3] = z4;
      g_check<7, 14, -1, true>(P, sm);
      stats(s5_w + 512);
    }
    STAMP(37);
    bar();
    bar();
    bar();
    post(hme + 6 * 256 * 64);
    u32 hO = OFF_HINT + (6 * 256 + (zg ^ 1) * 4 + (lane & 3)) * 64, hS = OFF_HINT + (4 * 256 + zg * 4 + (lane & 3)) * 64;
    PIN(hO); PIN(hS);
    __builtin_amdgcn_s_sleep(RF == 6 ? 8 : 4);
    peek_issue(rws, hO);
    bar();
    post(hme + 4 * 256 * 64);
    peek_take<1>(rws, hO, tag);
    STAMP(54);
    dma_ws<7>(rws, l16, OFF_RS + ((yg * 8 + mi) * 8 + (th ^ 1) * 4) * 7 * KB, L + L_ACT);
    poll(rws, hS, tag);
    STAMP(17);
    bar();
    if (b == 0) __builtin_amdgcn_raw_buffer_store_b32(epoch + 1, rws, (int)vz, (int)OFF_E, SC1);
    STAMP_END();
  }
}

extern "C" void kernel_launch(void* const* d_in, const int* in_sizes, int n_in, void* d_out, int out_size, void* d_ws, size_t ws_size,
                              hipStream_t stream) {
  fused_kernel<<<256, 512, 0, stream>>>((const u16*)d_in[0], (const u16*)d_in[1], (const u16*)d_in[2], (const u16*)d_in[3],
                                        (const u16*)d_in[5], (const u16*)d_in[6], (const u16*)d_in[7], (const u16*)d_in[8],
                                        (u16*)d_out, (unsigned char*)d_ws);
}
```

```cpp
#include <hip/hip_runtime.h>
#include <stdint.h>

typedef unsigned int u32;
typedef unsigned long long u64;
typedef unsigned short u16;
typedef u32 v4u __attribute__((ext_vector_type(4)));
typedef u32 v2u __attribute__((ext_vector_type(2)));
typedef float f4 __attribute__((ext_vector_type(4)));
typedef float f2 __attribute__((ext_vector_type(2)));
typedef __bf16 bf8 __attribute__((ext_vector_type(8)));
typedef __bf16 bf2 __attribute__((ext_vector_type(2)));
typedef short s4v __attribute__((ext_vector_type(4)));
typedef __amdgpu_buffer_rsrc_t rsrc_t;
#define GAS __attribute__((address_space(1)))
#define LAS __attribute__((address_space(3)))
#define DI __device__ __forceinline__
#define AI __attribute__((always_inline))
#define RLX_AGENT __ATOMIC_RELAXED, __HIP_MEMORY_SCOPE_AGENT

constexpr int S_ = 2048, D_ = 448, F_ = 1792;
constexpr u32 KB = 1024;
constexpr u32 OFF_E = 0, OFF_TMO = 64;
constexpr u32 OFF_HINT = 16 * KB;
constexpr u32 OFF_KVT = 192 * KB;
constexpr u32 OFF_WO = 256 * KB;
constexpr u32 OFF_W1 = OFF_WO + 448 * KB;
constexpr u32 OFF_W2 = OFF_W1 + 392 * KB;
constexpr u32 OFF_W3 = OFF_W2 + 392 * KB;
constexpr u32 OFF_W4 = OFF_W3 + 1568 * KB;
constexpr u32 OFF_KVP = OFF_W4 + 1568 * KB;
constexpr u32 OFF_O = OFF_KVP + 2048 * KB;
constexpr u32 OFF_H = OFF_O + 2048 * KB;
constexpr u32 OFF_HS = OFF_H + 1792 * KB;
constexpr u32 OFF_X1 = OFF_HS + 1792 * KB;
constexpr u32 OFF_RS = OFF_X1 + 1792 * KB;
constexpr u32 WS_BYTES = OFF_RS + 256 * 56 * KB;
constexpr u32 OFF_DBG = WS_BYTES;
constexpr u32 L_X = 0, X_RS = 928, SLOT = 16 * X_RS;
constexpr u32 L_VB = 0, L_QA = 28 * KB, L_QX = 0;
constexpr u32 L_K = 64 * X_RS;
constexpr u32 L_PK = L_K, PK_RS = 136;
constexpr u32 L_V0 = L_PK + 128 * PK_RS, V_RS = 72;
constexpr u32 L_QB = L_K + 28 * KB;
constexpr u32 L_VA = L_K + 56 * KB;
constexpr u32 L_U = L_VA;
constexpr u32 L_KX = L_VA + 28 * KB;
constexpr u32 L_V1 = L_KX;
constexpr u32 L_ST = L_KX + 9 * KB;
constexpr u32 L_SF = L_ST + 512, L_DUMP = L_ST + 768;
constexpr u32 L_PHI = L_ST + 1024;
constexpr u32 L_U0 = L_PHI + 4096;
constexpr u32 L_BYTES = L_U0 + 4096;
constexpr u32 L_KP = 8 * KB;
constexpr u32 L_KF = 16 * KB;
static_assert(L_BYTES <= 163840 && L_V0 + 128 * V_RS <= L_QB, "");
constexpr u32 L_ACT = 0;
constexpr u32 L_HID = 56 * KB;
constexpr u32 L_S5 = 84 * KB;
constexpr u32 SPIN_CAP = 1u << 15;
constexpr u32 OOB = 0x80000000u;
constexpr int SC1 = 16;
constexpr u32 CODE_BYTES = 40960;
#define XDMA dma_in
#define DT0 10
#define DT1 10
#define DT2 14
#define R1 3
#define PA 11
#define QPF 5
#define R10 R1
#define R20 4
#define PA2 15
#define DTX 22
#define PB PA2
#define R2 3
#define NL 7
#define PC (PB + R10 + R20)
#define PD (PC + NL + R1 + R2)
#define PE (PD + NL + R1 + R2)
static_assert(PC >= 16 && PC + NL + R1 >= 30 && PE >= 44 && PC + 2 <= 30, "");
#define FA 16
#define FB 14
#define NOW4 0
#define NORS 0
#define GG 2
#define OT 1
#define RF 7
#define HSE 0
#define HM 1
#define OM 0
#define KVC 2200
#define DTF 104
#define LDELAY 3
#define DLYA 1
#define DLYB 2
#define DLY0 1
#define DLY1 3
#define HS1 2

#define SB() __builtin_amdgcn_sched_barrier(0)
#define PIN(x) asm volatile("" : "+v"(x))
#define PINS(x) do { x = __builtin_amdgcn_readfirstlane(x); asm volatile("" : "+s"(x)); } while (0)
#define DIAG 0
#define STAMP(k) do { } while (0)
#define NRETRY(k) do { } while (0)
#define RSTAMP(k) do { } while (0)
#define STAMP_BEGIN() do { } while (0)
#define STAMP_END() do { } while (0)

template <int V> struct IC { static constexpr int v = V; };
template <int I, int N, class F> DI void sfor(F f) { if constexpr (I < N) { f(IC<I>{}); sfor<I + 1, N>(f); } }
DI f4 mfma(v4u a, v4u b, f4 c) {
  return __builtin_amdgcn_mfma_f32_16x16x32_bf16(__builtin_bit_cast(bf8, a), __builtin_bit_cast(bf8, b), c, 0, 0, 0);
}
DI u32 pk2(float a, float b) { f2 v = {a, b}; return __builtin_bit_cast(u32, __builtin_convertvector(v, bf2)); }
DI float blo(u32 u) { return __builtin_bit_cast(float, u << 16); }
DI float bhi(u32 u) { return __builtin_bit_cast(float, u & 0xffff0000u); }
DI u32 f2u(float x) { return __builtin_bit_cast(u32, x); }
DI float u2f(u32 x) { return __builtin_bit_cast(float, x); }
DI u32 nosent(u32 x) { return x < 0xfffffffeu ? x : 0xfffffffeu; }
DI v4u nosent4(v4u v) { return v4u{nosent(v.x), nosent(v.y), nosent(v.z), nosent(v.w)}; }
DI u32 umax(u32 a, u32 b) { return a > b ? a : b; }
DI u32 max4(u32 m, v4u v) { m = umax(umax(m, v.x), v.y); return umax(umax(m, v.z), v.w); }
DI float fmx(float a, float b) { return __builtin_elementwise_maximum(a, b); }
DI float fmn(float a, float b) { return __builtin_elementwise_minimum(a, b); }
DI float rsum(float x) {
  auto a = __builtin_amdgcn_permlane16_swap(f2u(x), f2u(x), false, false); x = u2f(a[0]) + u2f(a[1]);
  auto c = __builtin_amdgcn_permlane32_swap(f2u(x), f2u(x), false, false); return u2f(c[0]) + u2f(c[1]);
}
DI void bar() { SB(); asm volatile("s_waitcnt lgkmcnt(0)\n\ts_barrier" ::: "memory"); SB(); }
#define VMW(n) asm volatile("s_waitcnt vmcnt(" #n ")" ::: "memory")
DI rsrc_t mkrs(u64 p, u32 bytes) { return __builtin_amdgcn_make_buffer_rsrc((GAS void*)p, 0, (int)bytes, 0x00020000); }
DI v4u ldg(rsrc_t rs, u32 voff, u32 soff) { return __builtin_amdgcn_raw_buffer_load_b128(rs, (int)voff, (int)soff, SC1); }
DI void stg(rsrc_t rs, u32 off, v4u v) { __builtin_amdgcn_raw_buffer_store_b128(v, rs, (int)off, 0, SC1); }
template <int N, int DL>
DI void dma_in(rsrc_t rs, u32 voff, u32 soff, u32 ds, u32 lds) {
  asm volatile("s_mov_b32 m0, %4\n\ts_nop 0\n\tbuffer_load_dwordx4 %1, %2, %0 offen lds\n\t"
               ".rept %c5\n\ts_add_u32 m0, m0, %c6\n\ts_add_u32 %0, %0, %3\n\tbuffer_load_dwordx4 %1, %2, %0 offen lds\n\t.endr"
               : "+s"(soff) : "v"(voff), "s"(rs), "s"(ds), "s"(lds), "n"(N - 1), "n"(DL) : "memory", "scc");
}
template <int N, int DL>
DI void dma_nt(rsrc_t rs, u32 voff, u32 soff, u32 ds, u32 lds) {
  asm volatile("s_mov_b32 m0, %4\n\ts_nop 0\n\tbuffer_load_dwordx4 %1, %2, %0 offen nt lds\n\t"
               ".rept %c5\n\ts_add_u32 m0, m0, %c6\n\ts_add_u32 %0, %0, %3\n\tbuffer_load_dwordx4 %1, %2, %0 offen nt lds\n\t.endr"
               : "+s"(soff) : "v"(voff), "s"(rs), "s"(ds), "s"(lds), "n"(N - 1), "n"(DL) : "memory", "scc");
}
DI void touch(rsrc_t rs, u32 voff, u32 lds) {
  asm volatile("s_mov_b32 m0, %2\n\ts_nop 0\n\tbuffer_load_dword %0, %1, 0 offen sc1 lds" :: "v"(voff), "s"(rs), "s"(lds) : "memory");
}
template <int N>
DI void dma_ws(rsrc_t rs, u32 voff, u32 soff, u32 lds) {
#define L4_ "buffer_load_dwordx4 %1, %2, %0 offen sc1 lds\n\tbuffer_load_dwordx4 %1, %2, %0 offen offset:1024 sc1 lds\n\t" \
            "buffer_load_dwordx4 %1, %2, %0 offen offset:2048 sc1 lds\n\tbuffer_load_dwordx4 %1, %2, %0 offen offset:3072 sc1 lds\n\t"
  asm volatile("s_mov_b32 m0, %3\n\ts_nop 0\n\t" L4_
               ".rept %c4\n\ts_add_u32 m0, m0, 0x1000\n\ts_add_u32 %0, %0, 0x1000\n\t" L4_ ".endr"
               : "+s"(soff) : "v"(voff), "s"(rs), "s"(lds), "n"(N - 1) : "memory", "scc");
}
DI void poll(rsrc_t rs, u32 voff, u64 want) {
  u64 t; u32 cnt = SPIN_CAP;
  asm volatile("1:\n\tbuffer_load_dwordx2 %0, %2, %3, 0 offen sc1\n\ts_waitcnt vmcnt(0)\n\tv_cmp_ne_u64 vcc, %4, %0\n\ts_cbranch_vccz 2f\n\t"
               "s_sleep 1\n\ts_sub_u32 %1, %1, 1\n\ts_cbranch_scc0 1b\n2:"
               : "=&v"(t), "+s"(cnt) : "v"(voff), "s"(rs), "s"(want) : "vcc", "scc", "memory");
}
DI u32 now() { u64 t; asm volatile("s_memrealtime %0\n\ts_waitcnt lgkmcnt(0)" : "=s"(t) :: "memory"); return (u32)t; }
DI void peek_issue(rsrc_t rs, u32 voff) {
  asm volatile("buffer_load_dwordx4 v[248:251], %0, %1, 0 offen sc1" :: "v"(voff), "s"(rs) : "v248", "v249", "v250", "v251", "memory");
}
template <int N>
DI u32 peek_take(rsrc_t rs, u32 voff, u64 want) {
  v4u t; u32 cnt = SPIN_CAP;
  asm volatile("s_waitcnt vmcnt(%5)\n1:\n\tv_cmp_ne_u64 vcc, %4, v[248:249]\n\ts_cbranch_vccz 2f\n\t"
               "buffer_load_dwordx4 v[248:251], %2, %3, 0 offen sc1\n\ts_waitcnt vmcnt(0)\n\ts_sub_u32 %1, %1, 1\n\ts_cbranch_scc0 1b\n2:"
               : "=&{v[248:251]}"(t), "+s"(cnt) : "v"(voff), "s"(rs), "s"(want), "n"(N) : "vcc", "scc", "memory");
  return t.z;
}
template <int N, int DT>
DI void pace(rsrc_t rs, u32 voff, u64 want, u32 own) {
  u32 cnt = SPIN_CAP, c2 = 256, a;
  asm volatile("s_memrealtime s[90:91]\n\ts_waitcnt vmcnt(%7)\n1:\n\tv_cmp_ne_u64 vcc, %5, v[248:249]\n\ts_cbranch_vccz 2f\n\t"
               "buffer_load_dwordx4 v[248:251], %3, %4, 0 offen sc1\n\ts_waitcnt vmcnt(0)\n\ts_sub_u32 %0, %0, 1\n\ts_cbranch_scc0 1b\n2:\n\t"
               "v_readlane_b32 %2, v250, %6\n\ts_waitcnt lgkmcnt(0)\n\ts_sub_u32 %2, s90, %2\n\ts_add_u32 %2, %2, %8\n\tv_add_u32 v250, %2, v250\n"
               "3:\n\ts_memrealtime s[90:91]\n\ts_waitcnt lgkmcnt(0)\n\tv_sub_u32 v251, s90, v250\n\tv_cmp_gt_i32 vcc, 0, v251\n\ts_cbranch_vccz 4f\n\t"
               "s_sub_u32 %1, %1, 1\n\ts_cbranch_scc0 3b\n4:"
               : "+s"(cnt), "+s"(c2), "=&s"(a) : "v"(voff), "s"(rs), "s"(want), "s"(own), "n"(N), "n"(DT)
               : "v248", "v249", "v250", "v251", "s90", "s91", "vcc", "scc", "memory");
}
DI void wait_until(u32 when) {
  u64 t; u32 d, cnt = 256;
  asm volatile("1:\n\ts_memrealtime %0\n\ts_waitcnt lgkmcnt(0)\n\tv_sub_u32 %1, s90, %3\n\tv_cmp_gt_i32 vcc, 0, %1\n\ts_cbranch_vccz 2f\n\t"
               "s_sub_u32 %2, %2, 1\n\ts_cbranch_scc0 1b\n2:"
               : "=&{s[90:91]}"(t), "=&v"(d), "+s"(cnt) : "v"(when) : "vcc", "scc", "memory");
}
constexpr float GC0 = -2.0f * 0.7978845608028654f * 1.4426950408889634f, GC1 = GC0 * 0.044715f;
DI float gelu(float x) {
  const float e = __builtin_amdgcn_exp2f(x * fmaf(x * x, GC1, GC0));
  return x * __builtin_amdgcn_rcpf(1.0f + e);
}

DI void hedgehog(f4 p0, f4 p1, float keep, v4u& pos, v4u& neg) {
  const float L2E = 1.4426950408889634f;
  float mx = fmx(fmx(fmx(fmx(p0[0], p0[1]), p0[2]), fmx(fmx(p0[3], p1[0]), p1[1])), fmx(p1[2], p1[3]));
  float nm = -fmn(fmn(fmn(fmn(p0[0], p0[1]), p0[2]), fmn(fmn(p0[3], p1[0]), p1[1])), fmn(p1[2], p1[3]));
  {
    auto a = __builtin_amdgcn_permlane16_swap(f2u(mx), f2u(nm), false, false);
    float t = fmx(u2f(a[0]), u2f(a[1]));
    auto c = __builtin_amdgcn_permlane32_swap(f2u(t), f2u(t), false, false);
    t = fmx(u2f(c[0]), u2f(c[1])) * -L2E;
    auto d = __builtin_amdgcn_permlane16_swap(f2u(t), f2u(t), false, false);
    mx = u2f(d[0]); nm = u2f(d[1]);
  }
  float a[8], c[8];
#pragma unroll
  for (int r = 0; r < 4; ++r) {
    a[r] = __builtin_amdgcn_exp2f(fmaf(p0[r], L2E, mx));
    c[r] = __builtin_amdgcn_exp2f(fmaf(p0[r], -L2E, nm));
    a[4 + r] = __builtin_amdgcn_exp2f(fmaf(p1[r], L2E, mx));
    c[4 + r] = __builtin_amdgcn_exp2f(fmaf(p1[r], -L2E, nm));
  }
  float sa = fmaf((a[4] + a[5]) + (a[6] + a[7]), keep, (a[0] + a[1]) + (a[2] + a[3]));
  float sc = fmaf((c[4] + c[5]) + (c[6] + c[7]), keep, (c[0] + c[1]) + (c[2] + c[3]));
  {
    auto x = __builtin_amdgcn_permlane16_swap(f2u(sa), f2u(sc), false, false);
    float t = u2f(x[0]) + u2f(x[1]);
    auto y = __builtin_amdgcn_permlane32_swap(f2u(t), f2u(t), false, false);
    t = __builtin_amdgcn_rcpf(u2f(y[0]) + u2f(y[1]));
    auto d = __builtin_amdgcn_permlane16_swap(f2u(t), f2u(t), false, false);
    sa = u2f(d[0]); sc = u2f(d[1]);
  }
  const float sa1 = sa * keep, sc1 = sc * keep;
  pos = v4u{pk2(a[0] * sa, a[1] * sa), pk2(a[2] * sa, a[3] * sa), pk2(a[4] * sa1, a[5] * sa1), pk2(a[6] * sa1, a[7] * sa1)};
  neg = v4u{pk2(c[0] * sc, c[1] * sc), pk2(c[2] * sc, c[3] * sc), pk2(c[4] * sc1, c[5] * sc1), pk2(c[6] * sc1, c[7] * sc1)};
}
DI v4u pack8(f4 a, f4 b) { return v4u{pk2(a[0], a[1]), pk2(a[2], a[3]), pk2(b[0], b[1]), pk2(b[2], b[3])}; }
DI v4u to_chunk(v4u p) {
  auto x = __builtin_amdgcn_permlane16_swap(p.x, p.z, false, false);
  auto y = __builtin_amdgcn_permlane16_swap(p.y, p.w, false, false);
  return v4u{x[0], y[0], x[1], y[1]};
}
DI float diag(f4 q, int p4) {
  const float a = q[0], b = q[1], c = q[2], d = q[3];
  const float t0 = (p4 & 1) ? b : a, t1 = (p4 & 1) ? d : c;
  return (p4 & 2) ? t1 : t0;
}
DI v4u ld16(u32 addr) { return *(LAS v4u*)(uintptr_t)addr; }
DI void st16(u32 addr, v4u v) { *(LAS v4u*)(uintptr_t)addr = v; }
DI void st8(u32 addr, u32 a, u32 b) { *(LAS v2u*)(uintptr_t)addr = v2u{a, b}; }
DI f2 ldf2(u32 addr) { return *(LAS f2*)(uintptr_t)addr; }
DI v2u trread(u32 addr) {
  return __builtin_bit_cast(v2u, __builtin_amdgcn_ds_read_tr16_b64_v4i16((LAS s4v*)(uintptr_t)addr));
}
DI float dot2(u32 a, u32 sel, float c) {
  return __builtin_amdgcn_fdot2_f32_bf16(__builtin_bit_cast(bf2, a), __builtin_bit_cast(bf2, sel), c, false);
}

struct TrBuf { v2u r[3][4]; };
DI void tr_open(TrBuf& q, u32 t0, u32 t1) {
#pragma unroll
  for (int k = 0; k < 3; ++k) { q.r[k][0] = trread(t0 + k * 4096); q.r[k][1] = trread(t0 + k * 4096 + 512); q.r[k][2] = trread(t1 + k * 4096); q.r[k][3] = trread(t1 + k * 4096 + 512); }
  SB();
}
template <class PF>
DI void gemm_tr(TrBuf& q, u32 t0, u32 t1, const v4u (&xa)[7], const v4u (&xb)[7], f4& a0, f4& a1, f4& b0, f4& b1, PF pf) {
  constexpr int P = 3;
  a0 = f4{0, 0, 0, 0}; a1 = a0; b0 = a0; b1 = a0;
#pragma unroll
  for (int s = 0; s < 7; ++s) {
    const int k = s % P;
    const v4u f0 = {q.r[k][0].x, q.r[k][0].y, q.r[k][1].x, q.r[k][1].y}, f1 = {q.r[k][2].x, q.r[k][2].y, q.r[k][3].x, q.r[k][3].y};
    b1 = mfma(f1, xb[s], b1); b0 = mfma(f0, xb[s], b0);
    a1 = mfma(f1, xa[s], a1); a0 = mfma(f0, xa[s], a0);
    SB();
    if (s + P < 7) {
      q.r[k][0] = trread(t0 + (s + P) * 4096); q.r[k][1] = trread(t0 + (s + P) * 4096 + 512);
      q.r[k][2] = trread(t1 + (s + P) * 4096); q.r[k][3] = trread(t1 + (s + P) * 4096 + 512);
    }
    pf(s);
    SB();
  }
}
template <int NS, class PF>
DI void gemm_alds(u32 p, const v4u (&wf)[NS], f4& a0, f4& a1, PF pf) {
  constexpr int P = 6;
  v4u b0[P], b1[P];
#pragma unroll
  for (int k = 0; k < P; ++k) { b0[k] = ld16(p + 2 * k * KB); b1[k] = ld16(p + (2 * k + 1) * KB); }
  SB();
#pragma unroll
  for (int s = 0; s < NS; ++s) {
    a1 = mfma(wf[s], b1[s % P], a1);
    a0 = mfma(wf[s], b0[s % P], a0);
    SB();
    if (s == 2) {
      asm volatile("s_barrier" ::: "memory");
#pragma unroll
      for (int k = 0; k < 3; ++k) { b0[k] = ld16(p + 2 * (k + P) * KB); b1[k] = ld16(p + (2 * (k + P) + 1) * KB); }
    }
    if (s > 2 && s + P < NS) { b0[s % P] = ld16(p + 2 * (s + P) * KB); b1[s % P] = ld16(p + (2 * (s + P) + 1) * KB); }
    pf(s);
    SB();
  }
}
template <int J0, int J1, int EXTRA, bool ST>
DI bool g_check(u32 p, f4 (&sm)[4]) {
  const v4u ones = {0x3f803f80u, 0x3f803f80u, 0x3f803f80u, 0x3f803f80u};
  constexpr int LA = 2;
  v4u fr[4 * (J1 - J0)];
  u32 m = 0;
  sfor<J0, J1 + LA>([&](auto J) AI {
    constexpr int j = J.v;
    if constexpr (j < J1) {
      if constexpr (EXTRA >= 0) asm volatile("s_waitcnt vmcnt(%0)" :: "n"(EXTRA + 4 * (J1 - 1 - j)) : "memory");
#pragma unroll
      for (int e = 0; e < 4; ++e) fr[4 * (j - J0) + e] = ld16(p + (4 * j + e) * KB);
      SB();
    }
    constexpr int d = J0 == 0 ? 1 : LA;
    if constexpr (j >= J0 + d && j < J1 + d) {
#pragma unroll
      for (int e = 3; e >= 0; --e) {
        const v4u f = fr[4 * (j - d - J0) + e];
        if (ST) { sm[e & 1] = mfma(ones, f, sm[e & 1]); sm[2 + (e & 1)] = mfma(f, f, sm[2 + (e & 1)]); }
        else m = max4(m, f);
      }
      SB();
    }
  });
  if (ST) {
    const float t = sm[0][0] + sm[1][0];
    if (__all(t == t)) return true;
#pragma unroll
    for (int k = 0; k < 4 * (J1 - J0); ++k) m = max4(m, fr[k]);
  }
  return __all(m != 0xffffffffu);
}
#define RETRY_LOOP _Pragma("clang loop unroll(disable)") for (u32 sp = 0;; ++sp)
#define RETRY_END(k) NRETRY(k); if (sp > SPIN_CAP) break; asm volatile("s_waitcnt lgkmcnt(0)" ::: "memory")

__global__ void __launch_bounds__(512, 2)
fused_kernel(const u16* x_, const u16* wqkv_, const u16* wphi_, const u16* wo_a, const u16* w1_a, const u16* w2_a, const u16* w3_a,
             const u16* w4_a, u16* out_, unsigned char* ws_) {
  u64 p_x, p_q, p_phi, p_out, p_ws;
  asm volatile("s_load_dwordx2 %0, %5, 0\n\ts_load_dwordx2 %1, %5, 8\n\ts_load_dwordx2 %4, %5, 0x48\n\ts_load_dwordx2 %2, %5, 0x10\n\ts_load_dwordx2 %3, %5, 0x40"
               : "=&s"(p_x), "=&s"(p_q), "=&s"(p_phi), "=&s"(p_out), "=&s"(p_ws) : "s"(__builtin_amdgcn_kernarg_segment_ptr()));
  __shared__ __attribute__((aligned(1024))) unsigned char lds_raw[L_BYTES];
  const u32 L = (u32)(uintptr_t)(LAS unsigned char*)lds_raw;
  const int tid = threadIdx.x, lane = tid & 63, w = __builtin_amdgcn_readfirstlane(tid >> 6), g = lane >> 4, i16 = lane & 15;
  const int b = blockIdx.x, xcd = b & 7, ii = b >> 3;
  const int zg = xcd * 8 + (ii >> 2), js = ii & 3;
  const int yg = xcd * 4 + (ii >> 3), mi = ii & 7, ms = (mi + (ii >> 3)) & 7;
  const int th = (ii >> 2) & 1;
  const u32 l16 = lane * 16;
  STAMP_BEGIN();
  const int u = w >> 2, hh = w & 1, T = (w & 2) + (hh ^ u), head = mi * 2 + hh;
  if (w == 4) {
    u64 pc; asm volatile("s_getpc_b64 %0" : "=s"(pc));
    touch(mkrs(pc & ~255ull, CODE_BYTES), (ii + 32 * lane) * 128, L + L_DUMP);
  }
  u32 xs0 = (yg * 64 + w * 16 + (mi & 1) * 8) * 896, xl0 = L + L_X + w * SLOT + (mi & 1) * 8 * X_RS;
  u32 xs1 = (yg * 64 + w * 16 + (~mi & 1) * 8) * 896, xl1 = L + L_X + w * SLOT + (~mi & 1) * 8 * X_RS;
  const u32 tn = w == 5 ? 37 : 4, tb = w == 5 ? 0 : OFF_HINT;
  u32 tv = (tb + (b * tn + lane) * 128) | ((u32)lane < tn ? 0 : OOB);
  PINS(xs0); PINS(xl0); PINS(xs1); PINS(xl1); PIN(tv);
  SB();
  asm volatile("s_waitcnt lgkmcnt(0)" : "+s"(p_x), "+s"(p_q), "+s"(p_phi), "+s"(p_out), "+s"(p_ws));
  const rsrc_t rx = mkrs(p_x, S_ * D_ * 2);
  const rsrc_t rq = mkrs(p_q, D_ * 3 * D_ * 2);
  const rsrc_t rws = mkrs(p_ws, WS_BYTES);
  if (u == 0) {
    __builtin_amdgcn_s_setprio(2);
    asm volatile("s_bfm_b64 exec, 56, 0" ::: "memory");
    XDMA<8, X_RS>(rx, l16, xs0, 896, xl0);
    XDMA<8, X_RS>(rx, l16, xs1, 896, xl1);
    asm volatile("s_mov_b64 exec, -1" ::: "memory");
  } else if (w == 5) {
    __builtin_amdgcn_s_setprio(3);
    touch(rq, tv, L + L_DUMP);
    __builtin_amdgcn_s_setprio(0);
  } else if (w == 6) {
    __builtin_amdgcn_s_setprio(3);
    touch(rws, tv, L + L_DUMP);
    __builtin_amdgcn_s_setprio(0);
  }
  SB();
  const u32 wc = (lane & 7) ^ ((lane >> 3) & 2) ^ ((w & 1) << 2);
  u32 wv = (((w & 3) * 8 + (lane >> 3)) * 2688 + mi * 112 + wc * 16) | (wc < 7 ? 0 : OOB);
  u32 wl = L + (w & 3) * KB;
  PIN(wv); PINS(wl);
  SB();
  STAMP(24);
  u32 epoch;
  asm volatile("s_load_dword %0, %1, 0" : "=s"(epoch) : "s"(p_ws));
  const v4u SENT = {0xffffffffu, 0xffffffffu, 0xffffffffu, 0xffffffffu};
  const v4u ones = {0x3f803f80u, 0x3f803f80u, 0x3f803f80u, 0x3f803f80u};
  const f4 z4 = {0, 0, 0, 0};
  const int q4 = i16 >> 2, p4 = i16 & 3;
  const bool padg = g == 3;
  u32 vz = lane == 0 ? 0 : OOB;
  PIN(vz);
  auto post = [&](u32 off) AI { __builtin_amdgcn_raw_buffer_store_b64(v2u{epoch, 0x9e3779b9u}, rws, (int)vz, (int)off, SC1); };
  auto post_t = [&](u32 off) AI { const u32 t = now(); stg(rws, vz + off, v4u{epoch, 0x9e3779b9u, t, 0}); return t; };
  const int n = js * 7 + (w < 7 ? w : 0);
  u32 kvp_off = OFF_KVP + ((mi * 8 + w) * 4 + g) * 8 * KB + yg * 256 + i16 * 16;
  u32 kvt_off = g == 0 ? OFF_KVT + (mi * 32 + yg) * 256 + i16 * 16 : OOB;
  u32 kvt8 = g < 2 ? OFF_KVT + (mi * 32 + yg) * 256 + i16 * 16 + g * 8 : OOB;
  PIN(kvt8);
  u32 o_off = l16 + OFF_O + (((yg * 2 + (T >> 1)) * 16 + head) * 2 + (T & 1)) * KB;
  const u32 cko = (2 * (n & 1) + (g >> 1)) * 256;
  u32 h_off = ((zg * 14 + (n >> 1)) * 2 + (g & 1)) * KB + cko + i16 * 16 + OFF_H;
  PIN(h_off); PIN(kvp_off); PIN(o_off);
  PIN(kvt_off);
  float keep = padg ? 0.f : 1.f;
  const u32 trx = (((q4 >> 1) & 1) | ((g & 1) << 1)) * 32;
  u32 tr0 = L + (8 * g + q4) * 128 + ((56 * hh + 8 * p4) ^ trx);
  u32 tr1 = L + (8 * g + q4) * 128 + ((56 * hh + 32 + 8 * (p4 == 3 ? 2 : p4)) ^ trx);
  u32 kx_w = L + L_KX + (w ^ 4) * KB + l16, kx_r = L + L_KX + w * KB + l16;
  u32 pk_w = L + L_PK + (hh * 64 + T * 16 + i16) * PK_RS + g * 8;
  u32 va_w = L + (u ? L_V1 : L_V0) + (hh * 64 + T * 16 + i16) * V_RS + g * 8, vb_w = L + (u ? L_V1 : L_V0) + (hh * 64 + (T ^ 1) * 16 + i16) * V_RS + g * 8;
  u32 kv_a = L + L_PK + (u * 64 + g * 8 + q4) * PK_RS + ((w >> 1) & 1) * 64 + p4 * 8;
  u32 kv_b = L + L_V0 + (u * 64 + g * 8 + q4) * V_RS + (w & 1) * 32 + p4 * 8, kv_c = kv_b + (L_V1 - L_V0);
  u32 ph_a = L + L_PHI + hh * 1568 + (4 * g + q4) * 56 + p4 * 8;
  u32 xr_r = L + L_VA + i16 * 256 + ((((w < 7 ? w : 0) * 2 + (g >> 1)) ^ i16) * 16) + (g & 1) * 8;
  PIN(xr_r);
  u32 xo_r = L + L_X + (T ^ 1) * SLOT + u * 7 * KB + l16, xm_r = L + L_X + T * SLOT + 7 * KB + l16, ph_b = ph_a + (p4 == 3 ? 24 : 32);
  u32 tk0 = tr0 + L_K + u * 28 * KB, tk1 = tr1 + L_K + u * 28 * KB, tv0 = tr0 + (u ? L_VB : L_VA), tv1 = tr1 + (u ? L_VB : L_VA);
  u32 tq0 = tr0 + (u ? L_QB : L_QA), tq1 = tr1 + (u ? L_QB : L_QA);
  PIN(xm_r); PIN(ph_b); PIN(tk0); PIN(tk1); PIN(tv0); PIN(tv1); PIN(tq0); PIN(tq1);
  u32 h32 = OFF_HINT + (mi * 32 + (lane & 31)) * 64, uf_off = OFF_HINT + 4 * 256 * 64 + 32 + lane * 64;
  PIN(uf_off);
  PIN(keep); PIN(kx_w); PIN(kx_r);
  PIN(pk_w); PIN(va_w); PIN(vb_w); PIN(kv_a); PIN(kv_b); PIN(kv_c); PIN(ph_a); PIN(xo_r); PIN(h32);
  SB();
  const bool light = ((mi + 1) & 6) == 0;
  const u32 lid = yg * 2 + (mi & 1);
  const u32 nx = light ? ((lid & 15) == 5 ? 2 : 1) : 0;
  u32 u_ts = 0, u_dst = 0, u_lo = 0, u_hi = 0, u_keep = 0;
  if (u == 1) {
    const u32 unit = (light ? lid : 64 + yg * 6 + mi - 1) * 4 + (w - 4);
    const u32 sec = ((unit + 1936) >> 11) + ((unit + 1838) >> 11) + ((unit + 1740) >> 11) + ((unit + 1348) >> 11);
    const u64 base = ((const __attribute__((address_space(4))) u64*)__builtin_amdgcn_kernarg_segment_ptr())[3 + sec];
    const u32 first = (u32)(0x2bc1340d2070000ull >> (sec * 12)) & 0xfffu, uu = unit - first;
    const u32 sh3 = sec == 3 ? 2 : 0;
    const bool prm = (sec & 3) == 0;
    const u32 u7 = (uu * 9363) >> 16, st = u7 >> sh3, cb = uu - ((st * 7) << sh3), m = sec == 4 ? (u7 * 9363) >> 16 : 0;
    const rsrc_t rs = mkrs(base, sec >= 3 ? D_ * F_ * 2 : D_ * D_ * 2);
    const u32 vo = (st * (sec == 0 ? 28 : 32) + (lane >> 3)) * (896 << sh3) + cb * 128 + (lane & 7) * 16;
    const u32 r0 = (prm ? 4 * g : 8 * g) + q4;
    u_ts = ((0x7eee0u >> (sec * 4)) & 15) * KB + (sec == 0 ? 16 * KB : 0);
    u_dst = l16 + OFF_WO + first * 4 * KB + cb * 4 * u_ts + (m * 189 + st) * KB;
    u_lo = wl + L_VA + ((r0 >> 3) << 12) + (r0 & 7) * 128 + p4 * 8;
    u_hi = u_lo + (prm ? 8192 : 512);
    u_keep = (sec == 0 && padg) ? 0u : 0xffffffffu;
    dma_nt<4, 4 * KB>(rs, vo, 0, (8 * 896) << sh3, wl + L_VA);
  }
  PINS(u_ts); PIN(u_dst); PIN(u_lo); PIN(u_hi); PIN(u_keep);
  SB();
  STAMP(46);
  v4u xa[7], xb[7], uq[4];
  if (u == 0) {
    u32 xp = L + L_X + w * SLOT + i16 * X_RS + g * 16, xw = L + L_X + w * SLOT + l16;
    PIN(xp); PIN(xw);
    VMW(0);
    STAMP(25);
#pragma unroll
    for (int s = 0; s < 7; ++s) xb[s] = ld16(xp + 448 + s * 64);
#pragma unroll
    for (int s = 0; s < 7; ++s) xa[s] = ld16(xp + s * 64);
    SB();
    f4 sq = z4, sm = z4;
#pragma unroll
    for (int s = 0; s < 7; ++s) { sq = mfma(xb[s], xb[s], sq); sm = mfma(ones, xb[s], sm); }
#pragma unroll
    for (int s = 0; s < 7; ++s) { sq = mfma(xa[s], xa[s], sq); sm = mfma(ones, xa[s], sm); }
    const float mu = sm[0] * (1.0f / D_), dgv = diag(sq, p4);
    const float var = fmx(fmaf(-mu, mu, rsum(g == q4 ? dgv : 0.f) * (1.0f / D_)), 0.f);
    const float rs = __builtin_amdgcn_rsqf(var + 1e-5f), nb = -mu * rs;
    STAMP(26);
#pragma unroll
    for (int s = 0; s < 7; ++s) {
#pragma unroll
      for (int e = 0; e < 4; ++e) xb[s][e] = pk2(fmaf(blo(xb[s][e]), rs, nb), fmaf(bhi(xb[s][e]), rs, nb));
      st16(xw + (7 + s) * KB, xb[s]);
      SB();
    }
#pragma unroll
    for (int s = 0; s < 7; ++s) {
#pragma unroll
      for (int e = 0; e < 4; ++e) xa[s][e] = pk2(fmaf(blo(xa[s][e]), rs, nb), fmaf(bhi(xa[s][e]), rs, nb));
      st16(xw + s * KB, xa[s]);
      SB();
    }
    __builtin_amdgcn_s_setprio(0);
  } else {
    dma_in<1, 0>(mkrs(p_phi, 16 * 28 * 28 * 2), l16, mi * 3136 + (w - 4) * KB, 0, L + L_PHI + (w - 4) * KB);
    dma_in<14, 4 * KB>(rq, wv, 896, 32 * 2688, wl + L_K);
    STAMP(25);
    VMW(15);
    v2u lo[4], hi[4];
#pragma unroll
    for (int t = 0; t < 4; ++t) { lo[t] = trread(u_lo + t * 32); hi[t] = trread(u_hi + t * 32); }
#pragma unroll
    for (int t = 0; t < 4; ++t) { uq[t] = v4u{lo[t].x, lo[t].y, hi[t].x & u_keep, hi[t].y & u_keep}; PIN(uq[t]); }
    SB();
    dma_in<4, 4 * KB>(rq, wv, 1792, 32 * 2688, wl + L_VA);
    VMW(11);
  }
  STAMP(27);
  bar();
  asm volatile("" : "+s"(epoch));
  const u64 tag = (u64)epoch | (0x9e3779b9ull << 32);
  STAMP(18);
#pragma unroll
  for (int s = 0; s < 7; ++s) xb[s] = ld16(xo_r + s * KB);
  if (u == 1) {
#pragma unroll
    for (int s = 0; s < 7; ++s) xa[s] = ld16(xm_r + s * KB);
  }
  v4u phA, phB;
  {
    const v2u l0 = trread(ph_a), h0 = trread(ph_a + 16 * 56), l1 = trread(ph_b), h1 = trread(ph_b + 16 * 56);
    phA = v4u{l0.x, l0.y, padg ? 0u : h0.x, padg ? 0u : h0.y};
    phB = v4u{l1.x, l1.y, padg ? 0u : h1.x, padg ? 0u : h1.y};
  }
  TrBuf tb_;
  u32 rt0 = 0, cy0 = 0;
  if (u == 1) {
    { u64 a, c; asm volatile("s_memrealtime %0\n\ts_memtime %1\n\ts_waitcnt lgkmcnt(0)" : "=&s"(a), "=&s"(c) :: "memory"); rt0 = (u32)a; cy0 = (u32)c; }
    dma_in<3, 4 * KB>(rq, wv, 1792 + 4 * 32 * 2688, 32 * 2688, wl + L_VA + 16 * KB);
#pragma unroll
    for (int t = 0; t < 4; ++t) stg(rws, u_dst + t * u_ts, uq[t]);
    VMW(11);
    bar();
  }
  tr_open(tb_, tk0, tk1);
  PIN(phA); PIN(phB);
  STAMP(1);
  v4u wo_[16], w1_[14], w2_[14], w3f[28], w4f[28];
  u32 lw = w < 7 ? l16 : OOB;
  PIN(lw);
  auto pf = [&](int k) AI {
    if (k < 16) wo_[k] = ldg(rws, lw + (k & 3) * KB, OFF_WO + (n * 16 + (k & ~3)) * KB);
    else if (k < 30) w1_[k - 16] = ldg(rws, lw + (k & 3) * KB, OFF_W1 + (n * 14 + ((k - 16) & ~3)) * KB);
    else if (k < 44) w2_[k - 30] = ldg(rws, lw + ((k - 30) & 3) * KB, OFF_W2 + (n * 14 + ((k - 30) & ~3)) * KB);
    else if (k < 72) { const int t = (k - 44) & 1, st = (k - 44) >> 1;
      w3f[t * 14 + st] = ldg(rws, lw + (st & 3) * KB, OFF_W3 + ((ms * 14 + w * 2 + t) * 14 + (st & ~3)) * KB); }
    else if (k < 100) { const int sl = (k - 72) / 7, c = (k - 72) % 7;
      w4f[k - 72] = ldg(rws, (lw | NOW4) + (c & 3) * KB, OFF_W4 + ((ms * 28 + ((js + 1 + sl) & 3) * 7 + w) * 7 + (c & ~3)) * KB); }
  };
#define PFR(lo, hi) do { _Pragma("unroll") for (int k_ = (lo); k_ < (hi); ++k_) pf(k_); asm volatile("" ::: "memory"); SB(); } while (0)
#define PFS(base, cnt) [&](int s_) AI { if ((s_ & 1) == 0 && (s_ >> 1) < (cnt)) pf((base) + (s_ >> 1)); }
  {
    STAMP(20);
    f4 a0, a1, b0, b1;
    gemm_tr(tb_, tk0, tk1, xa, xb, a0, a1, b0, b1, [&](int s_) AI { if (s_ == 4) asm volatile("s_cmp_lg_u32 %0, 0\n\ts_cbranch_scc1 1f\n\ts_barrier\n1:" :: "s"(u) : "memory", "scc"); });
    st16(kx_w, pack8(b0, b1));
    const v4u ka = pack8(a0, a1);
    f4 p0 = mfma(phA, ka, z4), p1 = mfma(phB, ka, z4);
    STAMP(2);
    u32 qs = u * 224 * 2688, ql = wl + (u ? L_QB : L_QA);
    PINS(qs); PINS(ql);
    auto v_part = [&]() AI {
      gemm_tr(tb_, tv0, tv1, xa, xb, a0, a1, b0, b1, [&](int) AI {
        asm volatile("s_mov_b32 m0, %1\n\ts_add_u32 %1, %1, 0x1000\n\tbuffer_load_dwordx4 %2, %3, %0 offen lds\n\ts_add_u32 %0, %0, %4"
                     : "+s"(qs), "+s"(ql) : "v"(wv), "s"(rq), "n"(32 * 2688) : "memory", "scc"); });
    };
    auto v_out = [&](float one) AI {
      if (padg) { a1[0] = one; b1[0] = one; }
      const v4u va = pack8(a0, a1), vb = pack8(b0, b1);
      st8(vb_w, vb.x, vb.y); st8(vb_w + 32, vb.z, vb.w);
      st8(va_w, va.x, va.y); st8(va_w + 32, va.z, va.w);
    };
    auto k_part = [&]() AI {
      v4u pos, neg;
      hedgehog(p0, p1, keep, pos, neg);
      st8(pk_w, pos.x, pos.y); st8(pk_w + 32, pos.z, pos.w); st8(pk_w + 64, neg.x, neg.y); st8(pk_w + 96, neg.z, neg.w);
    };
    if (u == 0) {
      dma_in<7, 4 * KB>(rq, wv, 1792 + 224 * 2688, 32 * 2688, wl + L_VB);
      STAMP(19);
      bar();
      STAMP(3);
      tr_open(tb_, tv0, tv1);
      const v4u kb = ld16(kx_r);
      SB();
      v_part();
      p0 = mfma(phA, kb, p0); p1 = mfma(phB, kb, p1);
      v_out(1.f);
      STAMP(21);
      VMW(7);
      bar();
      STAMP(22);
      for (u32 e = 0; e < nx; ++e) {
        const u32 v = e == 0 ? 324 + lid : 388 + (lid >> 4), s2 = (v * 9363) >> 16, c2 = v - s2 * 7;
        const u32 a = L + (e == 0 ? L_U0 : L_PHI) + (4 * g + q4) * 128 + p4 * 8 + w * 32;
        const v2u lo = trread(a), hi = trread(a + 2048);
        stg(rws, l16 + OFF_W4 + (c2 * 28 + ((s2 * 9363) >> 16) * 189 + s2 + w * 7) * KB, v4u{lo.x, lo.y, hi.x, hi.y});
      }
      k_part();
#pragma unroll
      for (int d = 0; d < 2; ++d) {
        const u32 c = i16 ^ ((w & 1) * 8 + d * 4 + g);
        dma_in<1, 0>(rx, (g * 896 + js * 224 + c * 16) | (c < 14 ? 0 : OOB), (zg * 32 + w * 8 + d * 4) * 896, 0, L + L_VA + (w * 2 + d) * KB);
      }
      if (w == 3) peek_issue(rws, uf_off);
    } else {
      if (light) VMW(0); else VMW(4);
      stg(rws, kvp_off, SENT); stg(rws, kvp_off - 4 * 32 * KB, SENT);
      if (w == 7) stg(rws, kvt_off, SENT);
      STAMP(19);
      bar();
      STAMP(3);
      const v4u kb = ld16(kx_r);
      if ((u32)(w - 5) < nx) {
        const u32 v = w == 5 ? 324 + lid : 388 + (lid >> 4), s2 = (v * 9363) >> 16, c2 = v - s2 * 7;
        dma_nt<4, KB>(mkrs(((const __attribute__((address_space(4))) u64*)__builtin_amdgcn_kernarg_segment_ptr())[7], D_ * F_ * 2),
                      (s2 * 32 + (lane >> 3)) * 896 + c2 * 128 + (lane & 7) * 16, 0, 8 * 896, L + (w == 5 ? L_U0 : L_PHI));
      }
      if (light && w == 4) post(OFF_HINT + (4 * 256 + lid) * 64 + 32);
      p0 = mfma(phA, kb, p0); p1 = mfma(phB, kb, p1);
      k_part();
      STAMP(21);
      VMW(0);
      if (OM == 1) { stg(rws, o_off, SENT); stg(rws, o_off ^ KB, SENT); }
      bar();
      STAMP(22);
      tr_open(tb_, tv0, tv1);
      if (w == 5) {
        u64 a, c; asm volatile("s_memrealtime %0\n\ts_memtime %1\n\ts_waitcnt lgkmcnt(0)" : "=&s"(a), "=&s"(c) :: "memory");
        const u32 t = (u32)a + (u32)((float)KVC * (float)((u32)a - rt0) * __builtin_amdgcn_rcpf((float)((u32)c - cy0)));
        stg(rws, vz + OFF_HINT + (5 * 256 + mi * 32 + yg) * 64, v4u{epoch, 0x9e3779b9u, t, 0});
      }
      SB();
      v_part();
      v_out(0.f);
    }
  }
  STAMP(23);
  bar();
  STAMP(4);
  if (w == 7) peek_issue(rws, h32 + 5 * 256 * 64);
  {
    v2u a0[2], a1[2], b0[2], b1[2], d0[4], d1[4];
#pragma unroll
    for (int s = 0; s < 2; ++s) {
      a0[s] = trread(kv_a + s * 32 * PK_RS); a1[s] = trread(kv_a + (s * 32 + 4) * PK_RS);
      d0[s] = trread(kv_b + s * 32 * V_RS); d1[s] = trread(kv_b + (s * 32 + 4) * V_RS);
      b0[s] = trread(kv_a + s * 32 * PK_RS + 32); b1[s] = trread(kv_a + (s * 32 + 4) * PK_RS + 32);
    }
#pragma unroll
    for (int s = 0; s < 2; ++s) { d0[2 + s] = trread(kv_c + s * 32 * V_RS); d1[2 + s] = trread(kv_c + (s * 32 + 4) * V_RS); }
    SB();
    f4 c0 = z4, c1 = z4;
#pragma unroll
    for (int s = 0; s < 4; ++s) {
      const v4u vf = {d0[s].x, d0[s].y, d1[s].x, d1[s].y};
      c0 = mfma(v4u{a0[s & 1].x, a0[s & 1].y, a1[s & 1].x, a1[s & 1].y}, vf, c0);
      c1 = mfma(v4u{b0[s & 1].x, b0[s & 1].y, b1[s & 1].x, b1[s & 1].y}, vf, c1);
    }
    stg(rws, kvp_off, nosent4(pack8(c0, c1)));
    SB();
  }
  RSTAMP(60);
  VMW(1);
  if (w == 3) peek_take<1>(rws, uf_off, tag);
  bar();
  STAMP(29);
  u32 t_kv = 0;
  if (w == 7) {
    __builtin_amdgcn_s_setprio(3);
    t_kv = peek_take<1>(rws, h32 + 5 * 256 * 64, tag) + DTX;
    wait_until(t_kv);
    RSTAMP(56);
    dma_ws<2>(rws, l16, OFF_KVP + (mi * 32 + yg) * 8 * KB, L + L_KP);
    const u32 om = l16 + OFF_O + (yg * 64 + mi * 4) * KB;
#pragma unroll
    for (int k = 0; k < 8; ++k) if (OM == 0) stg(rws, om + ((k >> 2) * 32 + (k & 3)) * KB, SENT);
    STAMP(6);
  }
  u32 h8 = OFF_HINT + (yg * 8 + (lane & 7)) * 64;
  PIN(h8);
  f4 pq0, pq1;
  {
    f4 a0, a1, b0, b1;
    tr_open(tb_, tq0, tq1);
    gemm_tr(tb_, tq0, tq1, xa, xb, a0, a1, b0, b1, [&](int s_) AI { if (s_ < QPF) pf(s_); });
    st16(kx_w - (L_KX - L_QX), pack8(b0, b1));
    const v4u qa = pack8(a0, a1);
    pq0 = mfma(phA, qa, z4); pq1 = mfma(phB, qa, z4);
    PIN(pq0); PIN(pq1);
  }
  STAMP(5);
  v4u qpos, qneg;
  auto phi_q = [&]() AI {
    const v4u qb = ld16(kx_r - (L_KX - L_QX));
    hedgehog(mfma(phA, qb, pq0), mfma(phB, qb, pq1), keep, qpos, qneg);
    PIN(qpos); PIN(qneg);
    SB();
  };
  auto make_o = [&]() AI {
    v4u kf[4];
#pragma unroll
    for (int k = 0; k < 4; ++k) kf[k] = ld16(L + L_KF + (hh * 4 + k) * KB + l16);
    SB();
    f4 n1 = mfma(kf[1], qpos, z4), n0 = mfma(kf[0], qpos, z4);
    n1 = mfma(kf[3], qneg, n1); n0 = mfma(kf[2], qneg, n0);
    const float rd = __builtin_amdgcn_rcpf(rsum(padg ? n1[0] : 0.f) + 1e-6f);
    n0 *= rd; n1 *= rd;
    v4u ov = nosent4(pack8(n0, n1));
    if (padg) { ov.z = 0; ov.w = 0; }
    stg(rws, o_off, ov);
    SB();
  };
  const u32 hme = OFF_HINT + (zg * 4 + js) * 64;
  if (w < 7) {
  bar();
  STAMP(7);
  v2u xr[2];
  xr[0] = *(LAS v2u*)(uintptr_t)xr_r; xr[1] = *(LAS v2u*)(uintptr_t)(xr_r + 4 * KB);
  PFR(QPF, PA);
  phi_q();
  STAMP(31);
  PFR(PA, PA2);
  bar();
  STAMP(53);
  if (!OT && w == 0) post(OFF_HINT + (0 * 256 + yg * 8 + mi) * 64);
  make_o();
  bar();
  STAMP(8);
  if (HM == 1) stg(rws, h_off, SENT);
  f4 hold0, hold1;
  const u32 A = L + L_ACT + l16;
  const int tk = zg * 32 + (g & 1) * 16 + i16 + ((js & 1) ? -1 - (js >> 1) : 1 + (js >> 1));
  u32 hs_off = ((((tk & (S_ - 1)) >> 5) * 14 + (n >> 1)) * 2 + ((tk >> 4) & 1)) * KB + cko + (tk & 15) * 16 + OFF_HS;
  u32 hs_keep = (tk < 0 || tk >= S_) ? 0u : 0xffffffffu;
  u32 x1_off = h_off + (OFF_X1 - OFF_H);
  u32 st_r = L + L_ST + i16 * 8;
  PFR(PB, PB + R10);
  PIN(hs_off); PIN(hs_keep); PIN(x1_off); PIN(st_r);
  f4 a0 = {blo(xr[0].x), bhi(xr[0].x), blo(xr[0].y), bhi(xr[0].y)};
  f4 a1 = {blo(xr[1].x), bhi(xr[1].x), blo(xr[1].y), bhi(xr[1].y)};
  if (HM == 1) asm volatile("s_waitcnt vmcnt(%0)" :: "n"(R10) : "memory");
  bar();
  stg(rws, h_off + (OFF_HS - OFF_H), SENT);
  PFR(PB + R10, PC);
  PIN(a0); PIN(a1);
  bar();
  if (w == 0) post_t(hme + 1 * 256 * 64);
  STAMP(32);
  {
    gemm_alds<16>(A, wo_, a0, a1, PFS(PC, NL));
    STAMP(33);
    const v4u p = nosent4(pack8(a0, a1));
    stg(rws, h_off, to_chunk(p));
    asm volatile("s_waitcnt vmcnt(%0)" :: "n"(NL + 1) : "memory");
    SB();
    hold0 = f4{blo(p.x), bhi(p.x), blo(p.y), bhi(p.y)};
    hold1 = f4{blo(p.z), bhi(p.z), blo(p.w), bhi(p.w)};
  }
  bar();
  if (w == 0) post(hme + 7 * 256 * 64);
  STAMP(9);
  f4 xb0, xb1;
  {
    PFR(PC + NL, PC + NL + R1);
    bar();
    stg(rws, x1_off, SENT);
    PFR(PC + NL + R1, PD);
    f4 cs = z4;
#pragma unroll
    for (int s = 0; s < 14; ++s) cs = mfma(w1_[s], ones, cs);
    PIN(cs);
    bar();
    if (w == 0) post_t(hme + 2 * 256 * 64);
    STAMP(34);
    a0 = z4; a1 = z4;
    gemm_alds<14>(A, w1_, a0, a1, PFS(PD, NL));
    STAMP(35);
    for (u32 sp = 0; sp < (1u << 20) && __builtin_amdgcn_readfirstlane(*(volatile LAS u32*)(uintptr_t)(L + L_SF)) == 0; ++sp) { }
    STAMP(36);
    const f2 s0 = ldf2(st_r), s1 = ldf2(st_r + 128), e0 = ldf2(st_r + 256), e1 = ldf2(st_r + 384);
#pragma unroll
    for (int r = 0; r < 4; ++r) {
      a0[r] = gelu(fmaf(a0[r], s0[1], s0[0] * cs[r]));
      a1[r] = gelu(fmaf(a1[r], s1[1], s1[0] * cs[r]));
    }
    stg(rws, hs_off, to_chunk(nosent4(pack8(a0, a1))) & hs_keep);
    asm volatile("s_waitcnt vmcnt(%0)" :: "n"(NL + 1) : "memory");
    bar();
#pragma unroll
    for (int r = 0; r < 4; ++r) { xb0[r] = fmaf(hold0[r], e0[1], e0[0]); xb1[r] = fmaf(hold1[r], e1[1], e1[0]); }
    PIN(xb0); PIN(xb1);
  }
  STAMP(10);
  PFR(PD + NL, PD + NL + R1);
  bar();
  PFR(PD + NL + R1, PE);
  bar();
  if (w == 0) post_t(hme + 3 * 256 * 64);
  STAMP(37);
  {
    gemm_alds<14>(A, w2_, xb0, xb1, PFS(PE, NL));
    STAMP(38);
    const v4u p = nosent4(pack8(xb0, xb1));
    stg(rws, x1_off, to_chunk(p));
    bar();
    hold0 += f4{blo(p.x), bhi(p.x), blo(p.y), bhi(p.y)};
    hold1 += f4{blo(p.z), bhi(p.z), blo(p.w), bhi(p.w)};
    PIN(hold0); PIN(hold1);
  }
  STAMP(11);
  constexpr int W3E = (PE + NL + R1) & ~1;
  PFR(PE + NL, W3E);
  u32 hid_w = L + L_HID + w * 2 * KB + l16, hid_r = L + L_HID + l16;
  u32 rs_w = l16 + OFF_RS + ((yg * 64 + mi) * 7 + w) * KB;
  u32 out_off = (zg * 32 + (g & 1) * 16 + i16) * 896 + (n * 16 + (g >> 1) * 8) * 2;
  u32 xp_s = OFF_X1 + (w < 3 ? (zg ^ 1) * 28 + 16 + w * 4 : zg * 28 + w * 4) * KB, xp_l = L + L_ACT + (w < 3 ? 16 + w * 4 : 28 + w * 4) * KB;
  PINS(xp_s); PINS(xp_l);
  PIN(hid_w); PIN(hid_r); PIN(rs_w); PIN(out_off);
  constexpr int CSE = (PE + NL - 44) / 2 < 14 ? (PE + NL - 44) / 2 : 14;
  f4 cs[2] = {z4, z4};
#pragma unroll
  for (int s = 0; s < CSE; ++s) { cs[0] = mfma(w3f[s], ones, cs[0]); cs[1] = mfma(w3f[14 + s], ones, cs[1]); }
  PIN(cs[0]); PIN(cs[1]);
  bar();
  dma_ws<1>(rws, l16, xp_s, xp_l);
  STAMP(44);
  bar();
  STAMP(12);
  f4 pu[4][2] = {{z4, z4}, {z4, z4}, {z4, z4}, {z4, z4}};
  f4 sv[2];
  float fr0, fr1;
  u32 s5_r = L + L_S5 + i16 * 16;
  PIN(s5_r);
  float gy[2], gt[2];
  v4u gp;
  auto mop = [&](int t, int j) AI {
    const int pr = j / 17, o = j % 17, e = o & 1, c = pr >> 1, r = (pr & 1) * 2 + e;
    const f4& q = sv[t & 1];
    if (j == 68) st16(hid_w + (t >> 1) * 14 * KB + (t & 1) * KB, gp);
    else if (o < 2) gy[e] = fmaf(q[0], cs[c][r], pu[t][c][r]);
    else if (o < 4) gt[e] = gy[e] * gy[e];
    else if (o < 6) gt[e] = fmaf(gt[e], q[2], q[1]);
    else if (o < 8) gt[e] = gt[e] * gy[e];
    else if (o < 10) gt[e] = __builtin_amdgcn_exp2f(gt[e]);
    else if (o < 12) gt[e] = gt[e] + 1.0f;
    else if (o < 14) gt[e] = __builtin_amdgcn_rcpf(gt[e]);
    else if (o < 16) { gy[e] = gy[e] * gt[e]; if (e == 0) PIN(gy[0]); }
    else gp[pr] = pk2(gy[0], gy[1]);
  };
#define MOPS(T0, NOPS, m, NM) sfor<(m) * (NOPS) / (NM), ((m) + 1) * (NOPS) / (NM)>([&](auto J_) AI { mop((T0) + J_.v / 69, J_.v % 69); SB(); })
  constexpr int G0 = W3E, N3 = G0 < 72 ? 72 - G0 : 0, K3 = 4 + N3 * 3 / 2, GB = G0 + N3 + FA;
  v4u hf[14];
  {
    constexpr int P = 6;
    v4u bq[P];
    auto fa = [&](int k) AI { return A + (k < 12 ? k : k < 24 ? 28 + k - 12 : k < 32 ? 2 * (k - 18) : k < 40 ? 2 * (k - 26) + 1 : k) * KB; };
#pragma unroll
    for (int k = 0; k < P; ++k) bq[k] = ld16(fa(k));
    SB();
    sfor<0, 56>([&](auto K) AI {
      constexpr int k = K.v;
      constexpr int t = k < 12 ? k & 1 : k < 24 ? 2 + (k & 1) : k < 32 ? 0 : k < 40 ? 1 : 2 + (k & 1);
      constexpr int s = k < 12 ? k >> 1 : k < 24 ? (k - 12) >> 1 : k < 32 ? k - 18 : k < 40 ? k - 26 : (k - 28) >> 1;
      sfor<0, 2>([&](auto C) AI {
        constexpr int c = C.v;
        pu[t][c] = mfma(w3f[c * 14 + s], bq[k % P], pu[t][c]);
        SB();
        if constexpr (k >= 33 && k < 50) MOPS(0, 138, (k - 33) * 2 + c, 34);
      });
      if constexpr (t == 0 && s >= CSE) { cs[0] = mfma(w3f[s], ones, cs[0]); cs[1] = mfma(w3f[14 + s], ones, cs[1]); SB(); }
      if constexpr (k == 6 || k == 18) asm volatile("s_barrier" ::: "memory");
      if constexpr (k == 14) {
        asm volatile("s_waitcnt vmcnt(%0)" :: "n"(N3 ? (K3 < 14 ? N3 + (14 - K3) * FA / (56 - K3) : 10 * 2 / 3) : 10 * FA / 52) : "memory");
#pragma unroll
        for (int e = 0; e < 4; ++e) hf[e] = ld16(xp_l + l16 + e * KB);
      }
      if constexpr (k == 17) {
        if (!__all(max4(max4(max4(max4(0, hf[0]), hf[1]), hf[2]), hf[3]) != 0xffffffffu)) {
          RETRY_LOOP {
            RETRY_END(7);
            dma_ws<1>(rws, l16, xp_s, xp_l);
            VMW(0);
            if (__all(max4(max4(max4(max4(0, ld16(xp_l + l16)), ld16(xp_l + l16 + KB)), ld16(xp_l + l16 + 2 * KB)), ld16(xp_l + l16 + 3 * KB)) != 0xffffffffu)) break;
          }
        }
      }
      if constexpr (k == 24) STAMP(48);
      if constexpr (k == 31) { asm volatile("s_barrier" ::: "memory"); STAMP(49); sv[0] = __builtin_bit_cast(f4, ld16(s5_r)); sv[1] = __builtin_bit_cast(f4, ld16(s5_r + 256)); }
      if constexpr (k == 50) { STAMP(50); asm volatile("s_waitcnt lgkmcnt(0)\n\ts_barrier" ::: "memory"); }
      if constexpr (k + P < 56) bq[k % P] = ld16(fa(k + P));
      if constexpr (k >= 50) sfor<(k - 50) * 14 / 6, (k - 49) * 14 / 6>([&](auto E) AI { hf[E.v] = ld16(hid_r + E.v * KB); });
      if constexpr (k >= 4 && k < K3) sfor<(k - 4) * 2 / 3, (k - 3) * 2 / 3>([&](auto E) AI { pf(G0 + E.v); });
      if constexpr (k >= K3) sfor<(k - K3) * FA / (56 - K3), (k + 1 - K3) * FA / (56 - K3)>([&](auto E) AI { pf(G0 + N3 + E.v); });
      SB();
    });
  }
  STAMP(39);
  f4 own0, own1;
  {
    v4u pend[2];
    auto rs_off = [&](int rd) AI { return (rs_w | NORS) + ((((rd >> 2) ? th : th ^ 1) * 4 + ((js + 1 + rd) & 3)) * 56) * KB; };
    sv[0] = __builtin_bit_cast(f4, ld16(s5_r + 512)); sv[1] = __builtin_bit_cast(f4, ld16(s5_r + 768));
    fr0 = sv[0][3]; fr1 = sv[1][3];
    sfor<0, 8>([&](auto RD) AI {
      constexpr int rd = RD.v, sl = rd & 3;
      f4 c0 = z4, c1 = z4;
      sfor<0, 7>([&](auto C) AI {
        constexpr int c = C.v;
        c1 = mfma(w4f[sl * 7 + c], hf[2 * c + 1], c1);
        SB();
        if constexpr (rd < 3 && rd + c > 0) MOPS(2, 138, (rd * 7 + c) * 2 - 2, 40);
        c0 = mfma(w4f[sl * 7 + c], hf[2 * c], c0);
        SB();
        if constexpr (rd < 3 && rd + c > 0) MOPS(2, 138, (rd * 7 + c) * 2 - 1, 40);
        if constexpr (rd == 3 && c == 0) { STAMP(51); asm volatile("s_waitcnt lgkmcnt(0)\n\ts_barrier" ::: "memory"); }
        if constexpr (rd == 4 && c == 0) STAMP(52);
        if constexpr (rd == 3) { hf[2 * c] = ld16(hid_r + (14 + 2 * c) * KB); hf[2 * c + 1] = ld16(hid_r + (15 + 2 * c) * KB); }
        if constexpr (rd < 2) sfor<(rd * 7 + c) * (100 - GB) / 14, (rd * 7 + c + 1) * (100 - GB) / 14>([&](auto E) AI { pf(GB + E.v); });
        if constexpr (c == 1 && rd == 1) pend[0] = pack8(own0, own1);
        if constexpr (c == 1 && rd >= 2) stg(rws, rs_off(rd - 1), pack8(own0, own1));
        if constexpr (c == 4 && rd == 2) stg(rws, rs_off(0), pend[0]);
        if constexpr (rd == RF && c == 0) asm volatile("s_waitcnt vmcnt(%0)\n\ts_barrier" :: "n"(RF - 5) : "memory");
        SB();
      });
      own0 = c0; own1 = c1;
    });
    PIN(own0); PIN(own1);
  }
  static_assert(GB >= 86, "");
  STAMP(42);
  VMW(0);
  bar();
  STAMP(16);
  const u32 one = g != q4 ? 0u : (p4 & 1) ? 0x3f800000u : 0x00003f80u;
  v4u sl0 = {p4 < 2 ? one : 0u, p4 < 2 ? 0u : one, 0u, 0u}, sl1 = {0u, 0u, sl0.x, sl0.y};
  PIN(sl0); PIN(sl1);
  u32 rs_r[3];
#pragma unroll
  for (int k = 0; k < 3; ++k) { rs_r[k] = OFF_RS + (((yg * 8 + mi) * 8 + th * 4 + ((js + 1 + k) & 3)) * 7 + w) * KB; PINS(rs_r[k]); }
  bar();
  STAMP(43);
  {
    v4u pp[7];
#pragma unroll
    for (int k = 0; k < 3; ++k) pp[k] = ldg(rws, l16, rs_r[k]);
#pragma unroll
    for (int k = 0; k < 4; ++k) pp[3 + k] = ld16(A + (k * 7 + w) * KB);
    SB();
#pragma unroll
    for (int k = 6; k >= 3; --k) { own0 = mfma(sl0, pp[k], own0); own1 = mfma(sl1, pp[k], own1); }
    SB();
#pragma unroll
    for (int k = 0; k < 3; ++k) { own0 = mfma(sl0, pp[k], own0); own1 = mfma(sl1, pp[k], own1); }
#pragma unroll
    for (int r = 0; r < 4; ++r) { hold0[r] = fmaf(own0[r], fr0, hold0[r]); hold1[r] = fmaf(own1[r], fr1, hold1[r]); }
    *(GAS v4u*)(p_out + out_off) = to_chunk(pack8(hold0, hold1));
  }
  STAMP(17);
  STAMP_END();
  return;
  }
  {
    {
      const u32 one = (i16 & 1) ? 0x3f800000u : 0x00003f80u;
      v4u sel = {(i16 >> 1) == 0 ? one : 0u, (i16 >> 1) == 1 ? one : 0u, (i16 >> 1) == 2 ? one : 0u, (i16 >> 1) == 3 ? one : 0u};
      PIN(sel);
      f4 acc;
      asm volatile("s_waitcnt vmcnt(%0)" :: "n"(OM == 0 ? 8 + QPF : 0) : "memory");
      RETRY_LOOP {
        v4u kp[8];
#pragma unroll
        for (int k = 0; k < 8; ++k) kp[k] = ld16(L + L_KP + k * KB + l16);
        SB();
        acc = z4;
        u32 m = 0;
#pragma unroll
        for (int k = 0; k < 8; ++k) { acc = mfma(sel, kp[k], acc); m = max4(m, kp[k]); }
        if (__all(m != 0xffffffffu)) break;
        RETRY_END(1);
        dma_ws<2>(rws, l16, OFF_KVP + (mi * 32 + yg) * 8 * KB, L + L_KP);
        VMW(0);
      }
      __builtin_amdgcn_raw_buffer_store_b64(v2u{nosent(pk2(acc[0], acc[1])), nosent(pk2(acc[2], acc[3]))}, rws, (int)kvt8, 0, SC1);
      RSTAMP(57);
      if (OT) { VMW(1); post_t(OFF_HINT + (0 * 256 + yg * 8 + mi) * 64); }
      bar();
      __builtin_amdgcn_s_sleep(DLYB);
      wait_until(t_kv + DTF);
      dma_ws<2>(rws, l16, OFF_KVT + mi * 8 * KB, L + L_KF);
      if (HM == 0) {
        const u32 hm = zg * 28 * KB + (g & 1) * KB + (g >> 1) * 256 + i16 * 16 + OFF_H;
#pragma unroll
        for (int c = 0; c < 7; ++c) stg(rws, hm + ((js * 7 + c) >> 1) * 2 * KB + ((js * 7 + c) & 1) * 512, SENT);
      }
      RSTAMP(58);
      phi_q();
      STAMP(31);
      f4 sm[4];
      if (HM == 0) VMW(7); else VMW(0);
      RETRY_LOOP {
        if (g_check<0, 2, -1, false>(L + L_KF + l16, sm)) break;
        RETRY_END(2);
        dma_ws<2>(rws, l16, OFF_KVT + mi * 8 * KB, L + L_KF);
        VMW(0);
      }
      bar();
      RSTAMP(59);
      if (OT) peek_issue(rws, h8);
      make_o();
      bar();
      STAMP(8);
    }
    const u32 P = L + L_ACT + l16;
    f4 sm[4];
    u32 hN = OFF_HINT + (7 * 256 + (((js & 1) ? zg + 63 : zg + 1) & 63) * 4 + js) * 64;
#define GATHER(NK, ST, SRC, ID) \
      RETRY_LOOP { \
        dma_ws<NK / 4>(rws, l16, SRC, L + L_ACT); \
        if (ST) peek_issue(rws, hN); \
        if (sp == 0) bar(); \
        if (ST) { sm[0] = z4; sm[1] = z4; sm[2] = z4; sm[3] = z4; } \
        if (g_check<0, 3, NK - 12 + ST, ST>(P, sm)) break; \
        RETRY_END(ID); \
        VMW(0); \
      } \
      bar(); \
      RETRY_LOOP { \
        f4 sn[4] = {sm[0], sm[1], sm[2], sm[3]}; \
        const bool ok = g_check<3, NK / 4, ST, ST>(P, sn); \
        if (ok) { sm[0] = sn[0]; sm[1] = sn[1]; sm[2] = sn[2]; sm[3] = sn[3]; break; } \
        RETRY_END(ID); \
        dma_ws<NK / 4 - 3>(rws, l16, (SRC) + 12 * KB, L + L_ACT + 12 * KB); \
        VMW(0); \
      } \
      bar()
    {
      if (OT) pace<1, DT0>(rws, h8, tag, mi); else poll(rws, h8, tag);
    }
    STAMP(9);
    { GATHER(32, false, OFF_O + zg * 32 * KB, 3); }
    STAMP(10);
    u32 hA = OFF_HINT + (1 * 256 + zg * 4 + (lane & 3)) * 64;
    u32 hB = OFF_HINT + (2 * 256 + ((lane & 4) ? ((lane & 1) ? zg + 1 : zg + 63) & 63 : zg) * 4 + (lane & 3)) * 64;
    u32 st_w = L + (g == q4 ? L_ST : L_QB) + i16 * 8;
    PIN(hA); PIN(hB); PIN(st_w);
    *(LAS u32*)(uintptr_t)(L + L_SF) = 0;
    PIN(hN);
    peek_issue(rws, hA);
    STAMP(32);
    bar();
    pace<0, DT1>(rws, hA, tag, js);
    STAMP(11);
    {
      GATHER(28, true, OFF_H + zg * 28 * KB, 4);
        STAMP(12);
      const float m0 = sm[0][0] * (1.0f / D_), m1 = sm[1][0] * (1.0f / D_);
      const float v0 = fmx(fmaf(-m0, m0, diag(sm[2], p4) * (1.0f / D_)), 0.f), v1 = fmx(fmaf(-m1, m1, diag(sm[3], p4) * (1.0f / D_)), 0.f);
      const float r0 = __builtin_amdgcn_rsqf(v0 + 1e-5f), r1 = __builtin_amdgcn_rsqf(v1 + 1e-5f);
      const float t0 = r0 * __builtin_amdgcn_rsqf(v0 * r0 * r0 + 1e-5f), t1 = r1 * __builtin_amdgcn_rsqf(v1 * r1 * r1 + 1e-5f);
      { st8(st_w, f2u(-m0 * t0), f2u(t0)); st8(st_w + 128, f2u(-m1 * t1), f2u(t1)); st8(st_w + 256, f2u(-m0 * r0), f2u(r0)); st8(st_w + 384, f2u(-m1 * r1), f2u(r1)); }
      peek_take<0>(rws, hN, tag);
      asm volatile("s_waitcnt lgkmcnt(0)" ::: "memory");
      *(volatile LAS u32*)(uintptr_t)(L + L_SF) = 1;
    }
    STAMP(33);
    peek_issue(rws, hB);
    bar();
    pace<0, DT2>(rws, hB, tag, js);
    STAMP(13);
    { GATHER(28, false, OFF_HS + zg * 28 * KB, 5); }
    STAMP(14);
    u32 h83 = h8 + 3 * 256 * 64;
    PIN(h83);
    peek_issue(rws, h83);
    bar();
    pace<0, DT1>(rws, h83, tag, mi);
    STAMP(15);
    {
      const u32 s5_w = L + (g == q4 ? L_S5 : L_QB + 4 * KB) + i16 * 16;
      auto stats = [&](u32 dst) AI {
        const float m0 = sm[0][0] * (1.0f / D_), m1 = sm[1][0] * (1.0f / D_);
        const float r0 = __builtin_amdgcn_rsqf(fmx(fmaf(-m0, m0, diag(sm[2], p4) * (1.0f / D_)), 0.f) + 1e-5f);
        const float r1 = __builtin_amdgcn_rsqf(fmx(fmaf(-m1, m1, diag(sm[3], p4) * (1.0f / D_)), 0.f) + 1e-5f);
        {
          st16(dst, v4u{f2u(-m0), f2u(GC0 * r0), f2u(GC1 * r0 * r0 * r0), f2u(r0)});
          st16(dst + 256, v4u{f2u(-m1), f2u(GC0 * r1), f2u(GC1 * r1 * r1 * r1), f2u(r1)});
        }
      };
      const u32 xa_ = OFF_X1 + (zg ^ 1) * 28 * KB, xb_ = OFF_X1 + zg * 28 * KB;
      RETRY_LOOP {
        dma_ws<3>(rws, l16, xa_, L + L_ACT);
        dma_ws<3>(rws, l16, xb_, L + L_ACT + 28 * KB);
        dma_ws<1>(rws, l16, xa_ + 12 * KB, L + L_ACT + 12 * KB);
        if (sp == 0) bar();
        if (g_check<0, 3, 16, false>(P, sm)) break;
        RETRY_END(6);
        VMW(0);
      }
      bar();
      RETRY_LOOP {
        if (g_check<7, 10, 4, false>(P, sm)) break;
        RETRY_END(6);
        dma_ws<3>(rws, l16, xb_, L + L_ACT + 28 * KB);
        VMW(0);
      }
      bar();
      STAMP(16);
      sm[0] = z4; sm[1] = z4; sm[2] = z4; sm[3] = z4;
      RETRY_LOOP {
        VMW(0);
        if (g_check<3, 4, -1, false>(P, sm)) break;
        RETRY_END(7);
        dma_ws<1>(rws, l16, xa_ + 12 * KB, L + L_ACT + 12 * KB);
      }
      g_check<0, 4, -1, true>(P, sm);
      STAMP(35);
      bar();
      g_check<4, 7, -1, true>(P, sm);
      stats(s5_w);
      STAMP(36);
      bar();
      sm[0] = z4; sm[1] = z4; sm[2] = z4; sm[3] = z4;
      g_check<7, 14, -1, true>(P, sm);
      stats(s5_w + 512);
    }
    STAMP(37);
    bar();
    bar();
    bar();
    post(hme + 6 * 256 * 64);
    u32 hO = OFF_HINT + (6 * 256 + (zg ^ 1) * 4 + (lane & 3)) * 64, hS = OFF_HINT + (4 * 256 + zg * 4 + (lane & 3)) * 64;
    PIN(hO); PIN(hS);
    __builtin_amdgcn_s_sleep(RF == 6 ? 8 : 4);
    peek_issue(rws, hO);
    bar();
    post(hme + 4 * 256 * 64);
    peek_take<1>(rws, hO, tag);
    STAMP(54);
    dma_ws<7>(rws, l16, OFF_RS + ((yg * 8 + mi) * 8 + (th ^ 1) * 4) * 7 * KB, L + L_ACT);
    poll(rws, hS, tag);
    STAMP(17);
    bar();
    if (b == 0) __builtin_amdgcn_raw_buffer_store_b32(epoch + 1, rws, (int)vz, (int)OFF_E, SC1);
    STAMP_END();
  }
}

extern "C" void kernel_launch(void* const* d_in, const int* in_sizes, int n_in, void* d_out, int out_size, void* d_ws, size_t ws_size,
                              hipStream_t stream) {
  fused_kernel<<<256, 512, 0, stream>>>((const u16*)d_in[0], (const u16*)d_in[1], (const u16*)d_in[2], (const u16*)d_in[3],
                                        (const u16*)d_in[5], (const u16*)d_in[6], (const u16*)d_in[7], (const u16*)d_in[8],
                                        (u16*)d_out, (unsigned char*)d_ws);
}
```

```cpp
#include <hip/hip_runtime.h>
#include <stdint.h>

typedef unsigned int u32;
typedef unsigned long long u64;
typedef unsigned short u16;
typedef u32 v4u __attribute__((ext_vector_type(4)));
typedef u32 v2u __attribute__((ext_vector_type(2)));
typedef float f4 __attribute__((ext_vector_type(4)));
typedef float f2 __attribute__((ext_vector_type(2)));
typedef __bf16 bf8 __attribute__((ext_vector_type(8)));
typedef __bf16 bf2 __attribute__((ext_vector_type(2)));
typedef short s4v __attribute__((ext_vector_type(4)));
typedef __amdgpu_buffer_rsrc_t rsrc_t;
#define GAS __attribute__((address_space(1)))
#define LAS __attribute__((address_space(3)))
#define DI __device__ __forceinline__
#define AI __attribute__((always_inline))
#define RLX_AGENT __ATOMIC_RELAXED, __HIP_MEMORY_SCOPE_AGENT

constexpr int S_ = 2048, D_ = 448, F_ = 1792;
constexpr u32 KB = 1024;
constexpr u32 OFF_E = 0, OFF_TMO = 64;
constexpr u32 OFF_HINT = 16 * KB;
constexpr u32 OFF_KVT = 192 * KB;
constexpr u32 OFF_WO = 256 * KB;
constexpr u32 OFF_W1 = OFF_WO + 448 * KB;
constexpr u32 OFF_W2 = OFF_W1 + 392 * KB;
constexpr u32 OFF_W3 = OFF_W2 + 392 * KB;
constexpr u32 OFF_W4 = OFF_W3 + 1568 * KB;
constexpr u32 OFF_KVP = OFF_W4 + 1568 * KB;
constexpr u32 OFF_O = OFF_KVP + 2048 * KB;
constexpr u32 OFF_H = OFF_O + 2048 * KB;
constexpr u32 OFF_HS = OFF_H + 1792 * KB;
constexpr u32 OFF_X1 = OFF_HS + 1792 * KB;
constexpr u32 OFF_RS = OFF_X1 + 1792 * KB;
constexpr u32 WS_BYTES = OFF_RS + 256 * 56 * KB;
constexpr u32 OFF_DBG = WS_BYTES;
constexpr u32 L_X = 0, X_RS = 928, SLOT = 16 * X_RS;
constexpr u32 L_VB = 0, L_QA = 28 * KB, L_QX = 0;
constexpr u32 L_K = 64 * X_RS;
constexpr u32 L_PK = L_K, PK_RS = 136;
constexpr u32 L_V0 = L_PK + 128 * PK_RS, V_RS = 72;
constexpr u32 L_QB = L_K + 28 * KB;
constexpr u32 L_VA = L_K + 56 * KB;
constexpr u32 L_U = L_VA;
constexpr u32 L_KX = L_VA + 28 * KB;
constexpr u32 L_V1 = L_KX;
constexpr u32 L_ST = L_KX + 9 * KB;
constexpr u32 L_SF = L_ST + 512, L_DUMP = L_ST + 768;
constexpr u32 L_PHI = L_ST + 1024;
constexpr u32 L_U0 = L_PHI + 4096;
constexpr u32 L_BYTES = L_U0 + 4096;
constexpr u32 L_KP = 8 * KB;
constexpr u32 L_KF = 16 * KB;
static_assert(L_BYTES <= 163840 && L_V0 + 128 * V_RS <= L_QB, "");
constexpr u32 L_ACT = 0;
constexpr u32 L_HID = 56 * KB;
constexpr u32 L_S5 = 84 * KB;
constexpr u32 SPIN_CAP = 1u << 15;
constexpr u32 OOB = 0x80000000u;
constexpr int SC1 = 16;
constexpr u32 CODE_BYTES = 40960;
#define XDMA dma_in
#define DT0 10
#define DT1 10
#define DT2 14
#define R1 3
#define PA 11
#define QPF 5
#define R10 R1
#define R20 4
#define PA2 15
#define DTX 22
#define PB PA2
#define R2 3
#define NL 7
#define PC (PB + R10 + R20)
#define PD (PC + NL + R1 + R2)
#define PE (PD + NL + R1 + R2)
static_assert(PC >= 16 && PC + NL + R1 >= 30 && PE >= 44 && PC + 2 <= 30, "");
#define FA 16
#define FB 14
#define NOW4 0
#define NORS 0
#define GG 2
#define OT 1
#define RF 7
#define HSE 0
#define HM 1
#define OM 0
#define KVC 2200
#define DTF 104
#define LDELAY 3
#define DLYA 1
#define DLYB 2
#define DLY0 1
#define DLY1 3
#define HS1 2

#define SB() __builtin_amdgcn_sched_barrier(0)
#define PIN(x) asm volatile("" : "+v"(x))
#define PINS(x) do { x = __builtin_amdgcn_readfirstlane(x); asm volatile("" : "+s"(x)); } while (0)
#define DIAG 0
#define STAMP(k) do { } while (0)
#define NRETRY(k) do { } while (0)
#define RSTAMP(k) do { } while (0)
#define STAMP_BEGIN() do { } while (0)
#define STAMP_END() do { } while (0)

template <int V> struct IC { static constexpr int v = V; };
template <int I, int N, class F> DI void sfor(F f) { if constexpr (I < N) { f(IC<I>{}); sfor<I + 1, N>(f); } }
DI f4 mfma(v4u a, v4u b, f4 c) {
  return __builtin_amdgcn_mfma_f32_16x16x32_bf16(__builtin_bit_cast(bf8, a), __builtin_bit_cast(bf8, b), c, 0, 0, 0);
}
DI u32 pk2(float a, float b) { f2 v = {a, b}; return __builtin_bit_cast(u32, __builtin_convertvector(v, bf2)); }
DI float blo(u32 u) { return __builtin_bit_cast(float, u << 16); }
DI float bhi(u32 u) { return __builtin_bit_cast(float, u & 0xffff0000u); }
DI u32 f2u(float x) { return __builtin_bit_cast(u32, x); }
DI float u2f(u32 x) { return __builtin_bit_cast(float, x); }
DI u32 nosent(u32 x) { return x < 0xfffffffeu ? x : 0xfffffffeu; }
DI v4u nosent4(v4u v) { return v4u{nosent(v.x), nosent(v.y), nosent(v.z), nosent(v.w)}; }
DI u32 umax(u32 a, u32 b) { return a > b ? a : b; }
DI u32 max4(u32 m, v4u v) { m = umax(umax(m, v.x), v.y); return umax(umax(m, v.z), v.w); }
DI float fmx(float a, float b) { return __builtin_elementwise_maximum(a, b); }
DI float fmn(float a, float b) { return __builtin_elementwise_minimum(a, b); }
DI float rsum(float x) {
  auto a = __builtin_amdgcn_permlane16_swap(f2u(x), f2u(x), false, false); x = u2f(a[0]) + u2f(a[1]);
  auto c = __builtin_amdgcn_permlane32_swap(f2u(x), f2u(x), false, false); return u2f(c[0]) + u2f(c[1]);
}
DI void bar() { SB(); asm volatile("s_waitcnt lgkmcnt(0)\n\ts_barrier" ::: "memory"); SB(); }
#define VMW(n) asm volatile("s_waitcnt vmcnt(" #n ")" ::: "memory")
DI rsrc_t mkrs(u64 p, u32 bytes) { return __builtin_amdgcn_make_buffer_rsrc((GAS void*)p, 0, (int)bytes, 0x00020000); }
DI v4u ldg(rsrc_t rs, u32 voff, u32 soff) { return __builtin_amdgcn_raw_buffer_load_b128(rs, (int)voff, (int)soff, SC1); }
DI void stg(rsrc_t rs, u32 off, v4u v) { __builtin_amdgcn_raw_buffer_store_b128(v, rs, (int)off, 0, SC1); }
template <int N, int DL>
DI void dma_in(rsrc_t rs, u32 voff, u32 soff, u32 ds, u32 lds) {
  asm volatile("s_mov_b32 m0, %4\n\ts_nop 0\n\tbuffer_load_dwordx4 %1, %2, %0 offen lds\n\t"
               ".rept %c5\n\ts_add_u32 m0, m0, %c6\n\ts_add_u32 %0, %0, %3\n\tbuffer_load_dwordx4 %1, %2, %0 offen lds\n\t.endr"
               : "+s"(soff) : "v"(voff), "s"(rs), "s"(ds), "s"(lds), "n"(N - 1), "n"(DL) : "memory", "scc");
}
template <int N, int DL>
DI void dma_nt(rsrc_t rs, u32 voff, u32 soff, u32 ds, u32 lds) {
  asm volatile("s_mov_b32 m0, %4\n\ts_nop 0\n\tbuffer_load_dwordx4 %1, %2, %0 offen nt lds\n\t"
               ".rept %c5\n\ts_add_u32 m0, m0, %c6\n\ts_add_u32 %0, %0, %3\n\tbuffer_load_dwordx4 %1, %2, %0 offen nt lds\n\t.endr"
               : "+s"(soff) : "v"(voff), "s"(rs), "s"(ds), "s"(lds), "n"(N - 1), "n"(DL) : "memory", "scc");
}
DI void touch(rsrc_t rs, u32 voff, u32 lds) {
  asm volatile("s_mov_b32 m0, %2\n\ts_nop 0\n\tbuffer_load_dword %0, %1, 0 offen sc1 lds" :: "v"(voff), "s"(rs), "s"(lds) : "memory");
}
template <int N>
DI void dma_ws(rsrc_t rs, u32 voff, u32 soff, u32 lds) {
#define L4_ "buffer_load_dwordx4 %1, %2, %0 offen sc1 lds\n\tbuffer_load_dwordx4 %1, %2, %0 offen offset:1024 sc1 lds\n\t" \
            "buffer_load_dwordx4 %1, %2, %0 offen offset:2048 sc1 lds\n\tbuffer_load_dwordx4 %1, %2, %0 offen offset:3072 sc1 lds\n\t"
  asm volatile("s_mov_b32 m0, %3\n\ts_nop 0\n\t" L4_
               ".rept %c4\n\ts_add_u32 m0, m0, 0x1000\n\ts_add_u32 %0, %0, 0x1000\n\t" L4_ ".endr"
               : "+s"(soff) : "v"(voff), "s"(rs), "s"(lds), "n"(N - 1) : "memory", "scc");
}
DI void poll(rsrc_t rs, u32 voff, u64 want) {
  u64 t; u32 cnt = SPIN_CAP;
  asm volatile("1:\n\tbuffer_load_dwordx2 %0, %2, %3, 0 offen sc1\n\ts_waitcnt vmcnt(0)\n\tv_cmp_ne_u64 vcc, %4, %0\n\ts_cbranch_vccz 2f\n\t"
               "s_sleep 1\n\ts_sub_u32 %1, %1, 1\n\ts_cbranch_scc0 1b\n2:"
               : "=&v"(t), "+s"(cnt) : "v"(voff), "s"(rs), "s"(want) : "vcc", "scc", "memory");
}
DI u32 now() { u64 t; asm volatile("s_memrealtime %0\n\ts_waitcnt lgkmcnt(0)" : "=s"(t) :: "memory"); return (u32)t; }
DI void peek_issue(rsrc_t rs, u32 voff) {
  asm volatile("buffer_load_dwordx4 v[248:251], %0, %1, 0 offen sc1" :: "v"(voff), "s"(rs) : "v248", "v249", "v250", "v251", "memory");
}
template <int N>
DI u32 peek_take(rsrc_t rs, u32 voff, u64 want) {
  v4u t; u32 cnt = SPIN_CAP;
  asm volatile("s_waitcnt vmcnt(%5)\n1:\n\tv_cmp_ne_u64 vcc, %4, v[248:249]\n\ts_cbranch_vccz 2f\n\t"
               "buffer_load_dwordx4 v[248:251], %2, %3, 0 offen sc1\n\ts_waitcnt vmcnt(0)\n\ts_sub_u32 %1, %1, 1\n\ts_cbranch_scc0 1b\n2:"
               : "=&{v[248:251]}"(t), "+s"(cnt) : "v"(voff), "s"(rs), "s"(want), "n"(N) : "vcc", "scc", "memory");
  return t.z;
}
template <int N, int DT>
DI void pace(rsrc_t rs, u32 voff, u64 want, u32 own) {
  u32 cnt = SPIN_CAP, c2 = 256, a;
  asm volatile("s_memrealtime s[90:91]\n\ts_waitcnt vmcnt(%7)\n1:\n\tv_cmp_ne_u64 vcc, %5, v[248:249]\n\ts_cbranch_vccz 2f\n\t"
               "buffer_load_dwordx4 v[248:251], %3, %4, 0 offen sc1\n\ts_waitcnt vmcnt(0)\n\ts_sub_u32 %0, %0, 1\n\ts_cbranch_scc0 1b\n2:\n\t"
               "v_readlane_b32 %2, v250, %6\n\ts_waitcnt lgkmcnt(0)\n\ts_sub_u32 %2, s90, %2\n\ts_add_u32 %2, %2, %8\n\tv_add_u32 v250, %2, v250\n"
               "3:\n\ts_memrealtime s[90:91]\n\ts_waitcnt lgkmcnt(0)\n\tv_sub_u32 v251, s90, v250\n\tv_cmp_gt_i32 vcc, 0, v251\n\ts_cbranch_vccz 4f\n\t"
               "s_sub_u32 %1, %1, 1\n\ts_cbranch_scc0 3b\n4:"
               : "+s"(cnt), "+s"(c2), "=&s"(a) : "v"(voff), "s"(rs), "s"(want), "s"(own), "n"(N), "n"(DT)
               : "v248", "v249", "v250", "v251", "s90", "s91", "vcc", "scc", "memory");
}
DI void wait_until(u32 when) {
  u64 t; u32 d, cnt = 256;
  asm volatile("1:\n\ts_memrealtime %0\n\ts_waitcnt lgkmcnt(0)\n\tv_sub_u32 %1, s90, %3\n\tv_cmp_gt_i32 vcc, 0, %1\n\ts_cbranch_vccz 2f\n\t"
               "s_sub_u32 %2, %2, 1\n\ts_cbranch_scc0 1b\n2:"
               : "=&{s[90:91]}"(t), "=&v"(d), "+s"(cnt) : "v"(when) : "vcc", "scc", "memory");
}
constexpr float GC0 = -2.0f * 0.7978845608028654f * 1.4426950408889634f, GC1 = GC0 * 0.044715f;
DI float gelu(float x) {
  const float e = __builtin_amdgcn_exp2f(x * fmaf(x * x, GC1, GC0));
  return x * __builtin_amdgcn_rcpf(1.0f + e);
}

DI void hedgehog(f4 p0, f4 p1, float keep, v4u& pos, v4u& neg) {
  const float L2E = 1.4426950408889634f;
  float mx = fmx(fmx(fmx(fmx(p0[0], p0[1]), p0[2]), fmx(fmx(p0[3], p1[0]), p1[1])), fmx(p1[2], p1[3]));
  float nm = -fmn(fmn(fmn(fmn(p0[0], p0[1]), p0[2]), fmn(fmn(p0[3], p1[0]), p1[1])), fmn(p1[2], p1[3]));
  {
    auto a = __builtin_amdgcn_permlane16_swap(f2u(mx), f2u(nm), false, false);
    float t = fmx(u2f(a[0]), u2f(a[1]));
    auto c = __builtin_amdgcn_permlane32_swap(f2u(t), f2u(t), false, false);
    t = fmx(u2f(c[0]), u2f(c[1])) * -L2E;
    auto d = __builtin_amdgcn_permlane16_swap(f2u(t), f2u(t), false, false);
    mx = u2f(d[0]); nm = u2f(d[1]);
  }
  float a[8], c[8];
#pragma unroll
  for (int r = 0; r < 4; ++r) {
    a[r] = __builtin_amdgcn_exp2f(fmaf(p0[r], L2E, mx));
    c[r] = __builtin_amdgcn_exp2f(fmaf(p0[r], -L2E, nm));
    a[4 + r] = __builtin_amdgcn_exp2f(fmaf(p1[r], L2E, mx));
    c[4 + r] = __builtin_amdgcn_exp2f(fmaf(p1[r], -L2E, nm));
  }
  float sa = fmaf((a[4] + a[5]) + (a[6] + a[7]), keep, (a[0] + a[1]) + (a[2] + a[3]));
  float sc = fmaf((c[4] + c[5]) + (c[6] + c[7]), keep, (c[0] + c[1]) + (c[2] + c[3]));
  {
    auto x = __builtin_amdgcn_permlane16_swap(f2u(sa), f2u(sc), false, false);
    float t = u2f(x[0]) + u2f(x[1]);
    auto y = __builtin_amdgcn_permlane32_swap(f2u(t), f2u(t), false, false);
    t = __builtin_amdgcn_rcpf(u2f(y[0]) + u2f(y[1]));
    auto d = __builtin_amdgcn_permlane16_swap(f2u(t), f2u(t), false, false);
    sa = u2f(d[0]); sc = u2f(d[1]);
  }
  const float sa1 = sa * keep, sc1 = sc * keep;
  pos = v4u{pk2(a[0] * sa, a[1] * sa), pk2(a[2] * sa, a[3] * sa), pk2(a[4] * sa1, a[5] * sa1), pk2(a[6] * sa1, a[7] * sa1)};
  neg = v4u{pk2(c[0] * sc, c[1] * sc), pk2(c[2] * sc, c[3] * sc), pk2(c[4] * sc1, c[5] * sc1), pk2(c[6] * sc1, c[7] * sc1)};
}
DI v4u pack8(f4 a, f4 b) { return v4u{pk2(a[0], a[1]), pk2(a[2], a[3]), pk2(b[0], b[1]), pk2(b[2], b[3])}; }
DI v4u to_chunk(v4u p) {
  auto x = __builtin_amdgcn_permlane16_swap(p.x, p.z, false, false);
  auto y = __builtin_amdgcn_permlane16_swap(p.y, p.w, false, false);
  return v4u{x[0], y[0], x[1], y[1]};
}
DI float diag(f4 q, int p4) {
  const float a = q[0], b = q[1], c = q[2], d = q[3];
  const float t0 = (p4 & 1) ? b : a, t1 = (p4 & 1) ? d : c;
  return (p4 & 2) ? t1 : t0;
}
DI v4u ld16(u32 addr) { return *(LAS v4u*)(uintptr_t)addr; }
DI void st16(u32 addr, v4u v) { *(LAS v4u*)(uintptr_t)addr = v; }
DI void st8(u32 addr, u32 a, u32 b) { *(LAS v2u*)(uintptr_t)addr = v2u{a, b}; }
DI f2 ldf2(u32 addr) { return *(LAS f2*)(uintptr_t)addr; }
DI v2u trread(u32 addr) {
  return __builtin_bit_cast(v2u, __builtin_amdgcn_ds_read_tr16_b64_v4i16((LAS s4v*)(uintptr_t)addr));
}
DI float dot2(u32 a, u32 sel, float c) {
  return __builtin_amdgcn_fdot2_f32_bf16(__builtin_bit_cast(bf2, a), __builtin_bit_cast(bf2, sel), c, false);
}

struct TrBuf { v2u r[3][4]; };
DI void tr_open(TrBuf& q, u32 t0, u32 t1) {
#pragma unroll
  for (int k = 0; k < 3; ++k) { q.r[k][0] = trread(t0 + k * 4096); q.r[k][1] = trread(t0 + k * 4096 + 512); q.r[k][2] = trread(t1 + k * 4096); q.r[k][3] = trread(t1 + k * 4096 + 512); }
  SB();
}
template <class PF>
DI void gemm_tr(TrBuf& q, u32 t0, u32 t1, const v4u (&xa)[7], const v4u (&xb)[7], f4& a0, f4& a1, f4& b0, f4& b1, PF pf) {
  constexpr int P = 3;
  a0 = f4{0, 0, 0, 0}; a1 = a0; b0 = a0; b1 = a0;
#pragma unroll
  for (int s = 0; s < 7; ++s) {
    const int k = s % P;
    const v4u f0 = {q.r[k][0].x, q.r[k][0].y, q.r[k][1].x, q.r[k][1].y}, f1 = {q.r[k][2].x, q.r[k][2].y, q.r[k][3].x, q.r[k][3].y};
    b1 = mfma(f1, xb[s], b1); b0 = mfma(f0, xb[s], b0);
    a1 = mfma(f1, xa[s], a1); a0 = mfma(f0, xa[s], a0);
    SB();
    if (s + P < 7) {
      q.r[k][0] = trread(t0 + (s + P) * 4096); q.r[k][1] = trread(t0 + (s + P) * 4096 + 512);
      q.r[k][2] = trread(t1 + (s + P) * 4096); q.r[k][3] = trread(t1 + (s + P) * 4096 + 512);
    }
    pf(s);
    SB();
  }
}
template <int NS, class PF>
DI void gemm_alds(u32 p, const v4u (&wf)[NS], f4& a0, f4& a1, PF pf) {
  constexpr int P = 6;
  v4u b0[P], b1[P];
#pragma unroll
  for (int k = 0; k < P; ++k) { b0[k] = ld16(p + 2 * k * KB); b1[k] = ld16(p + (2 * k + 1) * KB); }
  SB();
#pragma unroll
  for (int s = 0; s < NS; ++s) {
    a1 = mfma(wf[s], b1[s % P], a1);
    a0 = mfma(wf[s], b0[s % P], a0);
    SB();
    if (s == 2) {
      asm volatile("s_barrier" ::: "memory");
#pragma unroll
      for (int k = 0; k < 3; ++k) { b0[k] = ld16(p + 2 * (k + P) * KB); b1[k] = ld16(p + (2 * (k + P) + 1) * KB); }
    }
    if (s > 2 && s + P < NS) { b0[s % P] = ld16(p + 2 * (s + P) * KB); b1[s % P] = ld16(p + (2 * (s + P) + 1) * KB); }
    pf(s);
    SB();
  }
}
template <int J0, int J1, int EXTRA, bool ST>
DI bool g_check(u32 p, f4 (&sm)[4]) {
  const v4u ones = {0x3f803f80u, 0x3f803f80u, 0x3f803f80u, 0x3f803f80u};
  constexpr int LA = 2;
  v4u fr[4 * (J1 - J0)];
  u32 m = 0;
  sfor<J0, J1 + LA>([&](auto J) AI {
    constexpr int j = J.v;
    if constexpr (j < J1) {
      if constexpr (EXTRA >= 0) asm volatile("s_waitcnt vmcnt(%0)" :: "n"(EXTRA + 4 * (J1 - 1 - j)) : "memory");
#pragma unroll
      for (int e = 0; e < 4; ++e) fr[4 * (j - J0) + e] = ld16(p + (4 * j + e) * KB);
      SB();
    }
    constexpr int d = J0 == 0 ? 1 : LA;
    if constexpr (j >= J0 + d && j < J1 + d) {
#pragma unroll
      for (int e = 3; e >= 0; --e) {
        const v4u f = fr[4 * (j - d - J0) + e];
        if (ST) { sm[e & 1] = mfma(ones, f, sm[e & 1]); sm[2 + (e & 1)] = mfma(f, f, sm[2 + (e & 1)]); }
        else m = max4(m, f);
      }
      SB();
    }
  });
  if (ST) {
    const float t = sm[0][0] + sm[1][0];
    if (__all(t == t)) return true;
#pragma unroll
    for (int k = 0; k < 4 * (J1 - J0); ++k) m = max4(m, fr[k]);
  }
  return __all(m != 0xffffffffu);
}
#define RETRY_LOOP _Pragma("clang loop unroll(disable)") for (u32 sp = 0;; ++sp)
#define RETRY_END(k) NRETRY(k); if (sp > SPIN_CAP) break; asm volatile("s_waitcnt lgkmcnt(0)" ::: "memory")

__global__ void __launch_bounds__(512, 2)
fused_kernel(const u16* x_, const u16* wqkv_, const u16* wphi_, const u16* wo_a, const u16* w1_a, const u16* w2_a, const u16* w3_a,
             const u16* w4_a, u16* out_, unsigned char* ws_) {
  u64 p_x, p_q, p_phi, p_out, p_ws;
  asm volatile("s_load_dwordx2 %0, %5, 0\n\ts_load_dwordx2 %1, %5, 8\n\ts_load_dwordx2 %4, %5, 0x48\n\ts_load_dwordx2 %2, %5, 0x10\n\ts_load_dwordx2 %3, %5, 0x40"
               : "=&s"(p_x), "=&s"(p_q), "=&s"(p_phi), "=&s"(p_out), "=&s"(p_ws) : "s"(__builtin_amdgcn_kernarg_segment_ptr()));
  __shared__ __attribute__((aligned(1024))) unsigned char lds_raw[L_BYTES];
  const u32 L = (u32)(uintptr_t)(LAS unsigned char*)lds_raw;
  const int tid = threadIdx.x, lane = tid & 63, w = __builtin_amdgcn_readfirstlane(tid >> 6), g = lane >> 4, i16 = lane & 15;
  const int b = blockIdx.x, xcd = b & 7, ii = b >> 3;
  const int zg = xcd * 8 + (ii >> 2), js = ii & 3;
  const int yg = xcd * 4 + (ii >> 3), mi = ii & 7, ms = (mi + (ii >> 3)) & 7;
  const int th = (ii >> 2) & 1;
  const u32 l16 = lane * 16;
  u64 pr0; asm volatile("s_memrealtime %0" : "=s"(pr0));
  u64 pr1 = 0;
  const int u = w >> 2, hh = w & 1, T = (w & 2) + (hh ^ u), head = mi * 2 + hh;
  if (w == 4) {
    u64 pc; asm volatile("s_getpc_b64 %0" : "=s"(pc));
    touch(mkrs(pc & ~255ull, CODE_BYTES), (ii + 32 * lane) * 128, L + L_DUMP);
  }
  u32 xs0 = (yg * 64 + w * 16 + (mi & 1) * 8) * 896, xl0 = L + L_X + w * SLOT + (mi & 1) * 8 * X_RS;
  u32 xs1 = (yg * 64 + w * 16 + (~mi & 1) * 8) * 896, xl1 = L + L_X + w * SLOT + (~mi & 1) * 8 * X_RS;
  const u32 tn = w == 5 ? 37 : 4, tb = w == 5 ? 0 : OFF_HINT;
  u32 tv = (tb + (b * tn + lane) * 128) | ((u32)lane < tn ? 0 : OOB);
  PINS(xs0); PINS(xl0); PINS(xs1); PINS(xl1); PIN(tv);
  SB();
  asm volatile("s_waitcnt lgkmcnt(0)" : "+s"(p_x), "+s"(p_q), "+s"(p_phi), "+s"(p_out), "+s"(p_ws));
  const rsrc_t rx = mkrs(p_x, S_ * D_ * 2);
  const rsrc_t rq = mkrs(p_q, D_ * 3 * D_ * 2);
  const rsrc_t rws = mkrs(p_ws, WS_BYTES);
  if (u == 0) {
    __builtin_amdgcn_s_setprio(2);
    asm volatile("s_bfm_b64 exec, 56, 0" ::: "memory");
    XDMA<8, X_RS>(rx, l16, xs0, 896, xl0);
    XDMA<8, X_RS>(rx, l16, xs1, 896, xl1);
    asm volatile("s_mov_b64 exec, -1" ::: "memory");
  } else if (w == 5) {
    __builtin_amdgcn_s_setprio(3);
    touch(rq, tv, L + L_DUMP);
    __builtin_amdgcn_s_setprio(0);
  } else if (w == 6) {
    __builtin_amdgcn_s_setprio(3);
    touch(rws, tv, L + L_DUMP);
    __builtin_amdgcn_s_setprio(0);
  }
  SB();
  const u32 wc = (lane & 7) ^ ((lane >> 3) & 2) ^ ((w & 1) << 2);
  u32 wv = (((w & 3) * 8 + (lane >> 3)) * 2688 + mi * 112 + wc * 16) | (wc < 7 ? 0 : OOB);
  u32 wl = L + (w & 3) * KB;
  PIN(wv); PINS(wl);
  SB();
  STAMP(24);
  asm volatile("s_memrealtime %0\n\ts_waitcnt lgkmcnt(0)" : "=s"(pr1) :: "memory");
  u32 epoch;
  asm volatile("s_load_dword %0, %1, 0" : "=s"(epoch) : "s"(p_ws));
  const v4u SENT = {0xffffffffu, 0xffffffffu, 0xffffffffu, 0xffffffffu};
  const v4u ones = {0x3f803f80u, 0x3f803f80u, 0x3f803f80u, 0x3f803f80u};
  const f4 z4 = {0, 0, 0, 0};
  const int q4 = i16 >> 2, p4 = i16 & 3;
  const bool padg = g == 3;
  u32 vz = lane == 0 ? 0 : OOB;
  PIN(vz);
  auto post = [&](u32 off) AI { __builtin_amdgcn_raw_buffer_store_b64(v2u{epoch, 0x9e3779b9u}, rws, (int)vz, (int)off, SC1); };
  auto post_t = [&](u32 off) AI { const u32 t = now(); stg(rws, vz + off, v4u{epoch, 0x9e3779b9u, t, 0}); return t; };
  const int n = js * 7 + (w < 7 ? w : 0);
  u32 kvp_off = OFF_KVP + ((mi * 8 + w) * 4 + g) * 8 * KB + yg * 256 + i16 * 16;
  u32 kvt_off = g == 0 ? OFF_KVT + (mi * 32 + yg) * 256 + i16 * 16 : OOB;
  u32 kvt8 = g < 2 ? OFF_KVT + (mi * 32 + yg) * 256 + i16 * 16 + g * 8 : OOB;
  PIN(kvt8);
  u32 o_off = l16 + OFF_O + (((yg * 2 + (T >> 1)) * 16 + head) * 2 + (T & 1)) * KB;
  const u32 cko = (2 * (n & 1) + (g >> 1)) * 256;
  u32 h_off = ((zg * 14 + (n >> 1)) * 2 + (g & 1)) * KB + cko + i16 * 16 + OFF_H;
  PIN(h_off); PIN(kvp_off); PIN(o_off);
  PIN(kvt_off);
  float keep = padg ? 0.f : 1.f;
  const u32 trx = (((q4 >> 1) & 1) | ((g & 1) << 1)) * 32;
  u32 tr0 = L + (8 * g + q4) * 128 + ((56 * hh + 8 * p4) ^ trx);
  u32 tr1 = L + (8 * g + q4) * 128 + ((56 * hh + 32 + 8 * (p4 == 3 ? 2 : p4)) ^ trx);
  u32 kx_w = L + L_KX + (w ^ 4) * KB + l16, kx_r = L + L_KX + w * KB + l16;
  u32 pk_w = L + L_PK + (hh * 64 + T * 16 + i16) * PK_RS + g * 8;
  u32 va_w = L + (u ? L_V1 : L_V0) + (hh * 64 + T * 16 + i16) * V_RS + g * 8, vb_w = L + (u ? L_V1 : L_V0) + (hh * 64 + (T ^ 1) * 16 + i16) * V_RS + g * 8;
  u32 kv_a = L + L_PK + (u * 64 + g * 8 + q4) * PK_RS + ((w >> 1) & 1) * 64 + p4 * 8;
  u32 kv_b = L + L_V0 + (u * 64 + g * 8 + q4) * V_RS + (w & 1) * 32 + p4 * 8, kv_c = kv_b + (L_V1 - L_V0);
  u32 ph_a = L + L_PHI + hh * 1568 + (4 * g + q4) * 56 + p4 * 8;
  u32 xr_r = L + L_VA + i16 * 256 + ((((w < 7 ? w : 0) * 2 + (g >> 1)) ^ i16) * 16) + (g & 1) * 8;
  PIN(xr_r);
  u32 xo_r = L + L_X + (T ^ 1) * SLOT + u * 7 * KB + l16, xm_r = L + L_X + T * SLOT + 7 * KB + l16, ph_b = ph_a + (p4 == 3 ? 24 : 32);
  u32 tk0 = tr0 + L_K + u * 28 * KB, tk1 = tr1 + L_K + u * 28 * KB, tv0 = tr0 + (u ? L_VB : L_VA), tv1 = tr1 + (u ? L_VB : L_VA);
  u32 tq0 = tr0 + (u ? L_QB : L_QA), tq1 = tr1 + (u ? L_QB : L_QA);
  PIN(xm_r); PIN(ph_b); PIN(tk0); PIN(tk1); PIN(tv0); PIN(tv1); PIN(tq0); PIN(tq1);
  u32 h32 = OFF_HINT + (mi * 32 + (lane & 31)) * 64, uf_off = OFF_HINT + 4 * 256 * 64 + 32 + lane * 64;
  PIN(uf_off);
  PIN(keep); PIN(kx_w); PIN(kx_r);
  PIN(pk_w); PIN(va_w); PIN(vb_w); PIN(kv_a); PIN(kv_b); PIN(kv_c); PIN(ph_a); PIN(xo_r); PIN(h32);
  SB();
  const bool light = ((mi + 1) & 6) == 0;
  const u32 lid = yg * 2 + (mi & 1);
  const u32 nx = light ? ((lid & 15) == 5 ? 2 : 1) : 0;
  u32 u_ts = 0, u_dst = 0, u_lo = 0, u_hi = 0, u_keep = 0;
  if (u == 1) {
    const u32 unit = (light ? lid : 64 + yg * 6 + mi - 1) * 4 + (w - 4);
    const u32 sec = ((unit + 1936) >> 11) + ((unit + 1838) >> 11) + ((unit + 1740) >> 11) + ((unit + 1348) >> 11);
    const u64 base = ((const __attribute__((address_space(4))) u64*)__builtin_amdgcn_kernarg_segment_ptr())[3 + sec];
    const u32 first = (u32)(0x2bc1340d2070000ull >> (sec * 12)) & 0xfffu, uu = unit - first;
    const u32 sh3 = sec == 3 ? 2 : 0;
    const bool prm = (sec & 3) == 0;
    const u32 u7 = (uu * 9363) >> 16, st = u7 >> sh3, cb = uu - ((st * 7) << sh3), m = sec == 4 ? (u7 * 9363) >> 16 : 0;
    const rsrc_t rs = mkrs(base, sec >= 3 ? D_ * F_ * 2 : D_ * D_ * 2);
    const u32 vo = (st * (sec == 0 ? 28 : 32) + (lane >> 3)) * (896 << sh3) + cb * 128 + (lane & 7) * 16;
    const u32 r0 = (prm ? 4 * g : 8 * g) + q4;
    u_ts = ((0x7eee0u >> (sec * 4)) & 15) * KB + (sec == 0 ? 16 * KB : 0);
    u_dst = l16 + OFF_WO + first * 4 * KB + cb * 4 * u_ts + (m * 189 + st) * KB;
    u_lo = wl + L_VA + ((r0 >> 3) << 12) + (r0 & 7) * 128 + p4 * 8;
    u_hi = u_lo + (prm ? 8192 : 512);
    u_keep = (sec == 0 && padg) ? 0u : 0xffffffffu;
    dma_nt<4, 4 * KB>(rs, vo, 0, (8 * 896) << sh3, wl + L_VA);
  }
  PINS(u_ts); PIN(u_dst); PIN(u_lo); PIN(u_hi); PIN(u_keep);
  SB();
  STAMP(46);
  v4u xa[7], xb[7], uq[4];
  if (u == 0) {
    u32 xp = L + L_X + w * SLOT + i16 * X_RS + g * 16, xw = L + L_X + w * SLOT + l16;
    PIN(xp); PIN(xw);
    VMW(0);
    STAMP(25);
#pragma unroll
    for (int s = 0; s < 7; ++s) xb[s] = ld16(xp + 448 + s * 64);
#pragma unroll
    for (int s = 0; s < 7; ++s) xa[s] = ld16(xp + s * 64);
    SB();
    f4 sq = z4, sm = z4;
#pragma unroll
    for (int s = 0; s < 7; ++s) { sq = mfma(xb[s], xb[s], sq); sm = mfma(ones, xb[s], sm); }
#pragma unroll
    for (int s = 0; s < 7; ++s) { sq = mfma(xa[s], xa[s], sq); sm = mfma(ones, xa[s], sm); }
    const float mu = sm[0] * (1.0f / D_), dgv = diag(sq, p4);
    const float var = fmx(fmaf(-mu, mu, rsum(g == q4 ? dgv : 0.f) * (1.0f / D_)), 0.f);
    const float rs = __builtin_amdgcn_rsqf(var + 1e-5f), nb = -mu * rs;
    STAMP(26);
#pragma unroll
    for (int s = 0; s < 7; ++s) {
#pragma unroll
      for (int e = 0; e < 4; ++e) xb[s][e] = pk2(fmaf(blo(xb[s][e]), rs, nb), fmaf(bhi(xb[s][e]), rs, nb));
      st16(xw + (7 + s) * KB, xb[s]);
      SB();
    }
#pragma unroll
    for (int s = 0; s < 7; ++s) {
#pragma unroll
      for (int e = 0; e < 4; ++e) xa[s][e] = pk2(fmaf(blo(xa[s][e]), rs, nb), fmaf(bhi(xa[s][e]), rs, nb));
      st16(xw + s * KB, xa[s]);
      SB();
    }
    __builtin_amdgcn_s_setprio(0);
  } else {
    dma_in<1, 0>(mkrs(p_phi, 16 * 28 * 28 * 2), l16, mi * 3136 + (w - 4) * KB, 0, L + L_PHI + (w - 4) * KB);
    dma_in<14, 4 * KB>(rq, wv, 896, 32 * 2688, wl + L_K);
    STAMP(25);
    VMW(15);
    v2u lo[4], hi[4];
#pragma unroll
    for (int t = 0; t < 4; ++t) { lo[t] = trread(u_lo + t * 32); hi[t] = trread(u_hi + t * 32); }
#pragma unroll
    for (int t = 0; t < 4; ++t) { uq[t] = v4u{lo[t].x, lo[t].y, hi[t].x & u_keep, hi[t].y & u_keep}; PIN(uq[t]); }
    SB();
    dma_in<4, 4 * KB>(rq, wv, 1792, 32 * 2688, wl + L_VA);
    VMW(11);
  }
  STAMP(27);
  bar();
  asm volatile("" : "+s"(epoch));
  const u64 tag = (u64)epoch | (0x9e3779b9ull << 32);
  STAMP(18);
#pragma unroll
  for (int s = 0; s < 7; ++s) xb[s] = ld16(xo_r + s * KB);
  if (u == 1) {
#pragma unroll
    for (int s = 0; s < 7; ++s) xa[s] = ld16(xm_r + s * KB);
  }
  v4u phA, phB;
  {
    const v2u l0 = trread(ph_a), h0 = trread(ph_a + 16 * 56), l1 = trread(ph_b), h1 = trread(ph_b + 16 * 56);
    phA = v4u{l0.x, l0.y, padg ? 0u : h0.x, padg ? 0u : h0.y};
    phB = v4u{l1.x, l1.y, padg ? 0u : h1.x, padg ? 0u : h1.y};
  }
  TrBuf tb_;
  u32 rt0 = 0, cy0 = 0;
  if (u == 1) {
    { u64 a, c; asm volatile("s_memrealtime %0\n\ts_memtime %1\n\ts_waitcnt lgkmcnt(0)" : "=&s"(a), "=&s"(c) :: "memory"); rt0 = (u32)a; cy0 = (u32)c; }
    dma_in<3, 4 * KB>(rq, wv, 1792 + 4 * 32 * 2688, 32 * 2688, wl + L_VA + 16 * KB);
#pragma unroll
    for (int t = 0; t < 4; ++t) stg(rws, u_dst + t * u_ts, uq[t]);
    VMW(11);
    bar();
  }
  tr_open(tb_, tk0, tk1);
  PIN(phA); PIN(phB);
  STAMP(1);
  v4u wo_[16], w1_[14], w2_[14], w3f[28], w4f[28];
  u32 lw = w < 7 ? l16 : OOB;
  PIN(lw);
  auto pf = [&](int k) AI {
    if (k < 16) wo_[k] = ldg(rws, lw + (k & 3) * KB, OFF_WO + (n * 16 + (k & ~3)) * KB);
    else if (k < 30) w1_[k - 16] = ldg(rws, lw + (k & 3) * KB, OFF_W1 + (n * 14 + ((k - 16) & ~3)) * KB);
    else if (k < 44) w2_[k - 30] = ldg(rws, lw + ((k - 30) & 3) * KB, OFF_W2 + (n * 14 + ((k - 30) & ~3)) * KB);
    else if (k < 72) { const int t = (k - 44) & 1, st = (k - 44) >> 1;
      w3f[t * 14 + st] = ldg(rws, lw + (st & 3) * KB, OFF_W3 + ((ms * 14 + w * 2 + t) * 14 + (st & ~3)) * KB); }
    else if (k < 100) { const int sl = (k - 72) / 7, c = (k - 72) % 7;
      w4f[k - 72] = ldg(rws, (lw | NOW4) + (c & 3) * KB, OFF_W4 + ((ms * 28 + ((js + 1 + sl) & 3) * 7 + w) * 7 + (c & ~3)) * KB); }
  };
#define PFR(lo, hi) do { _Pragma("unroll") for (int k_ = (lo); k_ < (hi); ++k_) pf(k_); asm volatile("" ::: "memory"); SB(); } while (0)
#define PFS(base, cnt) [&](int s_) AI { if ((s_ & 1) == 0 && (s_ >> 1) < (cnt)) pf((base) + (s_ >> 1)); }
  {
    STAMP(20);
    f4 a0, a1, b0, b1;
    gemm_tr(tb_, tk0, tk1, xa, xb, a0, a1, b0, b1, [&](int s_) AI { if (s_ == 4) asm volatile("s_cmp_lg_u32 %0, 0\n\ts_cbranch_scc1 1f\n\ts_barrier\n1:" :: "s"(u) : "memory", "scc"); });
    st16(kx_w, pack8(b0, b1));
    const v4u ka = pack8(a0, a1);
    f4 p0 = mfma(phA, ka, z4), p1 = mfma(phB, ka, z4);
    STAMP(2);
    u32 qs = u * 224 * 2688, ql = wl + (u ? L_QB : L_QA);
    PINS(qs); PINS(ql);
    auto v_part = [&]() AI {
      gemm_tr(tb_, tv0, tv1, xa, xb, a0, a1, b0, b1, [&](int) AI {
        asm volatile("s_mov_b32 m0, %1\n\ts_add_u32 %1, %1, 0x1000\n\tbuffer_load_dwordx4 %2, %3, %0 offen lds\n\ts_add_u32 %0, %0, %4"
                     : "+s"(qs), "+s"(ql) : "v"(wv), "s"(rq), "n"(32 * 2688) : "memory", "scc"); });
    };
    auto v_out = [&](float one) AI {
      if (padg) { a1[0] = one; b1[0] = one; }
      const v4u va = pack8(a0, a1), vb = pack8(b0, b1);
      st8(vb_w, vb.x, vb.y); st8(vb_w + 32, vb.z, vb.w);
      st8(va_w, va.x, va.y); st8(va_w + 32, va.z, va.w);
    };
    auto k_part = [&]() AI {
      v4u pos, neg;
      hedgehog(p0, p1, keep, pos, neg);
      st8(pk_w, pos.x, pos.y); st8(pk_w + 32, pos.z, pos.w); st8(pk_w + 64, neg.x, neg.y); st8(pk_w + 96, neg.z, neg.w);
    };
    if (u == 0) {
      dma_in<7, 4 * KB>(rq, wv, 1792 + 224 * 2688, 32 * 2688, wl + L_VB);
      STAMP(19);
      bar();
      STAMP(3);
      tr_open(tb_, tv0, tv1);
      const v4u kb = ld16(kx_r);
      SB();
      v_part();
      p0 = mfma(phA, kb, p0); p1 = mfma(phB, kb, p1);
      v_out(1.f);
      STAMP(21);
      VMW(7);
      bar();
      STAMP(22);
      for (u32 e = 0; e < nx; ++e) {
        const u32 v = e == 0 ? 324 + lid : 388 + (lid >> 4), s2 = (v * 9363) >> 16, c2 = v - s2 * 7;
        const u32 a = L + (e == 0 ? L_U0 : L_PHI) + (4 * g + q4) * 128 + p4 * 8 + w * 32;
        const v2u lo = trread(a), hi = trread(a + 2048);
        stg(rws, l16 + OFF_W4 + (c2 * 28 + ((s2 * 9363) >> 16) * 189 + s2 + w * 7) * KB, v4u{lo.x, lo.y, hi.x, hi.y});
      }
      k_part();
#pragma unroll
      for (int d = 0; d < 2; ++d) {
        const u32 c = i16 ^ ((w & 1) * 8 + d * 4 + g);
        dma_in<1, 0>(rx, (g * 896 + js * 224 + c * 16) | (c < 14 ? 0 : OOB), (zg * 32 + w * 8 + d * 4) * 896, 0, L + L_VA + (w * 2 + d) * KB);
      }
      if (w == 3) peek_issue(rws, uf_off);
    } else {
      if (light) VMW(0); else VMW(4);
      stg(rws, kvp_off, SENT); stg(rws, kvp_off - 4 * 32 * KB, SENT);
      if (w == 7) stg(rws, kvt_off, SENT);
      STAMP(19);
      bar();
      STAMP(3);
      const v4u kb = ld16(kx_r);
      if ((u32)(w - 5) < nx) {
        const u32 v = w == 5 ? 324 + lid : 388 + (lid >> 4), s2 = (v * 9363) >> 16, c2 = v - s2 * 7;
        dma_nt<4, KB>(mkrs(((const __attribute__((address_space(4))) u64*)__builtin_amdgcn_kernarg_segment_ptr())[7], D_ * F_ * 2),
                      (s2 * 32 + (lane >> 3)) * 896 + c2 * 128 + (lane & 7) * 16, 0, 8 * 896, L + (w == 5 ? L_U0 : L_PHI));
      }
      if (light && w == 4) post(OFF_HINT + (4 * 256 + lid) * 64 + 32);
      p0 = mfma(phA, kb, p0); p1 = mfma(phB, kb, p1);
      k_part();
      STAMP(21);
      VMW(0);
      if (OM == 1) { stg(rws, o_off, SENT); stg(rws, o_off ^ KB, SENT); }
      bar();
      STAMP(22);
      tr_open(tb_, tv0, tv1);
      if (w == 5) {
        u64 a, c; asm volatile("s_memrealtime %0\n\ts_memtime %1\n\ts_waitcnt lgkmcnt(0)" : "=&s"(a), "=&s"(c) :: "memory");
        const u32 t = (u32)a + (u32)((float)KVC * (float)((u32)a - rt0) * __builtin_amdgcn_rcpf((float)((u32)c - cy0)));
        stg(rws, vz + OFF_HINT + (5 * 256 + mi * 32 + yg) * 64, v4u{epoch, 0x9e3779b9u, t, 0});
      }
      SB();
      v_part();
      v_out(0.f);
    }
  }
  STAMP(23);
  bar();
  STAMP(4);
  if (w == 7) peek_issue(rws, h32 + 5 * 256 * 64);
  {
    v2u a0[2], a1[2], b0[2], b1[2], d0[4], d1[4];
#pragma unroll
    for (int s = 0; s < 2; ++s) {
      a0[s] = trread(kv_a + s * 32 * PK_RS); a1[s] = trread(kv_a + (s * 32 + 4) * PK_RS);
      d0[s] = trread(kv_b + s * 32 * V_RS); d1[s] = trread(kv_b + (s * 32 + 4) * V_RS);
      b0[s] = trread(kv_a + s * 32 * PK_RS + 32); b1[s] = trread(kv_a + (s * 32 + 4) * PK_RS + 32);
    }
#pragma unroll
    for (int s = 0; s < 2; ++s) { d0[2 + s] = trread(kv_c + s * 32 * V_RS); d1[2 + s] = trread(kv_c + (s * 32 + 4) * V_RS); }
    SB();
    f4 c0 = z4, c1 = z4;
#pragma unroll
    for (int s = 0; s < 4; ++s) {
      const v4u vf = {d0[s].x, d0[s].y, d1[s].x, d1[s].y};
      c0 = mfma(v4u{a0[s & 1].x, a0[s & 1].y, a1[s & 1].x, a1[s & 1].y}, vf, c0);
      c1 = mfma(v4u{b0[s & 1].x, b0[s & 1].y, b1[s & 1].x, b1[s & 1].y}, vf, c1);
    }
    stg(rws, kvp_off, nosent4(pack8(c0, c1)));
    SB();
  }
  RSTAMP(60);
  VMW(1);
  if (w == 3) peek_take<1>(rws, uf_off, tag);
  bar();
  STAMP(29);
  u32 t_kv = 0;
  if (w == 7) {
    __builtin_amdgcn_s_setprio(3);
    t_kv = peek_take<1>(rws, h32 + 5 * 256 * 64, tag) + DTX;
    wait_until(t_kv);
    RSTAMP(56);
    dma_ws<2>(rws, l16, OFF_KVP + (mi * 32 + yg) * 8 * KB, L + L_KP);
    const u32 om = l16 + OFF_O + (yg * 64 + mi * 4) * KB;
#pragma unroll
    for (int k = 0; k < 8; ++k) if (OM == 0) stg(rws, om + ((k >> 2) * 32 + (k & 3)) * KB, SENT);
    STAMP(6);
  }
  u32 h8 = OFF_HINT + (yg * 8 + (lane & 7)) * 64;
  PIN(h8);
  f4 pq0, pq1;
  {
    f4 a0, a1, b0, b1;
    tr_open(tb_, tq0, tq1);
    gemm_tr(tb_, tq0, tq1, xa, xb, a0, a1, b0, b1, [&](int s_) AI { if (s_ < QPF) pf(s_); });
    st16(kx_w - (L_KX - L_QX), pack8(b0, b1));
    const v4u qa = pack8(a0, a1);
    pq0 = mfma(phA, qa, z4); pq1 = mfma(phB, qa, z4);
    PIN(pq0); PIN(pq1);
  }
  STAMP(5);
  v4u qpos, qneg;
  auto phi_q = [&]() AI {
    const v4u qb = ld16(kx_r - (L_KX - L_QX));
    hedgehog(mfma(phA, qb, pq0), mfma(phB, qb, pq1), keep, qpos, qneg);
    PIN(qpos); PIN(qneg);
    SB();
  };
  auto make_o = [&]() AI {
    v4u kf[4];
#pragma unroll
    for (int k = 0; k < 4; ++k) kf[k] = ld16(L + L_KF + (hh * 4 + k) * KB + l16);
    SB();
    f4 n1 = mfma(kf[1], qpos, z4), n0 = mfma(kf[0], qpos, z4);
    n1 = mfma(kf[3], qneg, n1); n0 = mfma(kf[2], qneg, n0);
    const float rd = __builtin_amdgcn_rcpf(rsum(padg ? n1[0] : 0.f) + 1e-6f);
    n0 *= rd; n1 *= rd;
    v4u ov = nosent4(pack8(n0, n1));
    if (padg) { ov.z = 0; ov.w = 0; }
    stg(rws, o_off, ov);
    SB();
  };
  const u32 hme = OFF_HINT + (zg * 4 + js) * 64;
  if (w < 7) {
  bar();
  STAMP(7);
  v2u xr[2];
  xr[0] = *(LAS v2u*)(uintptr_t)xr_r; xr[1] = *(LAS v2u*)(uintptr_t)(xr_r + 4 * KB);
  PFR(QPF, PA);
  phi_q();
  STAMP(31);
  PFR(PA, PA2);
  bar();
  STAMP(53);
  if (!OT && w == 0) post(OFF_HINT + (0 * 256 + yg * 8 + mi) * 64);
  make_o();
  bar();
  STAMP(8);
  if (HM == 1) stg(rws, h_off, SENT);
  f4 hold0, hold1;
  const u32 A = L + L_ACT + l16;
  const int tk = zg * 32 + (g & 1) * 16 + i16 + ((js & 1) ? -1 - (js >> 1) : 1 + (js >> 1));
  u32 hs_off = ((((tk & (S_ - 1)) >> 5) * 14 + (n >> 1)) * 2 + ((tk >> 4) & 1)) * KB + cko + (tk & 15) * 16 + OFF_HS;
  u32 hs_keep = (tk < 0 || tk >= S_) ? 0u : 0xffffffffu;
  u32 x1_off = h_off + (OFF_X1 - OFF_H);
  u32 st_r = L + L_ST + i16 * 8;
  PFR(PB, PB + R10);
  PIN(hs_off); PIN(hs_keep); PIN(x1_off); PIN(st_r);
  f4 a0 = {blo(xr[0].x), bhi(xr[0].x), blo(xr[0].y), bhi(xr[0].y)};
  f4 a1 = {blo(xr[1].x), bhi(xr[1].x), blo(xr[1].y), bhi(xr[1].y)};
  if (HM == 1) asm volatile("s_waitcnt vmcnt(%0)" :: "n"(R10) : "memory");
  bar();
  stg(rws, h_off + (OFF_HS - OFF_H), SENT);
  PFR(PB + R10, PC);
  PIN(a0); PIN(a1);
  bar();
  if (w == 0) post_t(hme + 1 * 256 * 64);
  STAMP(32);
  {
    gemm_alds<16>(A, wo_, a0, a1, PFS(PC, NL));
    STAMP(33);
    const v4u p = nosent4(pack8(a0, a1));
    stg(rws, h_off, to_chunk(p));
    asm volatile("s_waitcnt vmcnt(%0)" :: "n"(NL + 1) : "memory");
    SB();
    hold0 = f4{blo(p.x), bhi(p.x), blo(p.y), bhi(p.y)};
    hold1 = f4{blo(p.z), bhi(p.z), blo(p.w), bhi(p.w)};
  }
  bar();
  if (w == 0) post(hme + 7 * 256 * 64);
  STAMP(9);
  f4 xb0, xb1;
  {
    PFR(PC + NL, PC + NL + R1);
    bar();
    stg(rws, x1_off, SENT);
    PFR(PC + NL + R1, PD);
    f4 cs = z4;
#pragma unroll
    for (int s = 0; s < 14; ++s) cs = mfma(w1_[s], ones, cs);
    PIN(cs);
    bar();
    if (w == 0) post_t(hme + 2 * 256 * 64);
    STAMP(34);
    a0 = z4; a1 = z4;
    gemm_alds<14>(A, w1_, a0, a1, PFS(PD, NL));
    STAMP(35);
    for (u32 sp = 0; sp < (1u << 20) && __builtin_amdgcn_readfirstlane(*(volatile LAS u32*)(uintptr_t)(L + L_SF)) == 0; ++sp) { }
    STAMP(36);
    const f2 s0 = ldf2(st_r), s1 = ldf2(st_r + 128), e0 = ldf2(st_r + 256), e1 = ldf2(st_r + 384);
#pragma unroll
    for (int r = 0; r < 4; ++r) {
      a0[r] = gelu(fmaf(a0[r], s0[1], s0[0] * cs[r]));
      a1[r] = gelu(fmaf(a1[r], s1[1], s1[0] * cs[r]));
    }
    stg(rws, hs_off, to_chunk(nosent4(pack8(a0, a1))) & hs_keep);
    asm volatile("s_waitcnt vmcnt(%0)" :: "n"(NL + 1) : "memory");
    bar();
#pragma unroll
    for (int r = 0; r < 4; ++r) { xb0[r] = fmaf(hold0[r], e0[1], e0[0]); xb1[r] = fmaf(hold1[r], e1[1], e1[0]); }
    PIN(xb0); PIN(xb1);
  }
  STAMP(10);
  PFR(PD + NL, PD + NL + R1);
  bar();
  PFR(PD + NL + R1, PE);
  bar();
  if (w == 0) post_t(hme + 3 * 256 * 64);
  STAMP(37);
  {
    gemm_alds<14>(A, w2_, xb0, xb1, PFS(PE, NL));
    STAMP(38);
    const v4u p = nosent4(pack8(xb0, xb1));
    stg(rws, x1_off, to_chunk(p));
    bar();
    hold0 += f4{blo(p.x), bhi(p.x), blo(p.y), bhi(p.y)};
    hold1 += f4{blo(p.z), bhi(p.z), blo(p.w), bhi(p.w)};
    PIN(hold0); PIN(hold1);
  }
  STAMP(11);
  constexpr int W3E = (PE + NL + R1) & ~1;
  PFR(PE + NL, W3E);
  u32 hid_w = L + L_HID + w * 2 * KB + l16, hid_r = L + L_HID + l16;
  u32 rs_w = l16 + OFF_RS + ((yg * 64 + mi) * 7 + w) * KB;
  u32 out_off = (zg * 32 + (g & 1) * 16 + i16) * 896 + (n * 16 + (g >> 1) * 8) * 2;
  u32 xp_s = OFF_X1 + (w < 3 ? (zg ^ 1) * 28 + 16 + w * 4 : zg * 28 + w * 4) * KB, xp_l = L + L_ACT + (w < 3 ? 16 + w * 4 : 28 + w * 4) * KB;
  PINS(xp_s); PINS(xp_l);
  PIN(hid_w); PIN(hid_r); PIN(rs_w); PIN(out_off);
  constexpr int CSE = (PE + NL - 44) / 2 < 14 ? (PE + NL - 44) / 2 : 14;
  f4 cs[2] = {z4, z4};
#pragma unroll
  for (int s = 0; s < CSE; ++s) { cs[0] = mfma(w3f[s], ones, cs[0]); cs[1] = mfma(w3f[14 + s], ones, cs[1]); }
  PIN(cs[0]); PIN(cs[1]);
  bar();
  dma_ws<1>(rws, l16, xp_s, xp_l);
  STAMP(44);
  bar();
  STAMP(12);
  f4 pu[4][2] = {{z4, z4}, {z4, z4}, {z4, z4}, {z4, z4}};
  f4 sv[2];
  float fr0, fr1;
  u32 s5_r = L + L_S5 + i16 * 16;
  PIN(s5_r);
  float gy[2], gt[2];
  v4u gp;
  auto mop = [&](int t, int j) AI {
    const int pr = j / 17, o = j % 17, e = o & 1, c = pr >> 1, r = (pr & 1) * 2 + e;
    const f4& q = sv[t & 1];
    if (j == 68) st16(hid_w + (t >> 1) * 14 * KB + (t & 1) * KB, gp);
    else if (o < 2) gy[e] = fmaf(q[0], cs[c][r], pu[t][c][r]);
    else if (o < 4) gt[e] = gy[e] * gy[e];
    else if (o < 6) gt[e] = fmaf(gt[e], q[2], q[1]);
    else if (o < 8) gt[e] = gt[e] * gy[e];
    else if (o < 10) gt[e] = __builtin_amdgcn_exp2f(gt[e]);
    else if (o < 12) gt[e] = gt[e] + 1.0f;
    else if (o < 14) gt[e] = __builtin_amdgcn_rcpf(gt[e]);
    else if (o < 16) { gy[e] = gy[e] * gt[e]; if (e == 0) PIN(gy[0]); }
    else gp[pr] = pk2(gy[0], gy[1]);
  };
#define MOPS(T0, NOPS, m, NM) sfor<(m) * (NOPS) / (NM), ((m) + 1) * (NOPS) / (NM)>([&](auto J_) AI { mop((T0) + J_.v / 69, J_.v % 69); SB(); })
  constexpr int G0 = W3E, N3 = G0 < 72 ? 72 - G0 : 0, K3 = 4 + N3 * 3 / 2, GB = G0 + N3 + FA;
  v4u hf[14];
  {
    constexpr int P = 6;
    v4u bq[P];
    auto fa = [&](int k) AI { return A + (k < 12 ? k : k < 24 ? 28 + k - 12 : k < 32 ? 2 * (k - 18) : k < 40 ? 2 * (k - 26) + 1 : k) * KB; };
#pragma unroll
    for (int k = 0; k < P; ++k) bq[k] = ld16(fa(k));
    SB();
    sfor<0, 56>([&](auto K) AI {
      constexpr int k = K.v;
      constexpr int t = k < 12 ? k & 1 : k < 24 ? 2 + (k & 1) : k < 32 ? 0 : k < 40 ? 1 : 2 + (k & 1);
      constexpr int s = k < 12 ? k >> 1 : k < 24 ? (k - 12) >> 1 : k < 32 ? k - 18 : k < 40 ? k - 26 : (k - 28) >> 1;
      sfor<0, 2>([&](auto C) AI {
        constexpr int c = C.v;
        pu[t][c] = mfma(w3f[c * 14 + s], bq[k % P], pu[t][c]);
        SB();
        if constexpr (k >= 33 && k < 50) MOPS(0, 138, (k - 33) * 2 + c, 34);
      });
      if constexpr (t == 0 && s >= CSE) { cs[0] = mfma(w3f[s], ones, cs[0]); cs[1] = mfma(w3f[14 + s], ones, cs[1]); SB(); }
      if constexpr (k == 6 || k == 18) asm volatile("s_barrier" ::: "memory");
      if constexpr (k == 14) {
        asm volatile("s_waitcnt vmcnt(%0)" :: "n"(N3 ? (K3 < 14 ? N3 + (14 - K3) * FA / (56 - K3) : 10 * 2 / 3) : 10 * FA / 52) : "memory");
#pragma unroll
        for (int e = 0; e < 4; ++e) hf[e] = ld16(xp_l + l16 + e * KB);
      }
      if constexpr (k == 17) {
        if (!__all(max4(max4(max4(max4(0, hf[0]), hf[1]), hf[2]), hf[3]) != 0xffffffffu)) {
          RETRY_LOOP {
            RETRY_END(7);
            dma_ws<1>(rws, l16, xp_s, xp_l);
            VMW(0);
            if (__all(max4(max4(max4(max4(0, ld16(xp_l + l16)), ld16(xp_l + l16 + KB)), ld16(xp_l + l16 + 2 * KB)), ld16(xp_l + l16 + 3 * KB)) != 0xffffffffu)) break;
          }
        }
      }
      if constexpr (k == 24) STAMP(48);
      if constexpr (k == 31) { asm volatile("s_barrier" ::: "memory"); STAMP(49); sv[0] = __builtin_bit_cast(f4, ld16(s5_r)); sv[1] = __builtin_bit_cast(f4, ld16(s5_r + 256)); }
      if constexpr (k == 50) { STAMP(50); asm volatile("s_waitcnt lgkmcnt(0)\n\ts_barrier" ::: "memory"); }
      if constexpr (k + P < 56) bq[k % P] = ld16(fa(k + P));
      if constexpr (k >= 50) sfor<(k - 50) * 14 / 6, (k - 49) * 14 / 6>([&](auto E) AI { hf[E.v] = ld16(hid_r + E.v * KB); });
      if constexpr (k >= 4 && k < K3) sfor<(k - 4) * 2 / 3, (k - 3) * 2 / 3>([&](auto E) AI { pf(G0 + E.v); });
      if constexpr (k >= K3) sfor<(k - K3) * FA / (56 - K3), (k + 1 - K3) * FA / (56 - K3)>([&](auto E) AI { pf(G0 + N3 + E.v); });
      SB();
    });
  }
  STAMP(39);
  f4 own0, own1;
  {
    v4u pend[2];
    auto rs_off = [&](int rd) AI { return (rs_w | NORS) + ((((rd >> 2) ? th : th ^ 1) * 4 + ((js + 1 + rd) & 3)) * 56) * KB; };
    sv[0] = __builtin_bit_cast(f4, ld16(s5_r + 512)); sv[1] = __builtin_bit_cast(f4, ld16(s5_r + 768));
    fr0 = sv[0][3]; fr1 = sv[1][3];
    sfor<0, 8>([&](auto RD) AI {
      constexpr int rd = RD.v, sl = rd & 3;
      f4 c0 = z4, c1 = z4;
      sfor<0, 7>([&](auto C) AI {
        constexpr int c = C.v;
        c1 = mfma(w4f[sl * 7 + c], hf[2 * c + 1], c1);
        SB();
        if constexpr (rd < 3 && rd + c > 0) MOPS(2, 138, (rd * 7 + c) * 2 - 2, 40);
        c0 = mfma(w4f[sl * 7 + c], hf[2 * c], c0);
        SB();
        if constexpr (rd < 3 && rd + c > 0) MOPS(2, 138, (rd * 7 + c) * 2 - 1, 40);
        if constexpr (rd == 3 && c == 0) { STAMP(51); asm volatile("s_waitcnt lgkmcnt(0)\n\ts_barrier" ::: "memory"); }
        if constexpr (rd == 4 && c == 0) STAMP(52);
        if constexpr (rd == 3) { hf[2 * c] = ld16(hid_r + (14 + 2 * c) * KB); hf[2 * c + 1] = ld16(hid_r + (15 + 2 * c) * KB); }
        if constexpr (rd < 2) sfor<(rd * 7 + c) * (100 - GB) / 14, (rd * 7 + c + 1) * (100 - GB) / 14>([&](auto E) AI { pf(GB + E.v); });
        if constexpr (c == 1 && rd == 1) pend[0] = pack8(own0, own1);
        if constexpr (c == 1 && rd >= 2) stg(rws, rs_off(rd - 1), pack8(own0, own1));
        if constexpr (c == 4 && rd == 2) stg(rws, rs_off(0), pend[0]);
        if constexpr (rd == RF && c == 0) asm volatile("s_waitcnt vmcnt(%0)\n\ts_barrier" :: "n"(RF - 5) : "memory");
        SB();
      });
      own0 = c0; own1 = c1;
    });
    PIN(own0); PIN(own1);
  }
  static_assert(GB >= 86, "");
  STAMP(42);
  VMW(0);
  bar();
  STAMP(16);
  const u32 one = g != q4 ? 0u : (p4 & 1) ? 0x3f800000u : 0x00003f80u;
  v4u sl0 = {p4 < 2 ? one : 0u, p4 < 2 ? 0u : one, 0u, 0u}, sl1 = {0u, 0u, sl0.x, sl0.y};
  PIN(sl0); PIN(sl1);
  u32 rs_r[3];
#pragma unroll
  for (int k = 0; k < 3; ++k) { rs_r[k] = OFF_RS + (((yg * 8 + mi) * 8 + th * 4 + ((js + 1 + k) & 3)) * 7 + w) * KB; PINS(rs_r[k]); }
  bar();
  STAMP(43);
  {
    v4u pp[7];
#pragma unroll
    for (int k = 0; k < 3; ++k) pp[k] = ldg(rws, l16, rs_r[k]);
#pragma unroll
    for (int k = 0; k < 4; ++k) pp[3 + k] = ld16(A + (k * 7 + w) * KB);
    SB();
#pragma unroll
    for (int k = 6; k >= 3; --k) { own0 = mfma(sl0, pp[k], own0); own1 = mfma(sl1, pp[k], own1); }
    SB();
#pragma unroll
    for (int k = 0; k < 3; ++k) { own0 = mfma(sl0, pp[k], own0); own1 = mfma(sl1, pp[k], own1); }
#pragma unroll
    for (int r = 0; r < 4; ++r) { hold0[r] = fmaf(own0[r], fr0, hold0[r]); hold1[r] = fmaf(own1[r], fr1, hold1[r]); }
    *(GAS v4u*)(p_out + out_off) = to_chunk(pack8(hold0, hold1));
  }
  if (w == 2) __builtin_amdgcn_raw_buffer_store_b128(v4u{(u32)pr0, (u32)(pr0 >> 32), (u32)pr1, (u32)(pr1 >> 32)}, mkrs(p_ws + (200u << 20), 4096), (int)(vz + b * 16), 0, 0);
  return;
  }
  {
    {
      const u32 one = (i16 & 1) ? 0x3f800000u : 0x00003f80u;
      v4u sel = {(i16 >> 1) == 0 ? one : 0u, (i16 >> 1) == 1 ? one : 0u, (i16 >> 1) == 2 ? one : 0u, (i16 >> 1) == 3 ? one : 0u};
      PIN(sel);
      f4 acc;
      asm volatile("s_waitcnt vmcnt(%0)" :: "n"(OM == 0 ? 8 + QPF : 0) : "memory");
      RETRY_LOOP {
        v4u kp[8];
#pragma unroll
        for (int k = 0; k < 8; ++k) kp[k] = ld16(L + L_KP + k * KB + l16);
        SB();
        acc = z4;
        u32 m = 0;
#pragma unroll
        for (int k = 0; k < 8; ++k) { acc = mfma(sel, kp[k], acc); m = max4(m, kp[k]); }
        if (__all(m != 0xffffffffu)) break;
        RETRY_END(1);
        dma_ws<2>(rws, l16, OFF_KVP + (mi * 32 + yg) * 8 * KB, L + L_KP);
        VMW(0);
      }
      __builtin_amdgcn_raw_buffer_store_b64(v2u{nosent(pk2(acc[0], acc[1])), nosent(pk2(acc[2], acc[3]))}, rws, (int)kvt8, 0, SC1);
      RSTAMP(57);
      if (OT) { VMW(1); post_t(OFF_HINT + (0 * 256 + yg * 8 + mi) * 64); }
      bar();
      __builtin_amdgcn_s_sleep(DLYB);
      wait_until(t_kv + DTF);
      dma_ws<2>(rws, l16, OFF_KVT + mi * 8 * KB, L + L_KF);
      if (HM == 0) {
        const u32 hm = zg * 28 * KB + (g & 1) * KB + (g >> 1) * 256 + i16 * 16 + OFF_H;
#pragma unroll
        for (int c = 0; c < 7; ++c) stg(rws, hm + ((js * 7 + c) >> 1) * 2 * KB + ((js * 7 + c) & 1) * 512, SENT);
      }
      RSTAMP(58);
      phi_q();
      STAMP(31);
      f4 sm[4];
      if (HM == 0) VMW(7); else VMW(0);
      RETRY_LOOP {
        if (g_check<0, 2, -1, false>(L + L_KF + l16, sm)) break;
        RETRY_END(2);
        dma_ws<2>(rws, l16, OFF_KVT + mi * 8 * KB, L + L_KF);
        VMW(0);
      }
      bar();
      RSTAMP(59);
      if (OT) peek_issue(rws, h8);
      make_o();
      bar();
      STAMP(8);
    }
    const u32 P = L + L_ACT + l16;
    f4 sm[4];
    u32 hN = OFF_HINT + (7 * 256 + (((js & 1) ? zg + 63 : zg + 1) & 63) * 4 + js) * 64;
#define GATHER(NK, ST, SRC, ID) \
      RETRY_LOOP { \
        dma_ws<NK / 4>(rws, l16, SRC, L + L_ACT); \
        if (ST) peek_issue(rws, hN); \
        if (sp == 0) bar(); \
        if (ST) { sm[0] = z4; sm[1] = z4; sm[2] = z4; sm[3] = z4; } \
        if (g_check<0, 3, NK - 12 + ST, ST>(P, sm)) break; \
        RETRY_END(ID); \
        VMW(0); \
      } \
      bar(); \
      RETRY_LOOP { \
        f4 sn[4] = {sm[0], sm[1], sm[2], sm[3]}; \
        const bool ok = g_check<3, NK / 4, ST, ST>(P, sn); \
        if (ok) { sm[0] = sn[0]; sm[1] = sn[1]; sm[2] = sn[2]; sm[3] = sn[3]; break; } \
        RETRY_END(ID); \
        dma_ws<NK / 4 - 3>(rws, l16, (SRC) + 12 * KB, L + L_ACT + 12 * KB); \
        VMW(0); \
      } \
      bar()
    {
      if (OT) pace<1, DT0>(rws, h8, tag, mi); else poll(rws, h8, tag);
    }
    STAMP(9);
    { GATHER(32, false, OFF_O + zg * 32 * KB, 3); }
    STAMP(10);
    u32 hA = OFF_HINT + (1 * 256 + zg * 4 + (lane & 3)) * 64;
    u32 hB = OFF_HINT + (2 * 256 + ((lane & 4) ? ((lane & 1) ? zg + 1 : zg + 63) & 63 : zg) * 4 + (lane & 3)) * 64;
    u32 st_w = L + (g == q4 ? L_ST : L_QB) + i16 * 8;
    PIN(hA); PIN(hB); PIN(st_w);
    *(LAS u32*)(uintptr_t)(L + L_SF) = 0;
    PIN(hN);
    peek_issue(rws, hA);
    STAMP(32);
    bar();
    pace<0, DT1>(rws, hA, tag, js);
    STAMP(11);
    {
      GATHER(28, true, OFF_H + zg * 28 * KB, 4);
        STAMP(12);
      const float m0 = sm[0][0] * (1.0f / D_), m1 = sm[1][0] * (1.0f / D_);
      const float v0 = fmx(fmaf(-m0, m0, diag(sm[2], p4) * (1.0f / D_)), 0.f), v1 = fmx(fmaf(-m1, m1, diag(sm[3], p4) * (1.0f / D_)), 0.f);
      const float r0 = __builtin_amdgcn_rsqf(v0 + 1e-5f), r1 = __builtin_amdgcn_rsqf(v1 + 1e-5f);
      const float t0 = r0 * __builtin_amdgcn_rsqf(v0 * r0 * r0 + 1e-5f), t1 = r1 * __builtin_amdgcn_rsqf(v1 * r1 * r1 + 1e-5f);
      { st8(st_w, f2u(-m0 * t0), f2u(t0)); st8(st_w + 128, f2u(-m1 * t1), f2u(t1)); st8(st_w + 256, f2u(-m0 * r0), f2u(r0)); st8(st_w + 384, f2u(-m1 * r1), f2u(r1)); }
      peek_take<0>(rws, hN, tag);
      asm volatile("s_waitcnt lgkmcnt(0)" ::: "memory");
      *(volatile LAS u32*)(uintptr_t)(L + L_SF) = 1;
    }
    STAMP(33);
    peek_issue(rws, hB);
    bar();
    pace<0, DT2>(rws, hB, tag, js);
    STAMP(13);
    { GATHER(28, false, OFF_HS + zg * 28 * KB, 5); }
    STAMP(14);
    u32 h83 = h8 + 3 * 256 * 64;
    PIN(h83);
    peek_issue(rws, h83);
    bar();
    pace<0, DT1>(rws, h83, tag, mi);
    STAMP(15);
    {
      const u32 s5_w = L + (g == q4 ? L_S5 : L_QB + 4 * KB) + i16 * 16;
      auto stats = [&](u32 dst) AI {
        const float m0 = sm[0][0] * (1.0f / D_), m1 = sm[1][0] * (1.0f / D_);
        const float r0 = __builtin_amdgcn_rsqf(fmx(fmaf(-m0, m0, diag(sm[2], p4) * (1.0f / D_)), 0.f) + 1e-5f);
        const float r1 = __builtin_amdgcn_rsqf(fmx(fmaf(-m1, m1, diag(sm[3], p4) * (1.0f / D_)), 0.f) + 1e-5f);
        {
          st16(dst, v4u{f2u(-m0), f2u(GC0 * r0), f2u(GC1 * r0 * r0 * r0), f2u(r0)});
          st16(dst + 256, v4u{f2u(-m1), f2u(GC0 * r1), f2u(GC1 * r1 * r1 * r1), f2u(r1)});
        }
      };
      const u32 xa_ = OFF_X1 + (zg ^ 1) * 28 * KB, xb_ = OFF_X1 + zg * 28 * KB;
      RETRY_LOOP {
        dma_ws<3>(rws, l16, xa_, L + L_ACT);
        dma_ws<3>(rws, l16, xb_, L + L_ACT + 28 * KB);
        dma_ws<1>(rws, l16, xa_ + 12 * KB, L + L_ACT + 12 * KB);
        if (sp == 0) bar();
        if (g_check<0, 3, 16, false>(P, sm)) break;
        RETRY_END(6);
        VMW(0);
      }
      bar();
      RETRY_LOOP {
        if (g_check<7, 10, 4, false>(P, sm)) break;
        RETRY_END(6);
        dma_ws<3>(rws, l16, xb_, L + L_ACT + 28 * KB);
        VMW(0);
      }
      bar();
      STAMP(16);
      sm[0] = z4; sm[1] = z4; sm[2] = z4; sm[3] = z4;
      RETRY_LOOP {
        VMW(0);
        if (g_check<3, 4, -1, false>(P, sm)) break;
        RETRY_END(7);
        dma_ws<1>(rws, l16, xa_ + 12 * KB, L + L_ACT + 12 * KB);
      }
      g_check<0, 4, -1, true>(P, sm);
      STAMP(35);
      bar();
      g_check<4, 7, -1, true>(P, sm);
      stats(s5_w);
      STAMP(36);
      bar();
      sm[0] = z4; sm[1] = z4; sm[2] = z4; sm[3] = z4;
      g_check<7, 14, -1, true>(P, sm);
      stats(s5_w + 512);
    }
    STAMP(37);
    bar();
    bar();
    bar();
    post(hme + 6 * 256 * 64);
    u32 hO = OFF_HINT + (6 * 256 + (zg ^ 1) * 4 + (lane & 3)) * 64, hS = OFF_HINT + (4 * 256 + zg * 4 + (lane & 3)) * 64;
    PIN(hO); PIN(hS);
    __builtin_amdgcn_s_sleep(RF == 6 ? 8 : 4);
    peek_issue(rws, hO);
    bar();
    post(hme + 4 * 256 * 64);
    peek_take<1>(rws, hO, tag);
    STAMP(54);
    dma_ws<7>(rws, l16, OFF_RS + ((yg * 8 + mi) * 8 + (th ^ 1) * 4) * 7 * KB, L + L_ACT);
    poll(rws, hS, tag);
    STAMP(17);
    bar();
    if (b == 0) __builtin_amdgcn_raw_buffer_store_b32(epoch + 1, rws, (int)vz, (int)OFF_E, SC1);
    STAMP_END();
  }
}

__global__ void k_probe(u64* q, int mode) {
  u64 lo = q[threadIdx.x * 2], hi = q[threadIdx.x * 2 + 1];
  __shared__ u64 a[256], c[256];
  a[threadIdx.x] = lo; c[threadIdx.x] = hi; __syncthreads();
  u64 sum = 0;
  for (int i = 0; i < 256; ++i) { lo = a[i] < lo ? a[i] : lo; }
  for (int i = 0; i < 256; ++i) { hi = c[i] > hi ? c[i] : hi; sum += c[i] - lo; }
  u64 ticks = (mode == 0 ? hi - lo : sum / 256) * 10;
  if (ticks > 100000) ticks = 100000;
  u64 r1 = __builtin_amdgcn_s_memrealtime();
  while (__builtin_amdgcn_s_memrealtime() - r1 < ticks) { }
}
extern "C" void kernel_launch(void* const* d_in, const int* in_sizes, int n_in, void* d_out, int out_size, void* d_ws, size_t ws_size,
                              hipStream_t stream) {
  fused_kernel<<<256, 512, 0, stream>>>((const u16*)d_in[0], (const u16*)d_in[1], (const u16*)d_in[2], (const u16*)d_in[3],
                                        (const u16*)d_in[5], (const u16*)d_in[6], (const u16*)d_in[7], (const u16*)d_in[8],
                                        (u16*)d_out, (unsigned char*)d_ws);
  k_probe<<<1, 256, 0, stream>>>((u64*)((char*)d_ws + (200u << 20)), 1);
}
```

```cpp
#include <hip/hip_runtime.h>
#include <stdint.h>

typedef unsigned int u32;
typedef unsigned long long u64;
typedef unsigned short u16;
typedef u32 v4u __attribute__((ext_vector_type(4)));
typedef u32 v2u __attribute__((ext_vector_type(2)));
typedef float f4 __attribute__((ext_vector_type(4)));
typedef float f2 __attribute__((ext_vector_type(2)));
typedef __bf16 bf8 __attribute__((ext_vector_type(8)));
typedef __bf16 bf2 __attribute__((ext_vector_type(2)));
typedef short s4v __attribute__((ext_vector_type(4)));
typedef __amdgpu_buffer_rsrc_t rsrc_t;
#define GAS __attribute__((address_space(1)))
#define LAS __attribute__((address_space(3)))
#define DI __device__ __forceinline__
#define AI __attribute__((always_inline))
#define RLX_AGENT __ATOMIC_RELAXED, __HIP_MEMORY_SCOPE_AGENT

constexpr int S_ = 2048, D_ = 448, F_ = 1792;
constexpr u32 KB = 1024;
constexpr u32 OFF_E = 0;
constexpr u32 OFF_HINT = 16 * KB;
constexpr u32 OFF_KVT = 192 * KB;
constexpr u32 OFF_WO = 256 * KB;
constexpr u32 OFF_W1 = OFF_WO + 448 * KB;
constexpr u32 OFF_W2 = OFF_W1 + 392 * KB;
constexpr u32 OFF_W3 = OFF_W2 + 392 * KB;
constexpr u32 OFF_W4 = OFF_W3 + 1568 * KB;
constexpr u32 OFF_KVP = OFF_W4 + 1568 * KB;
constexpr u32 OFF_O = OFF_KVP + 2048 * KB;
constexpr u32 OFF_H = OFF_O + 2048 * KB;
constexpr u32 OFF_HS = OFF_H + 1792 * KB;
constexpr u32 OFF_X1 = OFF_HS + 1792 * KB;
constexpr u32 OFF_RS = OFF_X1 + 1792 * KB;
constexpr u32 WS_BYTES = OFF_RS + 256 * 56 * KB;
constexpr u32 L_X = 0, X_RS = 928, SLOT = 16 * X_RS;
constexpr u32 L_VB = 0, L_QA = 28 * KB, L_QX = 0;
constexpr u32 L_K = 64 * X_RS;
constexpr u32 L_PK = L_K, PK_RS = 136;
constexpr u32 L_V0 = L_PK + 128 * PK_RS, V_RS = 72;
constexpr u32 L_QB = L_K + 28 * KB;
constexpr u32 L_VA = L_K + 56 * KB;
constexpr u32 L_U = L_VA;
constexpr u32 L_KX = L_VA + 28 * KB;
constexpr u32 L_V1 = L_KX;
constexpr u32 L_ST = L_KX + 9 * KB;
constexpr u32 L_SF = L_ST + 512, L_DUMP = L_ST + 768;
constexpr u32 L_PHI = L_ST + 1024;
constexpr u32 L_U0 = L_PHI + 4096;
constexpr u32 L_BYTES = L_U0 + 4096;
constexpr u32 L_KP = 8 * KB;
constexpr u32 L_KF = 16 * KB;
static_assert(L_BYTES <= 163840 && L_V0 + 128 * V_RS <= L_QB, "");
constexpr u32 L_ACT = 0;
constexpr u32 L_HID = 56 * KB;
constexpr u32 L_S5 = 84 * KB;
constexpr u32 SPIN_CAP = 1u << 15;
constexpr u32 OOB = 0x80000000u;
constexpr int SC1 = 16;
#define CODEB 36864
#define XDMA dma_in
#define DT0 22
#define DT1 14
#define DT2 20
#define R1 3
#define PA 11
#define QPF 5
#define R10 R1
#define R20 4
#define PA2 15
#define DTX 22
#define PB PA2
#define R2 3
#define NL 7
#define FA 16
#define NOW4 0
#define NORS 0
#define RF 7
#define HM 1
#define KVC 2200
#define DTF 94
#define XP 0
#define W4R 0
#define DT3 16
#define DEF 2
#define SLA 4
#define SLB 6
#define NSP 14
#define YP 0
#define HP 0
#define DLYB 2
constexpr u32 CODE_BYTES = CODEB;
#define PC (PB + R10 + R20)
#define PD (PC + NL + R1 + R2)
#define PE (PD + NL + R1 + R2)
static_assert(PC >= 16 && PC + NL + R1 >= 30 && PE >= 44 && PC + 2 <= 30, "");

#define SB() __builtin_amdgcn_sched_barrier(0)
#define PIN(x) asm volatile("" : "+v"(x))
#define PINS(x) do { x = __builtin_amdgcn_readfirstlane(x); asm volatile("" : "+s"(x)); } while (0)

template <int V> struct IC { static constexpr int v = V; };
template <int I, int N, class F> DI void sfor(F f) { if constexpr (I < N) { f(IC<I>{}); sfor<I + 1, N>(f); } }
DI f4 mfma(v4u a, v4u b, f4 c) {
  return __builtin_amdgcn_mfma_f32_16x16x32_bf16(__builtin_bit_cast(bf8, a), __builtin_bit_cast(bf8, b), c, 0, 0, 0);
}
DI u32 pk2(float a, float b) { f2 v = {a, b}; return __builtin_bit_cast(u32, __builtin_convertvector(v, bf2)); }
DI float blo(u32 u) { return __builtin_bit_cast(float, u << 16); }
DI float bhi(u32 u) { return __builtin_bit_cast(float, u & 0xffff0000u); }
DI u32 f2u(float x) { return __builtin_bit_cast(u32, x); }
DI float u2f(u32 x) { return __builtin_bit_cast(float, x); }
DI u32 nosent(u32 x) { return x < 0xfffffffeu ? x : 0xfffffffeu; }
DI v4u nosent4(v4u v) { return v4u{nosent(v.x), nosent(v.y), nosent(v.z), nosent(v.w)}; }
DI u32 umax(u32 a, u32 b) { return a > b ? a : b; }
DI u32 max4(u32 m, v4u v) { m = umax(umax(m, v.x), v.y); return umax(umax(m, v.z), v.w); }
DI float fmx(float a, float b) { return __builtin_elementwise_maximum(a, b); }
DI float fmn(float a, float b) { return __builtin_elementwise_minimum(a, b); }
DI float rsum(float x) {
  auto a = __builtin_amdgcn_permlane16_swap(f2u(x), f2u(x), false, false); x = u2f(a[0]) + u2f(a[1]);
  auto c = __builtin_amdgcn_permlane32_swap(f2u(x), f2u(x), false, false); return u2f(c[0]) + u2f(c[1]);
}
DI void bar() { SB(); asm volatile("s_waitcnt lgkmcnt(0)\n\ts_barrier" ::: "memory"); SB(); }
#define VMW(n) asm volatile("s_waitcnt vmcnt(" #n ")" ::: "memory")
DI rsrc_t mkrs(u64 p, u32 bytes) { return __builtin_amdgcn_make_buffer_rsrc((GAS void*)p, 0, (int)bytes, 0x00020000); }
DI v4u ldg(rsrc_t rs, u32 voff, u32 soff) { return __builtin_amdgcn_raw_buffer_load_b128(rs, (int)voff, (int)soff, SC1); }
DI void stg(rsrc_t rs, u32 off, v4u v) { __builtin_amdgcn_raw_buffer_store_b128(v, rs, (int)off, 0, SC1); }
template <int N, int DL>
DI void dma_in(rsrc_t rs, u32 voff, u32 soff, u32 ds, u32 lds) {
  asm volatile("s_mov_b32 m0, %4\n\ts_nop 0\n\tbuffer_load_dwordx4 %1, %2, %0 offen lds\n\t"
               ".rept %c5\n\ts_add_u32 m0, m0, %c6\n\ts_add_u32 %0, %0, %3\n\tbuffer_load_dwordx4 %1, %2, %0 offen lds\n\t.endr"
               : "+s"(soff) : "v"(voff), "s"(rs), "s"(ds), "s"(lds), "n"(N - 1), "n"(DL) : "memory", "scc");
}
template <int N, int DL>
DI void dma_nt(rsrc_t rs, u32 voff, u32 soff, u32 ds, u32 lds) {
  asm volatile("s_mov_b32 m0, %4\n\ts_nop 0\n\tbuffer_load_dwordx4 %1, %2, %0 offen nt lds\n\t"
               ".rept %c5\n\ts_add_u32 m0, m0, %c6\n\ts_add_u32 %0, %0, %3\n\tbuffer_load_dwordx4 %1, %2, %0 offen nt lds\n\t.endr"
               : "+s"(soff) : "v"(voff), "s"(rs), "s"(ds), "s"(lds), "n"(N - 1), "n"(DL) : "memory", "scc");
}
DI void touch(rsrc_t rs, u32 voff, u32 lds) {
  asm volatile("s_mov_b32 m0, %2\n\ts_nop 0\n\tbuffer_load_dword %0, %1, 0 offen sc1 lds" :: "v"(voff), "s"(rs), "s"(lds) : "memory");
}
template <int N>
DI void dma_ws(rsrc_t rs, u32 voff, u32 soff, u32 lds) {
#define L4_ "buffer_load_dwordx4 %1, %2, %0 offen sc1 lds\n\tbuffer_load_dwordx4 %1, %2, %0 offen offset:1024 sc1 lds\n\t" \
            "buffer_load_dwordx4 %1, %2, %0 offen offset:2048 sc1 lds\n\tbuffer_load_dwordx4 %1, %2, %0 offen offset:3072 sc1 lds\n\t"
  asm volatile("s_mov_b32 m0, %3\n\ts_nop 0\n\t" L4_
               ".rept %c4\n\ts_add_u32 m0, m0, 0x1000\n\ts_add_u32 %0, %0, 0x1000\n\t" L4_ ".endr"
               : "+s"(soff) : "v"(voff), "s"(rs), "s"(lds), "n"(N - 1) : "memory", "scc");
}
DI void poll(rsrc_t rs, u32 voff, u64 want) {
  u64 t; u32 cnt = SPIN_CAP;
  asm volatile("1:\n\tbuffer_load_dwordx2 %0, %2, %3, 0 offen sc1\n\ts_waitcnt vmcnt(0)\n\tv_cmp_ne_u64 vcc, %4, %0\n\ts_cbranch_vccz 2f\n\t"
               "s_sleep 1\n\ts_sub_u32 %1, %1, 1\n\ts_cbranch_scc0 1b\n2:"
               : "=&v"(t), "+s"(cnt) : "v"(voff), "s"(rs), "s"(want) : "vcc", "scc", "memory");
}
DI u32 now() { u64 t; asm volatile("s_memrealtime %0\n\ts_waitcnt lgkmcnt(0)" : "=s"(t) :: "memory"); return (u32)t; }
DI void peek_issue(rsrc_t rs, u32 voff) {
  asm volatile("buffer_load_dwordx4 v[248:251], %0, %1, 0 offen sc1" :: "v"(voff), "s"(rs) : "v248", "v249", "v250", "v251", "memory");
}
template <int N>
DI u32 peek_take(rsrc_t rs, u32 voff, u64 want) {
  v4u t; u32 cnt = SPIN_CAP;
  asm volatile("s_waitcnt vmcnt(%5)\n1:\n\tv_cmp_ne_u64 vcc, %4, v[248:249]\n\ts_cbranch_vccz 2f\n\t"
               "buffer_load_dwordx4 v[248:251], %2, %3, 0 offen sc1\n\ts_waitcnt vmcnt(0)\n\ts_sub_u32 %1, %1, 1\n\ts_cbranch_scc0 1b\n2:"
               : "=&{v[248:251]}"(t), "+s"(cnt) : "v"(voff), "s"(rs), "s"(want), "n"(N) : "vcc", "scc", "memory");
  return t.z;
}
template <int N, int DT>
DI void pace(rsrc_t rs, u32 voff, u64 want, u32 own) {
  u32 cnt = SPIN_CAP, c2 = 256, a;
  asm volatile("s_memrealtime s[90:91]\n\ts_waitcnt vmcnt(%7)\n1:\n\tv_cmp_ne_u64 vcc, %5, v[248:249]\n\ts_cbranch_vccz 2f\n\t"
               "buffer_load_dwordx4 v[248:251], %3, %4, 0 offen sc1\n\ts_waitcnt vmcnt(0)\n\ts_sub_u32 %0, %0, 1\n\ts_cbranch_scc0 1b\n2:\n\t"
               "v_readlane_b32 %2, v250, %6\n\ts_waitcnt lgkmcnt(0)\n\ts_sub_u32 %2, s90, %2\n\ts_add_u32 %2, %2, %8\n\tv_add_u32 v250, %2, v250\n"
               "3:\n\ts_memrealtime s[90:91]\n\ts_waitcnt lgkmcnt(0)\n\tv_sub_u32 v251, s90, v250\n\tv_cmp_gt_i32 vcc, 0, v251\n\ts_cbranch_vccz 4f\n\t"
               "s_sub_u32 %1, %1, 1\n\ts_cbranch_scc0 3b\n4:"
               : "+s"(cnt), "+s"(c2), "=&s"(a) : "v"(voff), "s"(rs), "s"(want), "s"(own), "n"(N), "n"(DT)
               : "v248", "v249", "v250", "v251", "s90", "s91", "vcc", "scc", "memory");
}
DI void wait_until(u32 when) {
  u64 t; u32 d, cnt = 256;
  asm volatile("1:\n\ts_memrealtime %0\n\ts_waitcnt lgkmcnt(0)\n\tv_sub_u32 %1, s90, %3\n\tv_cmp_gt_i32 vcc, 0, %1\n\ts_cbranch_vccz 2f\n\t"
               "s_sub_u32 %2, %2, 1\n\ts_cbranch_scc0 1b\n2:"
               : "=&{s[90:91]}"(t), "=&v"(d), "+s"(cnt) : "v"(when) : "vcc", "scc", "memory");
}
constexpr float GC0 = -2.0f * 0.7978845608028654f * 1.4426950408889634f, GC1 = GC0 * 0.044715f;
DI float gelu(float x) {
  const float e = __builtin_amdgcn_exp2f(x * fmaf(x * x, GC1, GC0));
  return x * __builtin_amdgcn_rcpf(1.0f + e);
}

DI void hedgehog(f4 p0, f4 p1, float keep, v4u& pos, v4u& neg) {
  const float L2E = 1.4426950408889634f;
  float mx = fmx(fmx(fmx(fmx(p0[0], p0[1]), p0[2]), fmx(fmx(p0[3], p1[0]), p1[1])), fmx(p1[2], p1[3]));
  float nm = -fmn(fmn(fmn(fmn(p0[0], p0[1]), p0[2]), fmn(fmn(p0[3], p1[0]), p1[1])), fmn(p1[2], p1[3]));
  {
    auto a = __builtin_amdgcn_permlane16_swap(f2u(mx), f2u(nm), false, false);
    float t = fmx(u2f(a[0]), u2f(a[1]));
    auto c = __builtin_amdgcn_permlane32_swap(f2u(t), f2u(t), false, false);
    t = fmx(u2f(c[0]), u2f(c[1])) * -L2E;
    auto d = __builtin_amdgcn_permlane16_swap(f2u(t), f2u(t), false, false);
    mx = u2f(d[0]); nm = u2f(d[1]);
  }
  float a[8], c[8];
#pragma unroll
  for (int r = 0; r < 4; ++r) {
    a[r] = __builtin_amdgcn_exp2f(fmaf(p0[r], L2E, mx));
    c[r] = __builtin_amdgcn_exp2f(fmaf(p0[r], -L2E, nm));
    a[4 + r] = __builtin_amdgcn_exp2f(fmaf(p1[r], L2E, mx));
    c[4 + r] = __builtin_amdgcn_exp2f(fmaf(p1[r], -L2E, nm));
  }
  float sa = fmaf((a[4] + a[5]) + (a[6] + a[7]), keep, (a[0] + a[1]) + (a[2] + a[3]));
  float sc = fmaf((c[4] + c[5]) + (c[6] + c[7]), keep, (c[0] + c[1]) + (c[2] + c[3]));
  {
    auto x = __builtin_amdgcn_permlane16_swap(f2u(sa), f2u(sc), false, false);
    float t = u2f(x[0]) + u2f(x[1]);
    auto y = __builtin_amdgcn_permlane32_swap(f2u(t), f2u(t), false, false);
    t = __builtin_amdgcn_rcpf(u2f(y[0]) + u2f(y[1]));
    auto d = __builtin_amdgcn_permlane16_swap(f2u(t), f2u(t), false, false);
    sa = u2f(d[0]); sc = u2f(d[1]);
  }
  const float sa1 = sa * keep, sc1 = sc * keep;
  pos = v4u{pk2(a[0] * sa, a[1] * sa), pk2(a[2] * sa, a[3] * sa), pk2(a[4] * sa1, a[5] * sa1), pk2(a[6] * sa1, a[7] * sa1)};
  neg = v4u{pk2(c[0] * sc, c[1] * sc), pk2(c[2] * sc, c[3] * sc), pk2(c[4] * sc1, c[5] * sc1), pk2(c[6] * sc1, c[7] * sc1)};
}
DI v4u pack8(f4 a, f4 b) { return v4u{pk2(a[0], a[1]), pk2(a[2], a[3]), pk2(b[0], b[1]), pk2(b[2], b[3])}; }
DI v4u to_chunk(v4u p) {
  auto x = __builtin_amdgcn_permlane16_swap(p.x, p.z, false, false);
  auto y = __builtin_amdgcn_permlane16_swap(p.y, p.w, false, false);
  return v4u{x[0], y[0], x[1], y[1]};
}
DI float diag(f4 q, int p4) {
  const float a = q[0], b = q[1], c = q[2], d = q[3];
  const float t0 = (p4 & 1) ? b : a, t1 = (p4 & 1) ? d : c;
  return (p4 & 2) ? t1 : t0;
}
DI v4u ld16(u32 addr) { return *(LAS v4u*)(uintptr_t)addr; }
DI void st16(u32 addr, v4u v) { *(LAS v4u*)(uintptr_t)addr = v; }
DI void st8(u32 addr, u32 a, u32 b) { *(LAS v2u*)(uintptr_t)addr = v2u{a, b}; }
DI f2 ldf2(u32 addr) { return *(LAS f2*)(uintptr_t)addr; }
DI v2u trread(u32 addr) {
  return __builtin_bit_cast(v2u, __builtin_amdgcn_ds_read_tr16_b64_v4i16((LAS s4v*)(uintptr_t)addr));
}
DI float dot2(u32 a, u32 sel, float c) {
  return __builtin_amdgcn_fdot2_f32_bf16(__builtin_bit_cast(bf2, a), __builtin_bit_cast(bf2, sel), c, false);
}

struct TrBuf { v2u r[3][4]; };
DI void tr_open(TrBuf& q, u32 t0, u32 t1) {
#pragma unroll
  for (int k = 0; k < 3; ++k) { q.r[k][0] = trread(t0 + k * 4096); q.r[k][1] = trread(t0 + k * 4096 + 512); q.r[k][2] = trread(t1 + k * 4096); q.r[k][3] = trread(t1 + k * 4096 + 512); }
  SB();
}
template <class PF>
DI void gemm_tr(TrBuf& q, u32 t0, u32 t1, const v4u (&xa)[7], const v4u (&xb)[7], f4& a0, f4& a1, f4& b0, f4& b1, PF pf) {
  constexpr int P = 3;
  a0 = f4{0, 0, 0, 0}; a1 = a0; b0 = a0; b1 = a0;
#pragma unroll
  for (int s = 0; s < 7; ++s) {
    const int k = s % P;
    const v4u f0 = {q.r[k][0].x, q.r[k][0].y, q.r[k][1].x, q.r[k][1].y}, f1 = {q.r[k][2].x, q.r[k][2].y, q.r[k][3].x, q.r[k][3].y};
    b1 = mfma(f1, xb[s], b1); b0 = mfma(f0, xb[s], b0);
    a1 = mfma(f1, xa[s], a1); a0 = mfma(f0, xa[s], a0);
    SB();
    if (s + P < 7) {
      q.r[k][0] = trread(t0 + (s + P) * 4096); q.r[k][1] = trread(t0 + (s + P) * 4096 + 512);
      q.r[k][2] = trread(t1 + (s + P) * 4096); q.r[k][3] = trread(t1 + (s + P) * 4096 + 512);
    }
    pf(s);
    SB();
  }
}
template <int NS, class PF>
DI void gemm_alds(u32 p, const v4u (&wf)[NS], f4& a0, f4& a1, PF pf) {
  constexpr int P = 6;
  v4u b0[P], b1[P];
#pragma unroll
  for (int k = 0; k < P; ++k) { b0[k] = ld16(p + 2 * k * KB); b1[k] = ld16(p + (2 * k + 1) * KB); }
  SB();
#pragma unroll
  for (int s = 0; s < NS; ++s) {
    a1 = mfma(wf[s], b1[s % P], a1);
    a0 = mfma(wf[s], b0[s % P], a0);
    SB();
    if (s == 2) {
      asm volatile("s_barrier" ::: "memory");
#pragma unroll
      for (int k = 0; k < 3; ++k) { b0[k] = ld16(p + 2 * (k + P) * KB); b1[k] = ld16(p + (2 * (k + P) + 1) * KB); }
    }
    if (s > 2 && s + P < NS) { b0[s % P] = ld16(p + 2 * (s + P) * KB); b1[s % P] = ld16(p + (2 * (s + P) + 1) * KB); }
    pf(s);
    SB();
  }
}
template <int J0, int J1, int EXTRA, bool ST>
DI bool g_check(u32 p, f4 (&sm)[4]) {
  const v4u ones = {0x3f803f80u, 0x3f803f80u, 0x3f803f80u, 0x3f803f80u};
  constexpr int LA = 2;
  v4u fr[4 * (J1 - J0)];
  u32 m = 0;
  sfor<J0, J1 + LA>([&](auto J) AI {
    constexpr int j = J.v;
    if constexpr (j < J1) {
      if constexpr (EXTRA >= 0) asm volatile("s_waitcnt vmcnt(%0)" :: "n"(EXTRA + 4 * (J1 - 1 - j)) : "memory");
#pragma unroll
      for (int e = 0; e < 4; ++e) fr[4 * (j - J0) + e] = ld16(p + (4 * j + e) * KB);
      SB();
    }
    constexpr int d = J0 == 0 ? 1 : LA;
    if constexpr (j >= J0 + d && j < J1 + d) {
#pragma unroll
      for (int e = 3; e >= 0; --e) {
        const v4u f = fr[4 * (j - d - J0) + e];
        if (ST) { sm[e & 1] = mfma(ones, f, sm[e & 1]); sm[2 + (e & 1)] = mfma(f, f, sm[2 + (e & 1)]); }
        else m = max4(m, f);
      }
      SB();
    }
  });
  if (ST) {
    const float t = sm[0][0] + sm[1][0];
    if (__all(t == t)) return true;
#pragma unroll
    for (int k = 0; k < 4 * (J1 - J0); ++k) m = max4(m, fr[k]);
  }
  return __all(m != 0xffffffffu);
}
#define RETRY_LOOP _Pragma("clang loop unroll(disable)") for (u32 sp = 0;; ++sp)
#define RETRY_END(k) if (sp > SPIN_CAP) break; asm volatile("s_waitcnt lgkmcnt(0)" ::: "memory")

__global__ void __launch_bounds__(512, 2)
fused_kernel(const u16* x_, const u16* wqkv_, const u16* wphi_, const u16* wo_a, const u16* w1_a, const u16* w2_a, const u16* w3_a,
             const u16* w4_a, u16* out_, unsigned char* ws_) {
  u64 p_x, p_q, p_phi, p_out, p_ws;
  asm volatile("s_load_dwordx2 %0, %5, 0\n\ts_load_dwordx2 %1, %5, 8\n\ts_load_dwordx2 %4, %5, 0x48\n\ts_load_dwordx2 %2, %5, 0x10\n\ts_load_dwordx2 %3, %5, 0x40"
               : "=&s"(p_x), "=&s"(p_q), "=&s"(p_phi), "=&s"(p_out), "=&s"(p_ws) : "s"(__builtin_amdgcn_kernarg_segment_ptr()));
  __shared__ __attribute__((aligned(1024))) unsigned char lds_raw[L_BYTES];
  const u32 L = (u32)(uintptr_t)(LAS unsigned char*)lds_raw;
  const int tid = threadIdx.x, lane = tid & 63, w = __builtin_amdgcn_readfirstlane(tid >> 6), g = lane >> 4, i16 = lane & 15;
  const int b = blockIdx.x, xcd = b & 7, ii = b >> 3;
  const int zg = xcd * 8 + (ii >> 2), js = ii & 3;
  const int yg = xcd * 4 + (ii >> 3), mi = ii & 7;
  const int zo = XP ? zg ^ 8 : zg ^ 1, th = XP ? xcd & 1 : (ii >> 2) & 1, gm = XP ? (xcd >> 1) * 8 + (ii >> 2) : yg, mm = th * 4 + js;
  const int ms = XP ? th * 4 + ((js + W4R * (ii >> 2)) & 3) : (mi + W4R * (ii >> 3)) & 7;
  const u32 l16 = lane * 16;
  const int u = w >> 2, hh = w & 1, T = (w & 2) + (hh ^ u), head = mi * 2 + hh;
  if (w == 4) {
    u64 pc; asm volatile("s_getpc_b64 %0" : "=s"(pc));
    touch(mkrs(pc & ~255ull, CODE_BYTES), (ii + 32 * lane) * 128, L + L_DUMP);
  }
  u32 xs0 = (yg * 64 + w * 16 + (mi & 1) * 8) * 896, xl0 = L + L_X + w * SLOT + (mi & 1) * 8 * X_RS;
  u32 xs1 = (yg * 64 + w * 16 + (~mi & 1) * 8) * 896, xl1 = L + L_X + w * SLOT + (~mi & 1) * 8 * X_RS;
  const u32 tn = w == 5 ? 37 : 4, tb = w == 5 ? 0 : OFF_HINT;
  u32 tv = (tb + (b * tn + lane) * 128) | ((u32)lane < tn ? 0 : OOB);
  PINS(xs0); PINS(xl0); PINS(xs1); PINS(xl1); PIN(tv);
  SB();
  asm volatile("s_waitcnt lgkmcnt(0)" : "+s"(p_x), "+s"(p_q), "+s"(p_phi), "+s"(p_out), "+s"(p_ws));
  const rsrc_t rx = mkrs(p_x, S_ * D_ * 2);
  const rsrc_t rq = mkrs(p_q, D_ * 3 * D_ * 2);
  const rsrc_t rws = mkrs(p_ws, WS_BYTES);
  if (u == 0) {
    __builtin_amdgcn_s_setprio(2);
    asm volatile("s_bfm_b64 exec, 56, 0" ::: "memory");
    XDMA<8, X_RS>(rx, l16, xs0, 896, xl0);
    XDMA<8, X_RS>(rx, l16, xs1, 896, xl1);
    asm volatile("s_mov_b64 exec, -1" ::: "memory");
  } else if (w == 5) {
    __builtin_amdgcn_s_setprio(3);
    touch(rq, tv, L + L_DUMP);
    __builtin_amdgcn_s_setprio(0);
  } else if (w == 6) {
    __builtin_amdgcn_s_setprio(3);
    touch(rws, tv, L + L_DUMP);
    __builtin_amdgcn_s_setprio(0);
  }
  SB();
  const u32 wc = (lane & 7) ^ ((lane >> 3) & 2) ^ ((w & 1) << 2);
  u32 wv = (((w & 3) * 8 + (lane >> 3)) * 2688 + mi * 112 + wc * 16) | (wc < 7 ? 0 : OOB);
  u32 wl = L + (w & 3) * KB;
  PIN(wv); PINS(wl);
  SB();
  u32 epoch;
  asm volatile("s_load_dword %0, %1, 0" : "=s"(epoch) : "s"(p_ws));
  const v4u SENT = {0xffffffffu, 0xffffffffu, 0xffffffffu, 0xffffffffu};
  const v4u ones = {0x3f803f80u, 0x3f803f80u, 0x3f803f80u, 0x3f803f80u};
  const f4 z4 = {0, 0, 0, 0};
  const int q4 = i16 >> 2, p4 = i16 & 3;
  const bool padg = g == 3;
  u32 vz = lane == 0 ? 0 : OOB;
  PIN(vz);
  auto post = [&](u32 off) AI { __builtin_amdgcn_raw_buffer_store_b64(v2u{epoch, 0x9e3779b9u}, rws, (int)vz, (int)off, SC1); };
  auto post_t = [&](u32 off) AI { const u32 t = now(); stg(rws, vz + off, v4u{epoch, 0x9e3779b9u, t, 0}); return t; };
  const int n = js * 7 + (w < 7 ? w : 0);
  u32 kvp_off = OFF_KVP + ((mi * 8 + w) * 4 + g) * 8 * KB + yg * 256 + i16 * 16;
  u32 kvt_off = g == 0 ? OFF_KVT + (mi * 32 + yg) * 256 + i16 * 16 : OOB;
  u32 kvt8 = g < 2 ? OFF_KVT + (mi * 32 + yg) * 256 + i16 * 16 + g * 8 : OOB;
  PIN(kvt8);
  u32 o_off = l16 + OFF_O + (((yg * 2 + (T >> 1)) * 16 + head) * 2 + (T & 1)) * KB;
  const u32 cko = (2 * (n & 1) + (g >> 1)) * 256;
  u32 h_off = ((zg * 14 + (n >> 1)) * 2 + (g & 1)) * KB + cko + i16 * 16 + OFF_H;
  PIN(h_off); PIN(kvp_off); PIN(o_off);
  PIN(kvt_off);
  float keep = padg ? 0.f : 1.f;
  const u32 trx = (((q4 >> 1) & 1) | ((g & 1) << 1)) * 32;
  u32 tr0 = L + (8 * g + q4) * 128 + ((56 * hh + 8 * p4) ^ trx);
  u32 tr1 = L + (8 * g + q4) * 128 + ((56 * hh + 32 + 8 * (p4 == 3 ? 2 : p4)) ^ trx);
  u32 kx_w = L + L_KX + (w ^ 4) * KB + l16, kx_r = L + L_KX + w * KB + l16;
  u32 pk_w = L + L_PK + (hh * 64 + T * 16 + i16) * PK_RS + g * 8;
  u32 va_w = L + (u ? L_V1 : L_V0) + (hh * 64 + T * 16 + i16) * V_RS + g * 8, vb_w = L + (u ? L_V1 : L_V0) + (hh * 64 + (T ^ 1) * 16 + i16) * V_RS + g * 8;
  u32 kv_a = L + L_PK + (u * 64 + g * 8 + q4) * PK_RS + ((w >> 1) & 1) * 64 + p4 * 8;
  u32 kv_b = L + L_V0 + (u * 64 + g * 8 + q4) * V_RS + (w & 1) * 32 + p4 * 8, kv_c = kv_b + (L_V1 - L_V0);
  u32 ph_a = L + L_PHI + hh * 1568 + (4 * g + q4) * 56 + p4 * 8;
  u32 xr_r = L + L_VA + i16 * 256 + ((((w < 7 ? w : 0) * 2 + (g >> 1)) ^ i16) * 16) + (g & 1) * 8;
  PIN(xr_r);
  u32 xo_r = L + L_X + (T ^ 1) * SLOT + u * 7 * KB + l16, xm_r = L + L_X + T * SLOT + 7 * KB + l16, ph_b = ph_a + (p4 == 3 ? 24 : 32);
  u32 tk0 = tr0 + L_K + u * 28 * KB, tk1 = tr1 + L_K + u * 28 * KB, tv0 = tr0 + (u ? L_VB : L_VA), tv1 = tr1 + (u ? L_VB : L_VA);
  u32 tq0 = tr0 + (u ? L_QB : L_QA), tq1 = tr1 + (u ? L_QB : L_QA);
  PIN(xm_r); PIN(ph_b); PIN(tk0); PIN(tk1); PIN(tv0); PIN(tv1); PIN(tq0); PIN(tq1);
  u32 h32 = OFF_HINT + (mi * 32 + (lane & 31)) * 64, uf_off = OFF_HINT + 4 * 256 * 64 + 32 + lane * 64;
  PIN(uf_off);
  PIN(keep); PIN(kx_w); PIN(kx_r);
  PIN(pk_w); PIN(va_w); PIN(vb_w); PIN(kv_a); PIN(kv_b); PIN(kv_c); PIN(ph_a); PIN(xo_r); PIN(h32);
  SB();
  const bool light = ((mi + 1) & 6) == 0;
  const u32 lid = yg * 2 + (mi & 1);
  const u32 nx = light ? ((lid & 15) == 5 ? 2 : 1) : 0;
  u32 u_ts = 0, u_dst = 0, u_lo = 0, u_hi = 0, u_keep = 0;
  if (u == 1) {
    const u32 unit = (light ? lid : 64 + yg * 6 + mi - 1) * 4 + (w - 4);
    const u32 sec = ((unit + 1936) >> 11) + ((unit + 1838) >> 11) + ((unit + 1740) >> 11) + ((unit + 1348) >> 11);
    const u64 base = ((const __attribute__((address_space(4))) u64*)__builtin_amdgcn_kernarg_segment_ptr())[3 + sec];
    const u32 first = (u32)(0x2bc1340d2070000ull >> (sec * 12)) & 0xfffu, uu = unit - first;
    const u32 sh3 = sec == 3 ? 2 : 0;
    const bool prm = (sec & 3) == 0;
    const u32 u7 = (uu * 9363) >> 16, st = u7 >> sh3, cb = uu - ((st * 7) << sh3), m = sec == 4 ? (u7 * 9363) >> 16 : 0;
    const rsrc_t rs = mkrs(base, sec >= 3 ? D_ * F_ * 2 : D_ * D_ * 2);
    const u32 vo = (st * (sec == 0 ? 28 : 32) + (lane >> 3)) * (896 << sh3) + cb * 128 + (lane & 7) * 16;
    const u32 r0 = (prm ? 4 * g : 8 * g) + q4;
    u_ts = ((0x7eee0u >> (sec * 4)) & 15) * KB + (sec == 0 ? 16 * KB : 0);
    u_dst = l16 + OFF_WO + first * 4 * KB + cb * 4 * u_ts + (m * 189 + st) * KB;
    u_lo = wl + L_VA + ((r0 >> 3) << 12) + (r0 & 7) * 128 + p4 * 8;
    u_hi = u_lo + (prm ? 8192 : 512);
    u_keep = (sec == 0 && padg) ? 0u : 0xffffffffu;
    dma_nt<4, 4 * KB>(rs, vo, 0, (8 * 896) << sh3, wl + L_VA);
  }
  PINS(u_ts); PIN(u_dst); PIN(u_lo); PIN(u_hi); PIN(u_keep);
  SB();
  v4u xa[7], xb[7], uq[4];
  if (u == 0) {
    u32 xp = L + L_X + w * SLOT + i16 * X_RS + g * 16, xw = L + L_X + w * SLOT + l16;
    PIN(xp); PIN(xw);
    VMW(0);
#pragma unroll
    for (int s = 0; s < 7; ++s) xb[s] = ld16(xp + 448 + s * 64);
#pragma unroll
    for (int s = 0; s < 7; ++s) xa[s] = ld16(xp + s * 64);
    SB();
    f4 sq = z4, sm = z4;
#pragma unroll
    for (int s = 0; s < 7; ++s) { sq = mfma(xb[s], xb[s], sq); sm = mfma(ones, xb[s], sm); }
#pragma unroll
    for (int s = 0; s < 7; ++s) { sq = mfma(xa[s], xa[s], sq); sm = mfma(ones, xa[s], sm); }
    const float mu = sm[0] * (1.0f / D_), dgv = diag(sq, p4);
    const float var = fmx(fmaf(-mu, mu, rsum(g == q4 ? dgv : 0.f) * (1.0f / D_)), 0.f);
    const float rs = __builtin_amdgcn_rsqf(var + 1e-5f), nb = -mu * rs;
#pragma unroll
    for (int s = 0; s < 7; ++s) {
#pragma unroll
      for (int e = 0; e < 4; ++e) xb[s][e] = pk2(fmaf(blo(xb[s][e]), rs, nb), fmaf(bhi(xb[s][e]), rs, nb));
      st16(xw + (7 + s) * KB, xb[s]);
      SB();
    }
#pragma unroll
    for (int s = 0; s < 7; ++s) {
#pragma unroll
      for (int e = 0; e < 4; ++e) xa[s][e] = pk2(fmaf(blo(xa[s][e]), rs, nb), fmaf(bhi(xa[s][e]), rs, nb));
      st16(xw + s * KB, xa[s]);
      SB();
    }
    __builtin_amdgcn_s_setprio(0);
  } else {
    dma_in<1, 0>(mkrs(p_phi, 16 * 28 * 28 * 2), l16, mi * 3136 + (w - 4) * KB, 0, L + L_PHI + (w - 4) * KB);
    dma_in<14, 4 * KB>(rq, wv, 896, 32 * 2688, wl + L_K);
    VMW(15);
    v2u lo[4], hi[4];
#pragma unroll
    for (int t = 0; t < 4; ++t) { lo[t] = trread(u_lo + t * 32); hi[t] = trread(u_hi + t * 32); }
#pragma unroll
    for (int t = 0; t < 4; ++t) { uq[t] = v4u{lo[t].x, lo[t].y, hi[t].x & u_keep, hi[t].y & u_keep}; PIN(uq[t]); }
    SB();
    dma_in<4, 4 * KB>(rq, wv, 1792, 32 * 2688, wl + L_VA);
    VMW(11);
  }
  bar();
  asm volatile("" : "+s"(epoch));
  const u64 tag = (u64)epoch | (0x9e3779b9ull << 32);
#pragma unroll
  for (int s = 0; s < 7; ++s) xb[s] = ld16(xo_r + s * KB);
  if (u == 1) {
#pragma unroll
    for (int s = 0; s < 7; ++s) xa[s] = ld16(xm_r + s * KB);
  }
  v4u phA, phB;
  {
    const v2u l0 = trread(ph_a), h0 = trread(ph_a + 16 * 56), l1 = trread(ph_b), h1 = trread(ph_b + 16 * 56);
    phA = v4u{l0.x, l0.y, padg ? 0u : h0.x, padg ? 0u : h0.y};
    phB = v4u{l1.x, l1.y, padg ? 0u : h1.x, padg ? 0u : h1.y};
  }
  TrBuf tb_;
  u32 rt0 = 0, cy0 = 0;
  if (u == 1) {
    { u64 a, c; asm volatile("s_memrealtime %0\n\ts_memtime %1\n\ts_waitcnt lgkmcnt(0)" : "=&s"(a), "=&s"(c) :: "memory"); rt0 = (u32)a; cy0 = (u32)c; }
    dma_in<3, 4 * KB>(rq, wv, 1792 + 4 * 32 * 2688, 32 * 2688, wl + L_VA + 16 * KB);
#pragma unroll
    for (int t = 0; t < 4; ++t) stg(rws, u_dst + t * u_ts, uq[t]);
    VMW(11);
    bar();
  }
  tr_open(tb_, tk0, tk1);
  PIN(phA); PIN(phB);
  v4u wo_[16], w1_[14], w2_[14], w3f[28], w4f[28];
  u32 lw = w < 7 ? l16 : OOB;
  PIN(lw);
  auto pf = [&](int k) AI {
    if (k < 16) wo_[k] = ldg(rws, lw + (k & 3) * KB, OFF_WO + (n * 16 + (k & ~3)) * KB);
    else if (k < 30) w1_[k - 16] = ldg(rws, lw + (k & 3) * KB, OFF_W1 + (n * 14 + ((k - 16) & ~3)) * KB);
    else if (k < 44) w2_[k - 30] = ldg(rws, lw + ((k - 30) & 3) * KB, OFF_W2 + (n * 14 + ((k - 30) & ~3)) * KB);
    else if (k < 72) { const int t = (k - 44) & 1, st = (k - 44) >> 1;
      w3f[t * 14 + st] = ldg(rws, lw + (st & 3) * KB, OFF_W3 + ((ms * 14 + w * 2 + t) * 14 + (st & ~3)) * KB); }
    else if (k < 100) { const int sl = (k - 72) / 7, c = (k - 72) % 7;
      w4f[k - 72] = ldg(rws, (lw | NOW4) + (c & 3) * KB, OFF_W4 + ((ms * 28 + ((js + 1 + sl) & 3) * 7 + w) * 7 + (c & ~3)) * KB); }
  };
#define PFR(lo, hi) do { _Pragma("unroll") for (int k_ = (lo); k_ < (hi); ++k_) pf(k_); asm volatile("" ::: "memory"); SB(); } while (0)
#define PFS(base, cnt) [&](int s_) AI { if (s_ < NSP && (s_ * (cnt) + NSP - 1) / NSP != ((s_ + 1) * (cnt) + NSP - 1) / NSP) pf((base) + (s_ * (cnt) + NSP - 1) / NSP); }
  {
    f4 a0, a1, b0, b1;
    gemm_tr(tb_, tk0, tk1, xa, xb, a0, a1, b0, b1, [&](int s_) AI { if (s_ == 4) asm volatile("s_cmp_lg_u32 %0, 0\n\ts_cbranch_scc1 1f\n\ts_barrier\n1:" :: "s"(u) : "memory", "scc"); });
    st16(kx_w, pack8(b0, b1));
    const v4u ka = pack8(a0, a1);
    f4 p0 = mfma(phA, ka, z4), p1 = mfma(phB, ka, z4);
    u32 qs = u * 224 * 2688, ql = wl + (u ? L_QB : L_QA);
    PINS(qs); PINS(ql);
    auto v_part = [&]() AI {
      gemm_tr(tb_, tv0, tv1, xa, xb, a0, a1, b0, b1, [&](int) AI {
        asm volatile("s_mov_b32 m0, %1\n\ts_add_u32 %1, %1, 0x1000\n\tbuffer_load_dwordx4 %2, %3, %0 offen lds\n\ts_add_u32 %0, %0, %4"
                     : "+s"(qs), "+s"(ql) : "v"(wv), "s"(rq), "n"(32 * 2688) : "memory", "scc"); });
    };
    auto v_out = [&](float one) AI {
      if (padg) { a1[0] = one; b1[0] = one; }
      const v4u va = pack8(a0, a1), vb = pack8(b0, b1);
      st8(vb_w, vb.x, vb.y); st8(vb_w + 32, vb.z, vb.w);
      st8(va_w, va.x, va.y); st8(va_w + 32, va.z, va.w);
    };
    auto k_part = [&]() AI {
      v4u pos, neg;
      hedgehog(p0, p1, keep, pos, neg);
      st8(pk_w, pos.x, pos.y); st8(pk_w + 32, pos.z, pos.w); st8(pk_w + 64, neg.x, neg.y); st8(pk_w + 96, neg.z, neg.w);
    };
    if (u == 0) {
      dma_in<7, 4 * KB>(rq, wv, 1792 + 224 * 2688, 32 * 2688, wl + L_VB);
      bar();
      tr_open(tb_, tv0, tv1);
      const v4u kb = ld16(kx_r);
      SB();
      v_part();
      p0 = mfma(phA, kb, p0); p1 = mfma(phB, kb, p1);
      v_out(1.f);
      VMW(7);
      bar();
      for (u32 e = 0; e < nx; ++e) {
        const u32 v = e == 0 ? 324 + lid : 388 + (lid >> 4), s2 = (v * 9363) >> 16, c2 = v - s2 * 7;
        const u32 a = L + (e == 0 ? L_U0 : L_PHI) + (4 * g + q4) * 128 + p4 * 8 + w * 32;
        const v2u lo = trread(a), hi = trread(a + 2048);
        stg(rws, l16 + OFF_W4 + (c2 * 28 + ((s2 * 9363) >> 16) * 189 + s2 + w * 7) * KB, v4u{lo.x, lo.y, hi.x, hi.y});
      }
      k_part();
#pragma unroll
      for (int d = 0; d < 2; ++d) {
        const u32 c = i16 ^ ((w & 1) * 8 + d * 4 + g);
        dma_in<1, 0>(rx, (g * 896 + js * 224 + c * 16) | (c < 14 ? 0 : OOB), (zg * 32 + w * 8 + d * 4) * 896, 0, L + L_VA + (w * 2 + d) * KB);
      }
      if (w == 3) peek_issue(rws, uf_off);
    } else {
      if (light) VMW(0); else VMW(4);
      stg(rws, kvp_off, SENT); stg(rws, kvp_off - 4 * 32 * KB, SENT);
      if (w == 7) stg(rws, kvt_off, SENT);
      bar();
      const v4u kb = ld16(kx_r);
      if ((u32)(w - 5) < nx) {
        const u32 v = w == 5 ? 324 + lid : 388 + (lid >> 4), s2 = (v * 9363) >> 16, c2 = v - s2 * 7;
        dma_nt<4, KB>(mkrs(((const __attribute__((address_space(4))) u64*)__builtin_amdgcn_kernarg_segment_ptr())[7], D_ * F_ * 2),
                      (s2 * 32 + (lane >> 3)) * 896 + c2 * 128 + (lane & 7) * 16, 0, 8 * 896, L + (w == 5 ? L_U0 : L_PHI));
      }
      if (light && w == 4) post(OFF_HINT + (4 * 256 + lid) * 64 + 32);
      p0 = mfma(phA, kb, p0); p1 = mfma(phB, kb, p1);
      k_part();
      VMW(0);
      if (w == 6) *(volatile LAS u32*)(uintptr_t)(L + L_SF) = 0;
      bar();
      tr_open(tb_, tv0, tv1);
      if (w == 5) {
        u64 a, c; asm volatile("s_memrealtime %0\n\ts_memtime %1\n\ts_waitcnt lgkmcnt(0)" : "=&s"(a), "=&s"(c) :: "memory");
        const u32 t = (u32)a + (u32)((float)KVC * (float)((u32)a - rt0) * __builtin_amdgcn_rcpf((float)((u32)c - cy0)));
        stg(rws, vz + OFF_HINT + (5 * 256 + mi * 32 + yg) * 64, v4u{epoch, 0x9e3779b9u, t, 0});
      }
      SB();
      v_part();
      v_out(0.f);
    }
  }
  bar();
  if (w == 7) peek_issue(rws, h32 + 5 * 256 * 64);
  {
    v2u a0[2], a1[2], b0[2], b1[2], d0[4], d1[4];
#pragma unroll
    for (int s = 0; s < 2; ++s) {
      a0[s] = trread(kv_a + s * 32 * PK_RS); a1[s] = trread(kv_a + (s * 32 + 4) * PK_RS);
      d0[s] = trread(kv_b + s * 32 * V_RS); d1[s] = trread(kv_b + (s * 32 + 4) * V_RS);
      b0[s] = trread(kv_a + s * 32 * PK_RS + 32); b1[s] = trread(kv_a + (s * 32 + 4) * PK_RS + 32);
    }
#pragma unroll
    for (int s = 0; s < 2; ++s) { d0[2 + s] = trread(kv_c + s * 32 * V_RS); d1[2 + s] = trread(kv_c + (s * 32 + 4) * V_RS); }
    SB();
    f4 c0 = z4, c1 = z4;
#pragma unroll
    for (int s = 0; s < 4; ++s) {
      const v4u vf = {d0[s].x, d0[s].y, d1[s].x, d1[s].y};
      c0 = mfma(v4u{a0[s & 1].x, a0[s & 1].y, a1[s & 1].x, a1[s & 1].y}, vf, c0);
      c1 = mfma(v4u{b0[s & 1].x, b0[s & 1].y, b1[s & 1].x, b1[s & 1].y}, vf, c1);
    }
    stg(rws, kvp_off, nosent4(pack8(c0, c1)));
    SB();
  }
  VMW(1);
  if (w == 3) peek_take<1>(rws, uf_off, tag);
  bar();
  u32 t_kv = 0;
  v4u sel;
  { const u32 one = (i16 & 1) ? 0x3f800000u : 0x00003f80u;
    sel = v4u{(i16 >> 1) == 0 ? one : 0u, (i16 >> 1) == 1 ? one : 0u, (i16 >> 1) == 2 ? one : 0u, (i16 >> 1) == 3 ? one : 0u}; }
  if (w == 7) {
    __builtin_amdgcn_s_setprio(3);
    PIN(sel);
    t_kv = peek_take<1>(rws, h32 + 5 * 256 * 64, tag) + DTX;
    wait_until(t_kv);
    dma_ws<2>(rws, l16, OFF_KVP + (mi * 32 + yg) * 8 * KB, L + L_KP);
    *(volatile LAS u32*)(uintptr_t)(L + L_SF) = 2;
  }
  u32 h8 = OFF_HINT + (yg * 8 + (lane & 7)) * 64;
  PIN(h8);
  f4 pq0, pq1;
  {
    f4 a0, a1, b0, b1;
    tr_open(tb_, tq0, tq1);
    gemm_tr(tb_, tq0, tq1, xa, xb, a0, a1, b0, b1, [&](int s_) AI { if (s_ < QPF) pf(s_); });
    st16(kx_w - (L_KX - L_QX), pack8(b0, b1));
    const v4u qa = pack8(a0, a1);
    pq0 = mfma(phA, qa, z4); pq1 = mfma(phB, qa, z4);
    PIN(pq0); PIN(pq1);
  }
  v4u qpos, qneg;
  auto phi_q = [&]() AI {
    const v4u qb = ld16(kx_r - (L_KX - L_QX));
    hedgehog(mfma(phA, qb, pq0), mfma(phB, qb, pq1), keep, qpos, qneg);
    PIN(qpos); PIN(qneg);
    SB();
  };
  auto make_o = [&]() AI {
    v4u kf[4];
#pragma unroll
    for (int k = 0; k < 4; ++k) kf[k] = ld16(L + L_KF + (hh * 4 + k) * KB + l16);
    SB();
    f4 n1 = mfma(kf[1], qpos, z4), n0 = mfma(kf[0], qpos, z4);
    n1 = mfma(kf[3], qneg, n1); n0 = mfma(kf[2], qneg, n0);
    const float rd = __builtin_amdgcn_rcpf(rsum(padg ? n1[0] : 0.f) + 1e-6f);
    n0 *= rd; n1 *= rd;
    v4u ov = nosent4(pack8(n0, n1));
    if (padg) { ov.z = 0; ov.w = 0; }
    stg(rws, o_off, ov);
    SB();
  };
  const u32 hme = OFF_HINT + (zg * 4 + js) * 64;
  if (w < 7) {
  if (w < 2) {
    for (u32 sp = 0; sp < (1u << 22) && __builtin_amdgcn_readfirstlane(*(volatile LAS u32*)(uintptr_t)(L + L_SF)) != 2; ++sp) { }
    const u32 om = l16 + OFF_O + (yg * 64 + w * 32 + mi * 4) * KB;
#pragma unroll
    for (int k = 0; k < 4; ++k) stg(rws, om + k * KB, SENT);
    VMW(0);
  }
  bar();
  v2u xr[2];
  xr[0] = *(LAS v2u*)(uintptr_t)xr_r; xr[1] = *(LAS v2u*)(uintptr_t)(xr_r + 4 * KB);
  PFR(QPF, PA);
  phi_q();
  PFR(PA, PA2);
  bar();
  make_o();
  bar();
  if (HM == 1) stg(rws, h_off, SENT);
  f4 hold0, hold1;
  const u32 A = L + L_ACT + l16;
  const int tk = zg * 32 + (g & 1) * 16 + i16 + ((js & 1) ? -1 - (js >> 1) : 1 + (js >> 1));
  u32 hs_off = ((((tk & (S_ - 1)) >> 5) * 14 + (n >> 1)) * 2 + ((tk >> 4) & 1)) * KB + cko + (tk & 15) * 16 + OFF_HS;
  u32 hs_keep = (tk < 0 || tk >= S_) ? 0u : 0xffffffffu;
  u32 x1_off = h_off + (OFF_X1 - OFF_H);
  u32 st_r = L + L_ST + i16 * 8;
  if (YP == 1 && w >= 4) __builtin_amdgcn_s_setprio(1);
  PFR(PB, PB + R10);
  PIN(hs_off); PIN(hs_keep); PIN(x1_off); PIN(st_r);
  f4 a0 = {blo(xr[0].x), bhi(xr[0].x), blo(xr[0].y), bhi(xr[0].y)};
  f4 a1 = {blo(xr[1].x), bhi(xr[1].x), blo(xr[1].y), bhi(xr[1].y)};
  if (HM == 1) asm volatile("s_waitcnt vmcnt(%0)" :: "n"(R10) : "memory");
  bar();
  stg(rws, h_off + (OFF_HS - OFF_H), SENT);
  PFR(PB + R10, PC);
  PIN(a0); PIN(a1);
  bar();
  if (w == 0) post_t(hme + 1 * 256 * 64);
  {
    gemm_alds<16>(A, wo_, a0, a1, PFS(PC, NL));
    const v4u p = nosent4(pack8(a0, a1));
    stg(rws, h_off, to_chunk(p));
    asm volatile("s_waitcnt vmcnt(%0)" :: "n"(NL + 1) : "memory");
    SB();
    hold0 = f4{blo(p.x), bhi(p.x), blo(p.y), bhi(p.y)};
    hold1 = f4{blo(p.z), bhi(p.z), blo(p.w), bhi(p.w)};
  }
  bar();
  if (w == 0) post(hme + 7 * 256 * 64);
  f4 xb0, xb1;
  {
    PFR(PC + NL, PC + NL + R1);
    bar();
    stg(rws, x1_off, SENT);
    PFR(PC + NL + R1, PD);
    f4 cs = z4;
#pragma unroll
    for (int s = 0; s < 14; ++s) cs = mfma(w1_[s], ones, cs);
    PIN(cs);
    bar();
    if (w == 0) post_t(hme + 2 * 256 * 64);
    a0 = z4; a1 = z4;
    gemm_alds<14>(A, w1_, a0, a1, PFS(PD, NL));
    for (u32 sp = 0; sp < (1u << 20) && __builtin_amdgcn_readfirstlane(*(volatile LAS u32*)(uintptr_t)(L + L_SF)) == 0; ++sp) { }
    const f2 s0 = ldf2(st_r), s1 = ldf2(st_r + 128), e0 = ldf2(st_r + 256), e1 = ldf2(st_r + 384);
#pragma unroll
    for (int r = 0; r < 4; ++r) {
      a0[r] = gelu(fmaf(a0[r], s0[1], s0[0] * cs[r]));
      a1[r] = gelu(fmaf(a1[r], s1[1], s1[0] * cs[r]));
    }
    stg(rws, hs_off, to_chunk(nosent4(pack8(a0, a1))) & hs_keep);
    asm volatile("s_waitcnt vmcnt(%0)" :: "n"(NL + 1) : "memory");
    bar();
#pragma unroll
    for (int r = 0; r < 4; ++r) { xb0[r] = fmaf(hold0[r], e0[1], e0[0]); xb1[r] = fmaf(hold1[r], e1[1], e1[0]); }
    PIN(xb0); PIN(xb1);
  }
  PFR(PD + NL, PD + NL + R1);
  bar();
  PFR(PD + NL + R1, PE);
  bar();
  if (w == 0) post_t(hme + 3 * 256 * 64);
  {
    gemm_alds<14>(A, w2_, xb0, xb1, PFS(PE, NL));
    const v4u p = nosent4(pack8(xb0, xb1));
    stg(rws, x1_off, to_chunk(p));
    bar();
    hold0 += f4{blo(p.x), bhi(p.x), blo(p.y), bhi(p.y)};
    hold1 += f4{blo(p.z), bhi(p.z), blo(p.w), bhi(p.w)};
    PIN(hold0); PIN(hold1);
  }
  if (YP == 2 && w >= 4) __builtin_amdgcn_s_setprio(1);
  constexpr int W3E = (PE + NL + R1) & ~1;
  PFR(PE + NL, W3E);
  u32 hid_w = L + L_HID + w * 2 * KB + l16, hid_r = L + L_HID + l16;
  u32 rs_w = l16 + OFF_RS + ((gm * 64 + mm) * 7 + w) * KB;
  u32 out_off = (zg * 32 + (g & 1) * 16 + i16) * 896 + (n * 16 + (g >> 1) * 8) * 2;
  u32 xp_s = OFF_X1 + (w < 3 ? zo * 28 + 16 + w * 4 : zg * 28 + w * 4) * KB, xp_l = L + L_ACT + (w < 3 ? 16 + w * 4 : 28 + w * 4) * KB;
  PINS(xp_s); PINS(xp_l);
  PIN(hid_w); PIN(hid_r); PIN(rs_w); PIN(out_off);
  constexpr int CSE = (PE + NL - 44) / 2 < 14 ? (PE + NL - 44) / 2 : 14;
  f4 cs[2] = {z4, z4};
#pragma unroll
  for (int s = 0; s < CSE; ++s) { cs[0] = mfma(w3f[s], ones, cs[0]); cs[1] = mfma(w3f[14 + s], ones, cs[1]); }
  PIN(cs[0]); PIN(cs[1]);
  bar();
  dma_ws<1>(rws, l16, xp_s, xp_l);
  bar();
  f4 pu[4][2] = {{z4, z4}, {z4, z4}, {z4, z4}, {z4, z4}};
  f4 sv[2];
  float fr0, fr1;
  u32 s5_r = L + L_S5 + i16 * 16;
  PIN(s5_r);
  float gy[2], gt[2];
  v4u gp;
  auto mop = [&](int t, int j) AI {
    const int pr = j / 17, o = j % 17, e = o & 1, c = pr >> 1, r = (pr & 1) * 2 + e;
    const f4& q = sv[t & 1];
    if (j == 68) st16(hid_w + (t >> 1) * 14 * KB + (t & 1) * KB, gp);
    else if (o < 2) gy[e] = fmaf(q[0], cs[c][r], pu[t][c][r]);
    else if (o < 4) gt[e] = gy[e] * gy[e];
    else if (o < 6) gt[e] = fmaf(gt[e], q[2], q[1]);
    else if (o < 8) gt[e] = gt[e] * gy[e];
    else if (o < 10) gt[e] = __builtin_amdgcn_exp2f(gt[e]);
    else if (o < 12) gt[e] = gt[e] + 1.0f;
    else if (o < 14) gt[e] = __builtin_amdgcn_rcpf(gt[e]);
    else if (o < 16) { gy[e] = gy[e] * gt[e]; if (e == 0) PIN(gy[0]); }
    else gp[pr] = pk2(gy[0], gy[1]);
  };
#define MOPS(T0, NOPS, m, NM) sfor<(m) * (NOPS) / (NM), ((m) + 1) * (NOPS) / (NM)>([&](auto J_) AI { mop((T0) + J_.v / 69, J_.v % 69); SB(); })
  constexpr int G0 = W3E, N3 = G0 < 72 ? 72 - G0 : 0, K3 = 4 + N3 * 3 / 2, GB = G0 + N3 + FA;
  v4u hf[14];
  {
    constexpr int P = 6;
    v4u bq[P];
    auto fa = [&](int k) AI { return A + (k < 12 ? k : k < 24 ? 28 + k - 12 : k < 32 ? 2 * (k - 18) : k < 40 ? 2 * (k - 26) + 1 : k) * KB; };
#pragma unroll
    for (int k = 0; k < P; ++k) bq[k] = ld16(fa(k));
    SB();
    sfor<0, 56>([&](auto K) AI {
      constexpr int k = K.v;
      constexpr int t = k < 12 ? k & 1 : k < 24 ? 2 + (k & 1) : k < 32 ? 0 : k < 40 ? 1 : 2 + (k & 1);
      constexpr int s = k < 12 ? k >> 1 : k < 24 ? (k - 12) >> 1 : k < 32 ? k - 18 : k < 40 ? k - 26 : (k - 28) >> 1;
      sfor<0, 2>([&](auto C) AI {
        constexpr int c = C.v;
        pu[t][c] = mfma(w3f[c * 14 + s], bq[k % P], pu[t][c]);
        SB();
        if constexpr (k >= 33 && k < 50) MOPS(0, 138, (k - 33) * 2 + c, 34);
      });
      if constexpr (t == 0 && s >= CSE) { cs[0] = mfma(w3f[s], ones, cs[0]); cs[1] = mfma(w3f[14 + s], ones, cs[1]); SB(); }
      if constexpr (k == 6 || k == 18) asm volatile("s_barrier" ::: "memory");
      if constexpr (k == 14) {
        asm volatile("s_waitcnt vmcnt(%0)" :: "n"(N3 ? (K3 < 14 ? N3 + (14 - K3) * FA / (56 - K3) : 10 * 2 / 3) : 10 * FA / 52) : "memory");
#pragma unroll
        for (int e = 0; e < 4; ++e) hf[e] = ld16(xp_l + l16 + e * KB);
      }
      if constexpr (k == 17) {
        if (!__all(max4(max4(max4(max4(0, hf[0]), hf[1]), hf[2]), hf[3]) != 0xffffffffu)) {
          RETRY_LOOP {
            RETRY_END(7);
            dma_ws<1>(rws, l16, xp_s, xp_l);
            VMW(0);
            if (__all(max4(max4(max4(max4(0, ld16(xp_l + l16)), ld16(xp_l + l16 + KB)), ld16(xp_l + l16 + 2 * KB)), ld16(xp_l + l16 + 3 * KB)) != 0xffffffffu)) break;
          }
        }
      }
      if constexpr (k == 31) { asm volatile("s_barrier" ::: "memory"); sv[0] = __builtin_bit_cast(f4, ld16(s5_r)); sv[1] = __builtin_bit_cast(f4, ld16(s5_r + 256)); }
      if constexpr (k == 50) { asm volatile("s_waitcnt lgkmcnt(0)\n\ts_barrier" ::: "memory"); }
      if constexpr (k + P < 56) bq[k % P] = ld16(fa(k + P));
      if constexpr (k >= 50) sfor<(k - 50) * 14 / 6, (k - 49) * 14 / 6>([&](auto E) AI { hf[E.v] = ld16(hid_r + E.v * KB); });
      if constexpr (k >= 4 && k < K3) sfor<(k - 4) * 2 / 3, (k - 3) * 2 / 3>([&](auto E) AI { pf(G0 + E.v); });
      if constexpr (k >= K3) sfor<(k - K3) * FA / (56 - K3), (k + 1 - K3) * FA / (56 - K3)>([&](auto E) AI { pf(G0 + N3 + E.v); });
      SB();
    });
  }
  f4 own0, own1;
  {
    v4u pend[2];
    auto rs_off = [&](int rd) AI { return (rs_w | NORS) + ((((rd >> 2) ? th : th ^ 1) * 4 + ((js + 1 + rd) & 3)) * 56) * KB; };
    sv[0] = __builtin_bit_cast(f4, ld16(s5_r + 512)); sv[1] = __builtin_bit_cast(f4, ld16(s5_r + 768));
    fr0 = sv[0][3]; fr1 = sv[1][3];
    sfor<0, 8>([&](auto RD) AI {
      constexpr int rd = RD.v, sl = rd & 3;
      f4 c0 = z4, c1 = z4;
      sfor<0, 7>([&](auto C) AI {
        constexpr int c = C.v;
        c1 = mfma(w4f[sl * 7 + c], hf[2 * c + 1], c1);
        SB();
        if constexpr (rd < 3 && rd + c > 0) MOPS(2, 138, (rd * 7 + c) * 2 - 2, 40);
        c0 = mfma(w4f[sl * 7 + c], hf[2 * c], c0);
        SB();
        if constexpr (rd < 3 && rd + c > 0) MOPS(2, 138, (rd * 7 + c) * 2 - 1, 40);
        if constexpr (rd == 3 && c == 0) { asm volatile("s_waitcnt lgkmcnt(0)\n\ts_barrier" ::: "memory"); }
        if constexpr (rd == 3) { hf[2 * c] = ld16(hid_r + (14 + 2 * c) * KB); hf[2 * c + 1] = ld16(hid_r + (15 + 2 * c) * KB); }
        if constexpr (rd < DEF) sfor<(rd * 7 + c) * (100 - GB) / (7 * DEF), (rd * 7 + c + 1) * (100 - GB) / (7 * DEF)>([&](auto E) AI { pf(GB + E.v); });
        if constexpr (DEF == 2 && c == 1 && rd == 1) pend[0] = pack8(own0, own1);
        if constexpr (c == 1 && rd >= DEF) stg(rws, rs_off(rd - 1), pack8(own0, own1));
        if constexpr (DEF == 2 && c == 4 && rd == 2) stg(rws, rs_off(0), pend[0]);
        if constexpr (rd == RF && c == 0) asm volatile("s_waitcnt vmcnt(%0)\n\ts_barrier" :: "n"(RF - 5) : "memory");
        SB();
      });
      own0 = c0; own1 = c1;
    });
    PIN(own0); PIN(own1);
  }
  static_assert(GB >= 86, "");
  VMW(0);
  {
    u32 old;
    asm volatile("s_mov_b64 exec, 1\n\tds_add_rtn_u32 %0, %1, %2\n\ts_waitcnt lgkmcnt(0)\n\ts_mov_b64 exec, -1" : "=&v"(old) : "v"(L + L_SF + 4), "v"(1u) : "memory");
    if (__builtin_amdgcn_readfirstlane(old) == 6) post(hme + 4 * 256 * 64);
  }
  const u32 one = g != q4 ? 0u : (p4 & 1) ? 0x3f800000u : 0x00003f80u;
  v4u sl0 = {p4 < 2 ? one : 0u, p4 < 2 ? 0u : one, 0u, 0u}, sl1 = {0u, 0u, sl0.x, sl0.y};
  PIN(sl0); PIN(sl1);
  u32 rs_r[3];
#pragma unroll
  for (int k = 0; k < 3; ++k) { rs_r[k] = OFF_RS + (((gm * 8 + mm) * 8 + th * 4 + ((js + 1 + k) & 3)) * 7 + w) * KB; PINS(rs_r[k]); }
  bar();
  {
    v4u pp[7];
#pragma unroll
    for (int k = 0; k < 3; ++k) pp[k] = ldg(rws, l16, rs_r[k]);
#pragma unroll
    for (int k = 0; k < 4; ++k) pp[3 + k] = ld16(A + (k * 7 + w) * KB);
    SB();
#pragma unroll
    for (int k = 6; k >= 3; --k) { own0 = mfma(sl0, pp[k], own0); own1 = mfma(sl1, pp[k], own1); }
    SB();
#pragma unroll
    for (int k = 0; k < 3; ++k) { own0 = mfma(sl0, pp[k], own0); own1 = mfma(sl1, pp[k], own1); }
#pragma unroll
    for (int r = 0; r < 4; ++r) { hold0[r] = fmaf(own0[r], fr0, hold0[r]); hold1[r] = fmaf(own1[r], fr1, hold1[r]); }
    *(GAS v4u*)(p_out + out_off) = to_chunk(pack8(hold0, hold1));
  }
  return;
  }
  {
    {
      f4 acc;
      asm volatile("s_waitcnt vmcnt(%0)" :: "n"(QPF) : "memory");
      RETRY_LOOP {
        v4u kp[8];
#pragma unroll
        for (int k = 0; k < 8; ++k) kp[k] = ld16(L + L_KP + k * KB + l16);
        SB();
        acc = z4;
        u32 m = 0;
#pragma unroll
        for (int k = 0; k < 8; ++k) { acc = mfma(sel, kp[k], acc); m = max4(m, kp[k]); }
        if (__all(m != 0xffffffffu)) break;
        RETRY_END(1);
        dma_ws<2>(rws, l16, OFF_KVP + (mi * 32 + yg) * 8 * KB, L + L_KP);
        VMW(0);
      }
      __builtin_amdgcn_raw_buffer_store_b64(v2u{nosent(pk2(acc[0], acc[1])), nosent(pk2(acc[2], acc[3]))}, rws, (int)kvt8, 0, SC1);
      bar();
      post_t(OFF_HINT + (0 * 256 + yg * 8 + mi) * 64);
      __builtin_amdgcn_s_sleep(DLYB);
      wait_until(t_kv + DTF);
      dma_ws<2>(rws, l16, OFF_KVT + mi * 8 * KB, L + L_KF);
      if (HM == 0) {
        const u32 hm = zg * 28 * KB + (g & 1) * KB + (g >> 1) * 256 + i16 * 16 + OFF_H;
#pragma unroll
        for (int c = 0; c < 7; ++c) stg(rws, hm + ((js * 7 + c) >> 1) * 2 * KB + ((js * 7 + c) & 1) * 512, SENT);
      }
      phi_q();
      f4 sm[4];
      if (HM == 0) VMW(7); else VMW(0);
      RETRY_LOOP {
        if (g_check<0, 2, -1, false>(L + L_KF + l16, sm)) break;
        RETRY_END(2);
        dma_ws<2>(rws, l16, OFF_KVT + mi * 8 * KB, L + L_KF);
        VMW(0);
      }
      bar();
      peek_issue(rws, h8);
      make_o();
      bar();
    }
    const u32 P = L + L_ACT + l16;
    f4 sm[4];
    u32 hN = OFF_HINT + (7 * 256 + (((js & 1) ? zg + 63 : zg + 1) & 63) * 4 + js) * 64;
#define GATHER(NK, ST, SRC, ID) \
      RETRY_LOOP { \
        dma_ws<NK / 4>(rws, l16, SRC, L + L_ACT); \
        if (ST) peek_issue(rws, hN); \
        if (sp == 0) bar(); \
        if (ST) { sm[0] = z4; sm[1] = z4; sm[2] = z4; sm[3] = z4; } \
        if (g_check<0, 3, NK - 12 + ST, ST>(P, sm)) break; \
        RETRY_END(ID); \
        VMW(0); \
      } \
      bar(); \
      RETRY_LOOP { \
        f4 sn[4] = {sm[0], sm[1], sm[2], sm[3]}; \
        const bool ok = g_check<3, NK / 4, ST, ST>(P, sn); \
        if (ok) { sm[0] = sn[0]; sm[1] = sn[1]; sm[2] = sn[2]; sm[3] = sn[3]; break; } \
        RETRY_END(ID); \
        dma_ws<NK / 4 - 3>(rws, l16, (SRC) + 12 * KB, L + L_ACT + 12 * KB); \
        VMW(0); \
      } \
      bar()
    {
      pace<1, DT0>(rws, h8, tag, mi);
    }
    { GATHER(32, false, OFF_O + zg * 32 * KB, 3); }
    u32 hA = OFF_HINT + (1 * 256 + zg * 4 + (lane & 3)) * 64;
    u32 hB = OFF_HINT + (2 * 256 + ((lane & 4) ? ((lane & 1) ? zg + 1 : zg + 63) & 63 : zg) * 4 + (lane & 3)) * 64;
    u32 st_w = L + (g == q4 ? L_ST : L_QB) + i16 * 8;
    PIN(hA); PIN(hB); PIN(st_w);
    *(LAS u32*)(uintptr_t)(L + L_SF) = 0;
    PIN(hN);
    peek_issue(rws, hA);
    bar();
    pace<0, DT1>(rws, hA, tag, js);
    {
      GATHER(28, true, OFF_H + zg * 28 * KB, 4);
      const float m0 = sm[0][0] * (1.0f / D_), m1 = sm[1][0] * (1.0f / D_);
      const float v0 = fmx(fmaf(-m0, m0, diag(sm[2], p4) * (1.0f / D_)), 0.f), v1 = fmx(fmaf(-m1, m1, diag(sm[3], p4) * (1.0f / D_)), 0.f);
      const float r0 = __builtin_amdgcn_rsqf(v0 + 1e-5f), r1 = __builtin_amdgcn_rsqf(v1 + 1e-5f);
      const float t0 = r0 * __builtin_amdgcn_rsqf(v0 * r0 * r0 + 1e-5f), t1 = r1 * __builtin_amdgcn_rsqf(v1 * r1 * r1 + 1e-5f);
      { st8(st_w, f2u(-m0 * t0), f2u(t0)); st8(st_w + 128, f2u(-m1 * t1), f2u(t1)); st8(st_w + 256, f2u(-m0 * r0), f2u(r0)); st8(st_w + 384, f2u(-m1 * r1), f2u(r1)); }
      peek_take<0>(rws, hN, tag);
      asm volatile("s_waitcnt lgkmcnt(0)" ::: "memory");
      *(volatile LAS u32*)(uintptr_t)(L + L_SF) = 1;
    }
    peek_issue(rws, hB);
    bar();
    pace<0, DT2>(rws, hB, tag, js);
    { GATHER(28, false, OFF_HS + zg * 28 * KB, 5); }
    u32 h83 = OFF_HINT + (3 * 256 + ((lane & 4) ? zo : zg) * 4 + (lane & 3)) * 64;
    PIN(h83);
    peek_issue(rws, h83);
    bar();
    pace<0, DT3>(rws, h83, tag, js);
    {
      const u32 s5_w = L + (g == q4 ? L_S5 : L_QB + 4 * KB) + i16 * 16;
      auto stats = [&](u32 dst) AI {
        const float m0 = sm[0][0] * (1.0f / D_), m1 = sm[1][0] * (1.0f / D_);
        const float r0 = __builtin_amdgcn_rsqf(fmx(fmaf(-m0, m0, diag(sm[2], p4) * (1.0f / D_)), 0.f) + 1e-5f);
        const float r1 = __builtin_amdgcn_rsqf(fmx(fmaf(-m1, m1, diag(sm[3], p4) * (1.0f / D_)), 0.f) + 1e-5f);
        {
          st16(dst, v4u{f2u(-m0), f2u(GC0 * r0), f2u(GC1 * r0 * r0 * r0), f2u(r0)});
          st16(dst + 256, v4u{f2u(-m1), f2u(GC0 * r1), f2u(GC1 * r1 * r1 * r1), f2u(r1)});
        }
      };
      const u32 xa_ = OFF_X1 + zo * 28 * KB, xb_ = OFF_X1 + zg * 28 * KB;
      RETRY_LOOP {
        dma_ws<3>(rws, l16, xa_, L + L_ACT);
        dma_ws<3>(rws, l16, xb_, L + L_ACT + 28 * KB);
        dma_ws<1>(rws, l16, xa_ + 12 * KB, L + L_ACT + 12 * KB);
        if (sp == 0) bar();
        if (g_check<0, 3, 16, false>(P, sm)) break;
        RETRY_END(6);
        VMW(0);
      }
      bar();
      RETRY_LOOP {
        if (g_check<7, 10, 4, false>(P, sm)) break;
        RETRY_END(6);
        dma_ws<3>(rws, l16, xb_, L + L_ACT + 28 * KB);
        VMW(0);
      }
      bar();
      sm[0] = z4; sm[1] = z4; sm[2] = z4; sm[3] = z4;
      RETRY_LOOP {
        VMW(0);
        if (g_check<3, 4, -1, false>(P, sm)) break;
        RETRY_END(7);
        dma_ws<1>(rws, l16, xa_ + 12 * KB, L + L_ACT + 12 * KB);
      }
      g_check<0, 4, -1, true>(P, sm);
      bar();
      if (HP) __builtin_amdgcn_s_setprio(0);
      g_check<4, 7, -1, true>(P, sm);
      stats(s5_w);
      bar();
      sm[0] = z4; sm[1] = z4; sm[2] = z4; sm[3] = z4;
      g_check<7, 14, -1, true>(P, sm);
      stats(s5_w + 512);
    }
    *(volatile LAS u32*)(uintptr_t)(L + L_SF + 4) = 0;
    bar();
    bar();
    bar();
    post(hme + 6 * 256 * 64);
    u32 hO = OFF_HINT + (6 * 256 + zo * 4 + (lane & 3)) * 64, hS = OFF_HINT + (4 * 256 + zg * 4 + (lane & 3)) * 64;
    PIN(hO); PIN(hS);
    __builtin_amdgcn_s_sleep(SLA);
    poll(rws, hO, tag);
    dma_ws<7>(rws, l16, OFF_RS + ((gm * 8 + mm) * 8 + (th ^ 1) * 4) * 7 * KB, L + L_ACT);
    for (u32 sp = 0; sp < (1u << 20) && __builtin_amdgcn_readfirstlane(*(volatile LAS u32*)(uintptr_t)(L + L_SF + 4)) != 7; ++sp) { }
    __builtin_amdgcn_s_sleep(SLB);
    poll(rws, hS, tag);
    bar();
    if (b == 0) __builtin_amdgcn_raw_buffer_store_b32(epoch + 1, rws, (int)vz, (int)OFF_E, SC1);
  }
}


extern "C" void kernel_launch(void* const* d_in, const int* in_sizes, int n_in, void* d_out, int out_size, void* d_ws, size_t ws_size,
                              hipStream_t stream) {
  fused_kernel<<<256, 512, 0, stream>>>((const u16*)d_in[0], (const u16*)d_in[1], (const u16*)d_in[2], (const u16*)d_in[3],
                                        (const u16*)d_in[5], (const u16*)d_in[6], (const u16*)d_in[7], (const u16*)d_in[8],
                                        (u16*)d_out, (unsigned char*)d_ws);
}
```
